```python
import torch

def _softmax(x, axis=-1):
    return torch.softmax(x, dim=axis)
def _gelu(x):
    return torch.nn.functional.gelu(x, approximate="tanh")
def _silu(x):
    return torch.nn.functional.silu(x)
def _sigmoid(x):
    return torch.sigmoid(x)
def _layernorm(x, axis=-1, eps=1e-5):
    m = x.mean(dim=axis, keepdim=True)
    v = x.var(dim=axis, keepdim=True, unbiased=False)
    return (x - m) / torch.sqrt(v + eps)
def _scatter_add(tgt, idx, src, axis=0):
    return tgt.clone().index_add_(axis, idx, src)
def _segment_sum(src, seg, n):
    out = torch.zeros((n,) + src.shape[1:], dtype=src.dtype)
    return out.index_add_(0, seg, src)
def _pad(x, pw):
    flat = tuple(v for pair in reversed(pw) for v in pair)
    return torch.nn.functional.pad(x, flat)

kernel_name = 'interleave_layers__linear_attn_block__glu_gated_bilinear__prompt'

def setup_inputs():

    _w = (lambda a: (lambda t: t.to(torch.bfloat16) if t.is_floating_point() else t)(torch.as_tensor(a)))
    def _A_setup():
        B, S, D = 1, 2048, 1024
        return {
            "x": _w((lambda *s: torch.randn(*s, dtype=torch.bfloat16) * 0.05)(B, S, D)),
            "wqkv": _w((lambda *s: torch.randn(*s, dtype=torch.bfloat16) * 0.05)(D, 3 * D)),
            "wo": _w((lambda *s: torch.randn(*s, dtype=torch.bfloat16) * 0.05)(D, D)),
        }
    _A_in = _A_setup()

    def _B_setup():
        B, S, D, F = 1, 2048, 1024, 3584
        return {
            "x": _w((lambda *s: torch.randn(*s, dtype=torch.bfloat16) * 0.05)(B, S, D)),
            "w_gate": _w((lambda *s: torch.randn(*s, dtype=torch.bfloat16) * 0.8)(D, F) * (D ** -0.5)),
            "w_up": _w((lambda *s: torch.randn(*s, dtype=torch.bfloat16) * 0.8)(D, F) * (D ** -0.5)),
            "w_down": _w((lambda *s: torch.randn(*s, dtype=torch.bfloat16) * 0.8)(F, D) * (F ** -0.5)),
        }
    _B_in = _B_setup()

    return {
        "A_x": _A_in["x"],
        "A_wqkv": _A_in["wqkv"],
        "A_wo": _A_in["wo"],
        "B_x": _B_in["x"],
        "B_w_gate": _B_in["w_gate"],
        "B_w_up": _B_in["w_up"],
        "B_w_down": _B_in["w_down"],
    }

def reference(A_x, A_wqkv, A_wo, B_x, B_w_gate, B_w_up, B_w_down):
    def _A_ref(x, wqkv, wo):
        B, S, D = x.shape
        H = 32; Dh = D // H
        qkv = (lambda x, *d: x.permute(*d))((x @ wqkv).reshape(B, S, 3, H, Dh), 2, 0, 3, 1, 4)
        q, k, v = qkv[0], qkv[1], qkv[2]
        phi = lambda t: torch.where(t > 0, t + 1, torch.exp(t)) + 1e-6
        pq, pk = phi(q), phi(k)
        kv = torch.einsum("bhsd,bhse->bhde", pk, v)
        z = torch.einsum("bhsd->bhd", pk)
        num = torch.einsum("bhqd,bhde->bhqe", pq, kv)
        den = torch.einsum("bhqd,bhd->bhq", pq, z)[..., None] + 1e-6
        o = num / den
        return (lambda x, *d: x.permute(*d))(o, 0, 2, 1, 3).reshape(B, S, D) @ wo

    def _B_ref(x, w_gate, w_up, w_down):
        g = torch.einsum("bsd,df->bsf", x, w_gate)
        u = torch.einsum("bsd,df->bsf", x, w_up)
        h = g * u
        return torch.einsum("bsf,fd->bsd", h, w_down)

    x = A_x
    x = x + _A_ref(_layernorm(x, axis=-1), A_wqkv, A_wo)
    x = x + _B_ref(_layernorm(x, axis=-1), B_w_gate, B_w_up, B_w_down)
    x = x + _A_ref(_layernorm(x, axis=-1), A_wqkv, A_wo)
    x = x + _B_ref(_layernorm(x, axis=-1), B_w_gate, B_w_up, B_w_down)
    return x
```

```cpp
#include <hip/hip_runtime.h>
#include <hip/hip_bf16.h>

namespace {
typedef unsigned short u16;
typedef short bf16x8 __attribute__((ext_vector_type(8)));
typedef float f32x2 __attribute__((ext_vector_type(2)));
typedef float f32x4 __attribute__((ext_vector_type(4)));
typedef float f32x16 __attribute__((ext_vector_type(16)));
typedef float f32x32 __attribute__((ext_vector_type(32)));
typedef unsigned u32x2 __attribute__((ext_vector_type(2)));
typedef unsigned u32x4 __attribute__((ext_vector_type(4)));
typedef unsigned u32x8 __attribute__((ext_vector_type(8)));
typedef unsigned u32x16 __attribute__((ext_vector_type(16)));
typedef __bf16 bf16x2 __attribute__((ext_vector_type(2)));
typedef __amdgpu_buffer_rsrc_t rsrc_t;
#define LAS __attribute__((address_space(3)))
#define DI __device__ __forceinline__

constexpr int S = 2048, D = 1024, F = 3584;
constexpr int KVREC = 1056;

constexpr unsigned XB = S * D * 2, SB = 16 * S * 8, HB = S * F * 2;
constexpr unsigned WS_XT = 0;
constexpr unsigned WS_PQ = WS_XT + 4 * XB;
constexpr unsigned WS_HT = WS_PQ + 2 * XB;
constexpr unsigned WS_ST = WS_HT + 2 * HB;
constexpr unsigned WS_CS = WS_ST + 4 * SB;
constexpr unsigned WS_R2 = WS_CS + (16 * 3 * D + 2 * F) * 4;
constexpr unsigned WS_KB = WS_R2 + (4 * S + 65536) * 4;
constexpr unsigned WS_ZB = WS_KB + 2 * 65536;
constexpr unsigned WS_CT = WS_ZB + 2 * 2048;
constexpr unsigned WS_KP = WS_CT + 1088 * 128;
constexpr unsigned WS_XC = WS_KP + 512 * 8448;
constexpr unsigned WS_WQ = WS_XC + 512 * 16384;
constexpr unsigned WS_END = WS_WQ + 3 * D * D * 2;

#define FSLEEP 2
#define PSLEEP 1
#define LDNT(p) __builtin_nontemporal_load(p)
DI rsrc_t make_rsrc(void* base, unsigned bytes) { return __builtin_amdgcn_make_buffer_rsrc(base, 0, bytes, 0x00020000); }
DI void st16(rsrc_t rs, unsigned off, u32x4 v) { __builtin_amdgcn_raw_buffer_store_b128(v, rs, off, 0, 16); }
DI void st8(rsrc_t rs, unsigned off, u32x2 v) { __builtin_amdgcn_raw_buffer_store_b64(v, rs, off, 0, 16); }
DI void st4(rsrc_t rs, unsigned off, float v) { __builtin_amdgcn_raw_buffer_store_b32(__float_as_uint(v), rs, off, 0, 16); }
DI void drain() { asm volatile("s_waitcnt vmcnt(0)" ::: "memory"); }
DI unsigned pk_bf16(float lo, float hi) { f32x2 v = {lo, hi}; return __builtin_bit_cast(unsigned, __builtin_convertvector(v, bf16x2)); }
DI float bf_lo(unsigned w) { return __uint_as_float(w << 16); }
DI float bf_hi(unsigned w) { return __uint_as_float(w & 0xffff0000u); }
DI float sum_x16(float v) { auto r = __builtin_amdgcn_permlane16_swap(__float_as_uint(v), __float_as_uint(v), false, false); return __uint_as_float(r[0]) + __uint_as_float(r[1]); }
DI float sum_x32(float v) { auto r = __builtin_amdgcn_permlane32_swap(__float_as_uint(v), __float_as_uint(v), false, false); return __uint_as_float(r[0]) + __uint_as_float(r[1]); }
DI float sum_rows(float v) { v += __uint_as_float(__builtin_amdgcn_update_dpp(0u, __float_as_uint(v), 0x128, 0xf, 0xf, false)); return sum_x32(sum_x16(v)); }
DI float phi(float t) { return fmaxf(t, 0.f) + __builtin_amdgcn_fmed3f(__builtin_amdgcn_exp2f(t * 1.44269504089f), 0.f, 1.f) + 1e-6f; }
constexpr int FL_Q = 0, FL_X = 64, FL_A = 128, FL_H = 160, FL_G = 288, FL_N = 544;
DI unsigned flag(int layer, int line, int word = 0) { return WS_CT + (layer * FL_N + line) * 128 + word * 4; }
DI void set_word(char* ws, unsigned f) {
  asm volatile("global_store_dword %0, %1, %2 sc1" :: "v"(f), "v"(1u), "s"(ws) : "memory"); }
DI void await(int w, char* ws, unsigned f) {
  if (w == 0) {
#pragma unroll 1
    for (int spin = 0; spin < (1 << 18); ++spin) {
      unsigned v;
      asm volatile("global_load_dword %0, %1, %2 sc1\n\ts_waitcnt vmcnt(0)" : "=v"(v) : "v"(f), "s"(ws) : "memory");
      if (__all(v == 1u)) break;
      __builtin_amdgcn_s_sleep(PSLEEP);
    }
  }
  asm volatile("s_barrier" ::: "memory");
}
constexpr unsigned EMPTY = 0xffffffffu;
DI bool filled(u32x4 v) { return v[0] != EMPTY && v[1] != EMPTY && v[2] != EMPTY && v[3] != EMPTY; }
template <int N, class OFF>
DI void fetch(rsrc_t rs, u32x4 (&v)[N], OFF off) {
#pragma unroll 1
  for (int spin = 0; spin < (1 << 16); ++spin) {
    bool ok = true;
#pragma unroll
    for (int i = 0; i < N; ++i) v[i] = __builtin_amdgcn_raw_buffer_load_b128(rs, off(i), 0, 16);
#pragma unroll
    for (int i = 0; i < N; ++i) ok &= filled(v[i]);
    if (__all(ok)) break;
    asm volatile("s_sleep %0" :: "i"(FSLEEP) : "memory");
  }
}
DI void wg_barrier() { asm volatile("s_waitcnt lgkmcnt(0)\n\ts_barrier" ::: "memory"); }
DI f32x2 stats64(const unsigned (&w)[8]) {
  float s = 0.f;
#pragma unroll
  for (int i = 0; i < 8; ++i) s += bf_lo(w[i]) + bf_hi(w[i]);
  const float mean = sum_x32(sum_x16(s)) * (1.f / 64.f);
  float q = 0.f;
#pragma unroll
  for (int i = 0; i < 8; ++i) { const float a = bf_lo(w[i]) - mean, b = bf_hi(w[i]) - mean; q += a * a + b * b; }
  return (f32x2){mean, sum_x32(sum_x16(q))};
}
template <int NM, class ACC, class RES>
DI void epi_res(ACC acc, RES res, char* ws, u16* out, LAS char* tile, int xi, int rt0, int ct, int lane) {
  const int g = lane >> 4, r = lane & 15;
  unsigned o[NM][8];
#pragma unroll
  for (int m = 0; m < NM; ++m)
#pragma unroll
    for (int kf = 0; kf < 2; ++kf)
#pragma unroll
      for (int i = 0; i < 4; ++i) {
        const int n = 2 * kf + (i >> 1), j = 2 * (i & 1);
        const unsigned x = __float_as_uint(res(kf, m, i));
        o[m][4 * kf + i] = pk_bf16(bf_lo(x) + acc(m, n, j), bf_hi(x) + acc(m, n, j + 1));
      }
  if (out) {
    const rsrc_t ro = make_rsrc(out, S * D * 2);
#pragma unroll
    for (int m = 0; m < NM; ++m)
#pragma unroll
      for (int i = 0; i < 4; ++i) *(LAS u32x2*)(tile + (16 * m + r) * 144 + i * 32 + g * 8) = (u32x2){o[m][2 * i], o[m][2 * i + 1]};
#pragma unroll
    for (int i = 0; i < 2 * NM; ++i)
      st16(ro, ((16 * rt0 + 8 * i + (lane >> 3)) * D + 64 * ct) * 2 + (lane & 7) * 16, *(const LAS u32x4*)(tile + (8 * i + (lane >> 3)) * 144 + (lane & 7) * 16));
  } else {
    const rsrc_t rw = make_rsrc(ws, WS_END);
#pragma unroll
    for (int m = 0; m < NM; ++m) {
      const unsigned xo = WS_XT + xi * XB + (((rt0 >> 3) * 32 + 2 * ct) * 8 + (rt0 & 7) + m) * 1024 + lane * 16;
      st16(rw, xo, (u32x4){o[m][0], o[m][1], o[m][2], o[m][3]});
      st16(rw, xo + 8192, (u32x4){o[m][4], o[m][5], o[m][6], o[m][7]});
    }
    const f32x2 s0 = stats64(o[0]), s1 = NM > 1 ? stats64(o[NM - 1]) : s0;
    const f32x2 s = (g & 1) ? s1 : s0;
    if (g < NM) st8(rw, WS_ST + xi * SB + (ct * S + 16 * rt0 + lane) * 8, __builtin_bit_cast(u32x2, s));
  }
}
}

#define TOUCH_CODE(end, line, scratch) do { unsigned t_; \
    asm volatile("s_getpc_b64 s[70:71]\n\ts_add_u32 s70, s70, lin_prep@rel32@lo+4\n\ts_addc_u32 s71, s71, lin_prep@rel32@hi+12\n\tv_lshlrev_b32 %0, 7, %1\n\t" \
                 "v_min_u32 %0, " end "-lin_prep-4, %0\n\ts_mov_b32 m0, %2\n\ts_nop 0\n\tglobal_load_lds_dword %0, s[70:71]" \
                 : "=&v"(t_) : "v"(line), "s"((unsigned)(size_t)(scratch)) : "s70", "s71", "scc", "memory"); } while (0)

extern "C" __global__ __launch_bounds__(256) void lin_prep(const u16* __restrict__ x, const u16* __restrict__ wqkv, const u16* __restrict__ wo, const u16* __restrict__ wg,
                                                           const u16* __restrict__ wu, const u16* __restrict__ wd, char* __restrict__ ws, const u16* __restrict__ out) {
  __shared__ float red[64];
  __shared__ __attribute__((aligned(16))) char xs[16 * 2064];
  const int tid = threadIdx.x, w = tid >> 6, lane = tid & 63, g = lane >> 4, r = lane & 15, b = blockIdx.x;
  const rsrc_t rw = make_rsrc(ws, WS_END);
  auto empty_rec = [&](int rec) {
#pragma unroll
    for (int i = 0; i < 5; ++i) if (i < 4 || tid < 32) st16(rw, WS_KP + rec * 16896 + (i * 256 + tid) * 16, (u32x4){EMPTY, EMPTY, EMPTY, EMPTY});
  };
  if (b >= 128 && b < 160) {
    empty_rec(b - 128); empty_rec(b + 96);
    st16(rw, WS_KB + (b - 128) * 4224 + tid * 16, (u32x4){EMPTY, EMPTY, EMPTY, EMPTY});
    if (tid < 8) st16(rw, WS_KB + (b - 128) * 4224 + 4096 + tid * 16, (u32x4){EMPTY, EMPTY, EMPTY, EMPTY});
    st16(rw, WS_CT + (b - 128) * 4352 + tid * 16, (u32x4){0u, 0u, 0u, 0u});
    if (tid < 16) st16(rw, WS_CT + (b - 128) * 4352 + 4096 + tid * 16, (u32x4){0u, 0u, 0u, 0u});
    TOUCH_CODE("lin_end", tid + ((b >> 3) & 3) * 256, (LAS float*)red);
    {
      const int k = __builtin_amdgcn_readfirstlane(w + 4 * ((b >> 3) & 1));
      const char* base = k == 0 || k == 4 ? (const char*)ws : k == 1 ? (const char*)wg : k == 2 ? (const char*)wu : k == 3 ? (const char*)wo : k == 5 ? (const char*)wd : (const char*)out;
      const unsigned lim = k == 0 || k == 4 ? WS_END : k == 3 ? D * D * 2 : k > 5 ? S * D * 2 : F * D * 2;
      __builtin_amdgcn_global_load_lds((const unsigned*)(base + min((unsigned)lane << ((k & 3) == 0 ? 21 : 20), lim - 4)), (LAS unsigned*)&red[0], 4, 0, 0);
    }
#pragma unroll
    for (int i = 0; i < 2; ++i) __builtin_amdgcn_global_load_lds((const unsigned*)((const char*)wo + ((b - 128) * 512 + i * 256 + tid) * 128), (LAS unsigned*)&red[0], 4, 0, 0);
    drain();
    return;
  }
  u32x4 v[8];
  const int c = 4 * ((b - 160) >> 4) + w, kc = (b - 160) & 15;
  if (b < 128) {
#pragma unroll
    for (int i = 0; i < 8; ++i) v[i] = LDNT((const u32x4*)(x + (size_t)b * 16 * D) + i * 256 + tid);
  } else {
    const u16* p = wqkv + (size_t)(kc * 64 + (lane >> 3)) * 3 * D + c * 64 + (lane & 7) * 8;
#pragma unroll
    for (int i = 0; i < 8; ++i) v[i] = LDNT((const u32x4*)(p + (size_t)i * 8 * 3 * D));
    empty_rec(b - 128);
  }
  TOUCH_CODE("lin_main", lane, (LAS float*)red);
  if (b < 128) {
#pragma unroll
    for (int i = 0; i < 8; ++i) *(LAS u32x4*)((LAS char*)xs + (2 * i + (tid >> 7)) * 2064 + (tid & 127) * 16) = v[i];
    wg_barrier();
#pragma unroll
    for (int i = 0; i < 8; ++i) {
      const LAS char* sp = (const LAS char*)xs + r * 2064 + (8 * w + i) * 64 + g * 8;
      const u32x2 lo = *(const LAS u32x2*)sp, hi = *(const LAS u32x2*)(sp + 32);
      v[i] = (u32x4){lo[0], lo[1], hi[0], hi[1]};
    }
#pragma unroll
    for (int i = 0; i < 8; ++i) st16(rw, WS_XT + ((b >> 3) * 32 + 8 * w + i) * 8192 + (b & 7) * 1024 + lane * 16, v[i]);
    f32x2 s = {0.f, 0.f};
#pragma unroll
    for (int t = 0; t < 4; ++t) {
      const unsigned ww[8] = {v[2 * t][0], v[2 * t][1], v[2 * t][2], v[2 * t][3], v[2 * t + 1][0], v[2 * t + 1][1], v[2 * t + 1][2], v[2 * t + 1][3]};
      const f32x2 st = stats64(ww);
      s = g == t ? st : s;
    }
    st8(rw, WS_ST + ((4 * w + g) * S + b * 16 + r) * 8, __builtin_bit_cast(u32x2, s));
  } else {
    const unsigned to = WS_WQ + (((c & 15) * 32 + kc * 2) * 12 + (c >> 4)) * 1024 + (((((lane & 7) >> 1) ^ g) << 1) | (lane & 1)) * 16 + (lane >> 3) * 128;
#pragma unroll
    for (int i = 0; i < 8; ++i) st16(rw, to + i * 3072, v[i]);
    float a[8] = {0.f, 0.f, 0.f, 0.f, 0.f, 0.f, 0.f, 0.f};
#pragma unroll
    for (int i = 0; i < 8; ++i)
#pragma unroll
      for (int j = 0; j < 4; ++j) { a[2 * j] += bf_lo(v[i][j]); a[2 * j + 1] += bf_hi(v[i][j]); }
#pragma unroll
    for (int j = 0; j < 8; ++j) a[j] = sum_rows(a[j]);
    if (lane < 16)
      st16(rw, WS_CS + (kc * 3 * D + c * 64) * 4 + (lane & 7) * 32 + (lane >> 3) * 16,
           lane < 8 ? __builtin_bit_cast(u32x4, (f32x4){a[0], a[1], a[2], a[3]}) : __builtin_bit_cast(u32x4, (f32x4){a[4], a[5], a[6], a[7]}));
  }
  drain();
}

#define DOWN_PM (4 * (xcd >> 1) + (bi & 3))
#define DOWN_PN (bi >> 2)
#define WD_OFF (layer ? 0 : min(bx * 256 + tid, 57343u) * 128)
#define LANE_IDS int lane; asm volatile("v_mbcnt_lo_u32_b32 %0, -1, 0\n\tv_mbcnt_hi_u32_b32 %0, -1, %0" : "=v"(lane)); \
  const int tid = w * 64 + lane, g = lane >> 4, r = lane & 15, q = r >> 2, p = lane & 3; (void)tid; (void)g; (void)q; (void)p;
namespace {
constexpr int L_KV = 124928, L_Z = L_KV + 32768, L_TAB = L_Z + 2048, L_SCR = L_TAB + 3072, L_END = L_SCR + 256;
constexpr int G3_B = 65536;
struct Args { const u16 *wo, *wg, *wu, *wd; char* ws; u16* out; int w; };

constexpr int NB_1 = 18, NB_2 = 20, NB_3 = 16, NB_4 = 16;
DI void stage_qkv(const Args& A, LAS char* lds) {
  int w = A.w; unsigned bx = blockIdx.x; char* ws = A.ws;
  asm volatile("" : "+s"(w), "+s"(bx), "+s"(ws));
  LANE_IDS
  const unsigned lb = (unsigned)(size_t)lds;
  const int xcd = bx & 7, bi = bx >> 3, pn = 4 * (xcd >> 1) + (bi >> 3);
  const char* pB = ws + WS_WQ + pn * 32 * 12288;
  asm volatile(
    "s_nop 4\n" \
    "s_add_u32 m0, s45, 0\n" \
    "s_nop 0\n" \
    "global_load_lds_dwordx4 v33, s[42:43] offset:0\n" \
    "global_load_lds_dwordx4 v33, s[42:43] offset:1024\n" \
    "global_load_lds_dwordx4 v33, s[42:43] offset:2048\n" \
    "s_add_u32 s42, s42, 12288\n" \
    "s_addc_u32 s43, s43, 0\n" \
    "s_add_u32 m0, s45, 20480\n" \
    "s_nop 0\n" \
    "global_load_lds_dwordx4 v33, s[42:43] offset:0\n" \
    "global_load_lds_dwordx4 v33, s[42:43] offset:1024\n" \
    "global_load_lds_dwordx4 v33, s[42:43] offset:2048\n" \
    "s_add_u32 s42, s42, 12288\n" \
    "s_addc_u32 s43, s43, 0\n" \
    "s_add_u32 m0, s45, 40960\n" \
    "s_nop 0\n" \
    "global_load_lds_dwordx4 v33, s[42:43] offset:0\n" \
    "global_load_lds_dwordx4 v33, s[42:43] offset:1024\n" \
    "global_load_lds_dwordx4 v33, s[42:43] offset:2048\n" \
    "s_add_u32 s42, s42, 12288\n" \
    "s_addc_u32 s43, s43, 0\n" \
    "s_add_u32 m0, s45, 61440\n" \
    "s_nop 0\n" \
    "global_load_lds_dwordx4 v33, s[42:43] offset:0\n" \
    "global_load_lds_dwordx4 v33, s[42:43] offset:1024\n" \
    "global_load_lds_dwordx4 v33, s[42:43] offset:2048\n" \
    "s_add_u32 s42, s42, 12288\n" \
    "s_addc_u32 s43, s43, 0\n" \
    "s_add_u32 m0, s45, 81920\n" \
    "s_nop 0\n" \
    "global_load_lds_dwordx4 v33, s[42:43] offset:0\n" \
    "global_load_lds_dwordx4 v33, s[42:43] offset:1024\n" \
    "global_load_lds_dwordx4 v33, s[42:43] offset:2048\n" \
    "s_add_u32 s42, s42, 12288\n" \
    "s_addc_u32 s43, s43, 0\n" \
    "s_add_u32 m0, s45, 102400\n" \
    "s_nop 0\n" \
    "global_load_lds_dwordx4 v33, s[42:43] offset:0\n" \
    "global_load_lds_dwordx4 v33, s[42:43] offset:1024\n" \
    "global_load_lds_dwordx4 v33, s[42:43] offset:2048\n" \
    "s_add_u32 s42, s42, 12288\n" \
    "s_addc_u32 s43, s43, 0\n"
    : "+{s[42:43]}"(pB) : "{v33}"(lane * 16 + w * 3072), "{s45}"(lb + 8192 + w * 3072) : "memory", "scc");
}
DI unsigned attn_voffB(int lane, int wl) { return (lane >> 3) * 2048 + wl * 128 + (((((lane & 7) >> 1) ^ (lane >> 4)) << 1) | (lane & 1)) * 16; }
DI void stage_attn(const Args& A, LAS char* lds) {
  int w = A.w; unsigned bx = blockIdx.x; char* ws = A.ws;
  asm volatile("" : "+s"(w), "+s"(bx), "+s"(ws));
  LANE_IDS
  const unsigned lb = (unsigned)(size_t)lds;
  const int xcd = bx & 7, bi = bx >> 3, pn = 4 * (xcd >> 2) + (bi >> 3), gs = w >> 1, wl = w & 1;
  const char* pB = (const char*)A.wo + gs * 65536 + pn * 256;
  const unsigned voffB = attn_voffB(lane, wl);
  asm volatile(
    "s_nop 4\n" \
    "s_add_u32 m0, s45, 0\n" \
    "s_nop 0\n" \
    "global_load_lds_dwordx4 v33, s[42:43] offset:0\n" \
    "global_load_lds_dwordx4 v27, s[42:43] offset:1024\n" \
    "global_load_lds_dwordx4 v29, s[42:43] offset:2048\n" \
    "global_load_lds_dwordx4 v3, s[42:43] offset:3072\n" \
    "s_add_u32 s42, s42, 131072\n" \
    "s_addc_u32 s43, s43, 0\n" \
    "s_add_u32 m0, s45, 12288\n" \
    "s_nop 0\n" \
    "global_load_lds_dwordx4 v33, s[42:43] offset:0\n" \
    "global_load_lds_dwordx4 v27, s[42:43] offset:1024\n" \
    "global_load_lds_dwordx4 v29, s[42:43] offset:2048\n" \
    "global_load_lds_dwordx4 v3, s[42:43] offset:3072\n" \
    "s_add_u32 s42, s42, 131072\n" \
    "s_addc_u32 s43, s43, 0\n" \
    "s_add_u32 m0, s45, 24576\n" \
    "s_nop 0\n" \
    "global_load_lds_dwordx4 v33, s[42:43] offset:0\n" \
    "global_load_lds_dwordx4 v27, s[42:43] offset:1024\n" \
    "global_load_lds_dwordx4 v29, s[42:43] offset:2048\n" \
    "global_load_lds_dwordx4 v3, s[42:43] offset:3072\n" \
    "s_add_u32 s42, s42, 131072\n" \
    "s_addc_u32 s43, s43, 0\n" \
    "s_add_u32 m0, s45, 36864\n" \
    "s_nop 0\n" \
    "global_load_lds_dwordx4 v33, s[42:43] offset:0\n" \
    "global_load_lds_dwordx4 v27, s[42:43] offset:1024\n" \
    "global_load_lds_dwordx4 v29, s[42:43] offset:2048\n" \
    "global_load_lds_dwordx4 v3, s[42:43] offset:3072\n" \
    "s_add_u32 s42, s42, 131072\n" \
    "s_addc_u32 s43, s43, 0\n" \
    "s_add_u32 m0, s45, 49152\n" \
    "s_nop 0\n" \
    "global_load_lds_dwordx4 v33, s[42:43] offset:0\n" \
    "global_load_lds_dwordx4 v27, s[42:43] offset:1024\n" \
    "global_load_lds_dwordx4 v29, s[42:43] offset:2048\n" \
    "global_load_lds_dwordx4 v3, s[42:43] offset:3072\n" \
    "s_add_u32 s42, s42, 131072\n" \
    "s_addc_u32 s43, s43, 0\n"
    : "+{s[42:43]}"(pB) : "{v33}"(voffB), "{v27}"(voffB + 15360), "{v29}"(voffB + 2 * 15360), "{v3}"(voffB + 3 * 15360), "{s45}"(lb + gs * 61440 + 4096 + wl * 4096) : "memory", "scc");
}
DI unsigned gu_voffB(int lane, int wn, int j) { const unsigned qq = 64 * (3 * wn + j) + lane, row = qq / 14, cc = qq - row * 14; return row * 7168 + cc * 16 - 1024 * j; }
DI void stage_gu(const Args& A, LAS char* lds) {
  int w = A.w; unsigned bx = blockIdx.x; char* ws = A.ws;
  asm volatile("" : "+s"(w), "+s"(bx), "+s"(ws));
  LANE_IDS
  const unsigned lb = (unsigned)(size_t)lds;
  const int xcd = bx & 7, bi = bx >> 3, pn = 8 * (xcd >> 1) + (bi >> 2), wm = w >> 1, wn = w & 1;
  const char* pB = (const char*)(wm ? A.wu : A.wg) + pn * 224;
  asm volatile(
    "s_nop 4\n" \
    "s_add_u32 m0, s45, 0\n" \
    "s_nop 0\n" \
    "global_load_lds_dwordx4 v3, s[42:43] offset:0\n" \
    "global_load_lds_dwordx4 v4, s[42:43] offset:1024\n" \
    "global_load_lds_dwordx4 v5, s[42:43] offset:2048\n" \
    "global_load_lds_dwordx4 v6, s[42:43] offset:3072\n" \
    "s_add_u32 s42, s42, 229376\n" \
    "s_addc_u32 s43, s43, 0\n" \
    "s_add_u32 m0, s45, 14464\n" \
    "s_nop 0\n" \
    "global_load_lds_dwordx4 v3, s[42:43] offset:0\n" \
    "global_load_lds_dwordx4 v4, s[42:43] offset:1024\n" \
    "global_load_lds_dwordx4 v5, s[42:43] offset:2048\n" \
    "global_load_lds_dwordx4 v6, s[42:43] offset:3072\n" \
    "s_add_u32 s42, s42, 229376\n" \
    "s_addc_u32 s43, s43, 0\n" \
    "s_add_u32 m0, s45, 28928\n" \
    "s_nop 0\n" \
    "global_load_lds_dwordx4 v3, s[42:43] offset:0\n" \
    "global_load_lds_dwordx4 v4, s[42:43] offset:1024\n" \
    "global_load_lds_dwordx4 v5, s[42:43] offset:2048\n" \
    "global_load_lds_dwordx4 v6, s[42:43] offset:3072\n" \
    "s_add_u32 s42, s42, 229376\n" \
    "s_addc_u32 s43, s43, 0\n" \
    "s_add_u32 m0, s45, 43392\n" \
    "s_nop 0\n" \
    "global_load_lds_dwordx4 v3, s[42:43] offset:0\n" \
    "global_load_lds_dwordx4 v4, s[42:43] offset:1024\n" \
    "global_load_lds_dwordx4 v5, s[42:43] offset:2048\n" \
    "global_load_lds_dwordx4 v6, s[42:43] offset:3072\n" \
    "s_add_u32 s42, s42, 229376\n" \
    "s_addc_u32 s43, s43, 0\n"
    : "+{s[42:43]}"(pB) : "{v3}"(gu_voffB(lane, wn, 0)), "{v4}"(gu_voffB(lane, wn, 1)), "{v5}"(gu_voffB(lane, wn, 2)), "{v6}"(gu_voffB(lane, wn, 3)),
      "{s45}"(lb + G3_B + wm * 7184 + wn * 3072) : "memory", "scc");
}
DI unsigned down_voffB(int lane, int w) { return (8 * w + (lane >> 3)) * 2048 + (((((lane & 7) >> 1) ^ (lane >> 4)) << 1) | (lane & 1)) * 16; }
DI void stage_down(const Args& A, LAS char* lds) {
  int w = A.w; unsigned bx = blockIdx.x; char* ws = A.ws;
  asm volatile("" : "+s"(w), "+s"(bx), "+s"(ws));
  LANE_IDS
  const unsigned lb = (unsigned)(size_t)lds;
  const int xcd = bx & 7, bi = bx >> 3, ks = xcd & 1, pn = DOWN_PN;
  const char* pB = (const char*)A.wd + (size_t)ks * 1792 * 2048 + pn * 256 - 1024;
  const unsigned voffB = down_voffB(lane, w);
  asm volatile(
    "s_nop 4\n" \
    "s_add_u32 m0, s44, 8192\n" \
    "s_nop 0\n" \
    "global_load_lds_dwordx4 v34, s[42:43] offset:0\n" \
    "global_load_lds_dwordx4 v35, s[42:43] offset:1024\n" \
    "s_add_u32 s42, s42, 65536\n" \
    "s_addc_u32 s43, s43, 0\n" \
    "s_add_u32 m0, s44, 24576\n" \
    "s_nop 0\n" \
    "global_load_lds_dwordx4 v34, s[42:43] offset:0\n" \
    "global_load_lds_dwordx4 v35, s[42:43] offset:1024\n" \
    "s_add_u32 s42, s42, 65536\n" \
    "s_addc_u32 s43, s43, 0\n" \
    "s_add_u32 m0, s44, 40960\n" \
    "s_nop 0\n" \
    "global_load_lds_dwordx4 v34, s[42:43] offset:0\n" \
    "global_load_lds_dwordx4 v35, s[42:43] offset:1024\n" \
    "s_add_u32 s42, s42, 65536\n" \
    "s_addc_u32 s43, s43, 0\n" \
    "s_add_u32 m0, s44, 57344\n" \
    "s_nop 0\n" \
    "global_load_lds_dwordx4 v34, s[42:43] offset:0\n" \
    "global_load_lds_dwordx4 v35, s[42:43] offset:1024\n" \
    "s_add_u32 s42, s42, 65536\n" \
    "s_addc_u32 s43, s43, 0\n" \
    "s_add_u32 m0, s44, 73728\n" \
    "s_nop 0\n" \
    "global_load_lds_dwordx4 v34, s[42:43] offset:0\n" \
    "global_load_lds_dwordx4 v35, s[42:43] offset:1024\n" \
    "s_add_u32 s42, s42, 65536\n" \
    "s_addc_u32 s43, s43, 0\n" \
    "s_add_u32 m0, s44, 90112\n" \
    "s_nop 0\n" \
    "global_load_lds_dwordx4 v34, s[42:43] offset:0\n" \
    "global_load_lds_dwordx4 v35, s[42:43] offset:1024\n" \
    "s_add_u32 s42, s42, 65536\n" \
    "s_addc_u32 s43, s43, 0\n" \
    "s_add_u32 m0, s44, 106496\n" \
    "s_nop 0\n" \
    "global_load_lds_dwordx4 v34, s[42:43] offset:0\n" \
    "global_load_lds_dwordx4 v35, s[42:43] offset:1024\n" \
    "s_add_u32 s42, s42, 65536\n" \
    "s_addc_u32 s43, s43, 0\n" \
    "s_add_u32 m0, s44, 122880\n" \
    "s_nop 0\n" \
    "global_load_lds_dwordx4 v34, s[42:43] offset:0\n" \
    "global_load_lds_dwordx4 v35, s[42:43] offset:1024\n" \
    "s_add_u32 s42, s42, 65536\n" \
    "s_addc_u32 s43, s43, 0\n"
    : "+{s[42:43]}"(pB) : "{v34}"(voffB + 1024), "{v35}"(voffB + 128), "{s44}"(lb + w * 2048) : "memory", "scc");
}
#define PUBLISH(stage, nb, set) drain(); wg_barrier(); set; stage(A, lds);
template <int N> DI void drain_but() { asm volatile("s_waitcnt vmcnt(%0)" :: "i"(N) : "memory"); }

DI void phase_qkv(const Args& A, LAS char* lds, int layer) {
  int w = A.w; unsigned bx = blockIdx.x; char* ws = A.ws;
  asm volatile("" : "+s"(w), "+s"(bx), "+s"(ws));
  LANE_IDS
  const unsigned lb = (unsigned)(size_t)lds;
  const int xcd = bx & 7, bi = bx >> 3, pm = 8 * (xcd & 1) + (bi & 7), pn = 4 * (xcd >> 1) + (bi >> 3);
  const int wm = w >> 1, wn = w & 1, hd = 2 * pn + wn;
  const char* pA = ws + WS_XT + layer * 2 * XB + pm * 32 * 8192;
  const char* pB = ws + WS_WQ + (pn * 32 + 6) * 12288;
  const char* pS = ws + WS_ST + layer * 2 * SB + pm * 128 * 8;
  const char* pC = ws + WS_CS;
  const unsigned voffA = lane * 16 + w * 2048;
  const unsigned voffB = lane * 16 + w * 3072;
  const unsigned dsA = lb + wm * 4096 + lane * 16;
  const unsigned rowo = lb + 8192 + (g >> 1) * 3072 + (4 * (g & 1) + q) * 128 + p * 8, sw = 2 * (g & 1) + (q >> 1);
  const unsigned dsB0 = rowo + ((2 * wn) ^ sw) * 32, dsB1 = rowo + ((2 * wn + 1) ^ sw) * 32;
  const unsigned voffS = (32 * w + (lane & 31)) * 8 + (lane >> 5) * 8 * 16384;
  const unsigned voffC = (min(w, 2) * D + pn * 64 + lane) * 4;
  const unsigned m0A = lb + w * 2048, m0B = lb + 8192 + w * 3072;
  f32x32 aq, ak, av; f32x16 stt, cp;
  if (layer) await(w, ws, flag(0, FL_X + pm * 4 + (pn & 3), r));
  asm volatile(
    "s_nop 4\n"
    "s_add_u32 m0, s44, 0\n"
    "s_nop 0\n"
    "global_load_lds_dwordx4 v32, s[40:41] offset:0 sc1\n"
    "global_load_lds_dwordx4 v32, s[40:41] offset:1024 sc1\n"
    "s_add_u32 s40, s40, 8192\n"
    "s_addc_u32 s41, s41, 0\n"
    "s_add_u32 m0, s44, 20480\n"
    "s_nop 0\n"
    "global_load_lds_dwordx4 v32, s[40:41] offset:0 sc1\n"
    "global_load_lds_dwordx4 v32, s[40:41] offset:1024 sc1\n"
    "s_add_u32 s40, s40, 8192\n"
    "s_addc_u32 s41, s41, 0\n"
    "s_add_u32 m0, s44, 40960\n"
    "s_nop 0\n"
    "global_load_lds_dwordx4 v32, s[40:41] offset:0 sc1\n"
    "global_load_lds_dwordx4 v32, s[40:41] offset:1024 sc1\n"
    "s_add_u32 s40, s40, 8192\n"
    "s_addc_u32 s41, s41, 0\n"
    "s_waitcnt vmcnt(4)\n"
    "s_barrier\n"
    "ds_read_b64_tr_b16 v[96:97], v38 offset:0\n"
    "ds_read_b64_tr_b16 v[98:99], v38 offset:6144\n"
    "ds_read_b64_tr_b16 v[100:101], v39 offset:0\n"
    "ds_read_b128 v[80:83], v36 offset:0\n"
    "ds_read_b64_tr_b16 v[102:103], v39 offset:6144\n"
    "ds_read_b64_tr_b16 v[104:105], v38 offset:1024\n"
    "ds_read_b64_tr_b16 v[106:107], v38 offset:7168\n"
    "ds_read_b128 v[84:87], v36 offset:1024\n"
    "ds_read_b64_tr_b16 v[108:109], v39 offset:1024\n"
    "ds_read_b64_tr_b16 v[110:111], v39 offset:7168\n"
    "ds_read_b64_tr_b16 v[112:113], v38 offset:2048\n"
    "ds_read_b128 v[88:91], v36 offset:2048\n"
    "ds_read_b64_tr_b16 v[114:115], v38 offset:8192\n"
    "ds_read_b64_tr_b16 v[116:117], v39 offset:2048\n"
    "ds_read_b64_tr_b16 v[118:119], v39 offset:8192\n"
    "ds_read_b128 v[92:95], v36 offset:3072\n"
    "s_add_u32 m0, s44, 61440\n"
    "s_nop 0\n"
    "global_load_lds_dwordx4 v32, s[40:41] offset:0 sc1\n"
    "global_load_lds_dwordx4 v32, s[40:41] offset:1024 sc1\n"
    "s_add_u32 s40, s40, 8192\n"
    "s_addc_u32 s41, s41, 0\n"
    "s_waitcnt vmcnt(4) lgkmcnt(0)\n"
    "s_barrier\n"
    "v_mfma_f32_16x16x32_bf16 v[160:163], v[96:99], v[80:83], 0\n"
    "s_add_u32 m0, s44, 81920\n"
    "ds_read_b64_tr_b16 v[136:137], v38 offset:20480\n"
    "v_mfma_f32_16x16x32_bf16 v[176:179], v[100:103], v[80:83], 0\n"
    "global_load_lds_dwordx4 v32, s[40:41] offset:0 sc1\n"
    "ds_read_b64_tr_b16 v[138:139], v38 offset:26624\n"
    "v_mfma_f32_16x16x32_bf16 v[192:195], v[80:83], v[104:107], 0\n"
    "global_load_lds_dwordx4 v32, s[40:41] offset:1024 sc1\n"
    "s_add_u32 s40, s40, 8192\n"
    "s_addc_u32 s41, s41, 0\n"
    "ds_read_b64_tr_b16 v[140:141], v39 offset:20480\n"
    "v_mfma_f32_16x16x32_bf16 v[208:211], v[80:83], v[108:111], 0\n"
    "s_add_u32 m0, s44, 102400\n"
    "ds_read_b128 v[120:123], v36 offset:20480\n"
    "v_mfma_f32_16x16x32_bf16 v[224:227], v[80:83], v[112:115], 0\n"
    "global_load_lds_dwordx4 v32, s[40:41] offset:0 sc1\n"
    "ds_read_b64_tr_b16 v[142:143], v39 offset:26624\n"
    "v_mfma_f32_16x16x32_bf16 v[240:243], v[80:83], v[116:119], 0\n"
    "ds_read_b64_tr_b16 v[144:145], v38 offset:21504\n"
    "v_mfma_f32_16x16x32_bf16 v[164:167], v[96:99], v[84:87], 0\n"
    "global_load_lds_dwordx4 v32, s[40:41] offset:1024 sc1\n"
    "s_add_u32 s40, s40, 8192\n"
    "s_addc_u32 s41, s41, 0\n"
    "ds_read_b64_tr_b16 v[146:147], v38 offset:27648\n"
    "v_mfma_f32_16x16x32_bf16 v[180:183], v[100:103], v[84:87], 0\n"
    "s_add_u32 m0, s44, 0\n"
    "ds_read_b128 v[124:127], v36 offset:21504\n"
    "v_mfma_f32_16x16x32_bf16 v[196:199], v[84:87], v[104:107], 0\n"
    "global_load_lds_dwordx4 v32, s[40:41] offset:0 sc1\n"
    "ds_read_b64_tr_b16 v[148:149], v39 offset:21504\n"
    "v_mfma_f32_16x16x32_bf16 v[212:215], v[84:87], v[108:111], 0\n"
    "ds_read_b64_tr_b16 v[150:151], v39 offset:27648\n"
    "v_mfma_f32_16x16x32_bf16 v[228:231], v[84:87], v[112:115], 0\n"
    "global_load_lds_dwordx4 v32, s[40:41] offset:1024 sc1\n"
    "s_add_u32 s40, s40, 8192\n"
    "s_addc_u32 s41, s41, 0\n"
    "ds_read_b64_tr_b16 v[152:153], v38 offset:22528\n"
    "v_mfma_f32_16x16x32_bf16 v[244:247], v[84:87], v[116:119], 0\n"
    "s_add_u32 m0, s45, 0\n"
    "ds_read_b128 v[128:131], v36 offset:22528\n"
    "v_mfma_f32_16x16x32_bf16 v[168:171], v[96:99], v[88:91], 0\n"
    "global_load_lds_dwordx4 v33, s[42:43] offset:0\n"
    "ds_read_b64_tr_b16 v[154:155], v38 offset:28672\n"
    "v_mfma_f32_16x16x32_bf16 v[184:187], v[100:103], v[88:91], 0\n"
    "ds_read_b64_tr_b16 v[156:157], v39 offset:22528\n"
    "v_mfma_f32_16x16x32_bf16 v[200:203], v[88:91], v[104:107], 0\n"
    "global_load_lds_dwordx4 v33, s[42:43] offset:1024\n"
    "ds_read_b64_tr_b16 v[158:159], v39 offset:28672\n"
    "v_mfma_f32_16x16x32_bf16 v[216:219], v[88:91], v[108:111], 0\n"
    "ds_read_b128 v[132:135], v36 offset:23552\n"
    "v_mfma_f32_16x16x32_bf16 v[232:235], v[88:91], v[112:115], 0\n"
    "global_load_lds_dwordx4 v33, s[42:43] offset:2048\n"
    "s_add_u32 s42, s42, 12288\n"
    "s_addc_u32 s43, s43, 0\n"
    "v_mfma_f32_16x16x32_bf16 v[248:251], v[88:91], v[116:119], 0\n"
    "v_mfma_f32_16x16x32_bf16 v[172:175], v[96:99], v[92:95], 0\n"
    "v_mfma_f32_16x16x32_bf16 v[188:191], v[100:103], v[92:95], 0\n"
    "v_mfma_f32_16x16x32_bf16 v[204:207], v[92:95], v[104:107], 0\n"
    "v_mfma_f32_16x16x32_bf16 v[220:223], v[92:95], v[108:111], 0\n"
    "v_mfma_f32_16x16x32_bf16 v[236:239], v[92:95], v[112:115], 0\n"
    "s_waitcnt vmcnt(11) lgkmcnt(0)\n"
    "s_barrier\n"
    "v_mfma_f32_16x16x32_bf16 v[252:255], v[92:95], v[116:119], 0\n"
    "v_mfma_f32_16x16x32_bf16 v[160:163], v[136:139], v[120:123], v[160:163]\n"
    "s_add_u32 m0, s44, 20480\n"
    "ds_read_b64_tr_b16 v[96:97], v38 offset:40960\n"
    "v_mfma_f32_16x16x32_bf16 v[176:179], v[140:143], v[120:123], v[176:179]\n"
    "global_load_lds_dwordx4 v32, s[40:41] offset:0 sc1\n"
    "ds_read_b64_tr_b16 v[98:99], v38 offset:47104\n"
    "v_mfma_f32_16x16x32_bf16 v[192:195], v[120:123], v[144:147], v[192:195]\n"
    "ds_read_b64_tr_b16 v[100:101], v39 offset:40960\n"
    "v_mfma_f32_16x16x32_bf16 v[208:211], v[120:123], v[148:151], v[208:211]\n"
    "ds_read_b128 v[80:83], v36 offset:40960\n"
    "v_mfma_f32_16x16x32_bf16 v[224:227], v[120:123], v[152:155], v[224:227]\n"
    "global_load_lds_dwordx4 v32, s[40:41] offset:1024 sc1\n"
    "s_add_u32 s40, s40, 8192\n"
    "s_addc_u32 s41, s41, 0\n"
    "ds_read_b64_tr_b16 v[102:103], v39 offset:47104\n"
    "v_mfma_f32_16x16x32_bf16 v[240:243], v[120:123], v[156:159], v[240:243]\n"
    "ds_read_b64_tr_b16 v[104:105], v38 offset:41984\n"
    "v_mfma_f32_16x16x32_bf16 v[164:167], v[136:139], v[124:127], v[164:167]\n"
    "s_add_u32 m0, s45, 20480\n"
    "ds_read_b64_tr_b16 v[106:107], v38 offset:48128\n"
    "v_mfma_f32_16x16x32_bf16 v[180:183], v[140:143], v[124:127], v[180:183]\n"
    "global_load_lds_dwordx4 v33, s[42:43] offset:0\n"
    "ds_read_b128 v[84:87], v36 offset:41984\n"
    "v_mfma_f32_16x16x32_bf16 v[196:199], v[124:127], v[144:147], v[196:199]\n"
    "ds_read_b64_tr_b16 v[108:109], v39 offset:41984\n"
    "v_mfma_f32_16x16x32_bf16 v[212:215], v[124:127], v[148:151], v[212:215]\n"
    "ds_read_b64_tr_b16 v[110:111], v39 offset:48128\n"
    "v_mfma_f32_16x16x32_bf16 v[228:231], v[124:127], v[152:155], v[228:231]\n"
    "ds_read_b64_tr_b16 v[112:113], v38 offset:43008\n"
    "v_mfma_f32_16x16x32_bf16 v[244:247], v[124:127], v[156:159], v[244:247]\n"
    "global_load_lds_dwordx4 v33, s[42:43] offset:1024\n"
    "ds_read_b128 v[88:91], v36 offset:43008\n"
    "v_mfma_f32_16x16x32_bf16 v[168:171], v[136:139], v[128:131], v[168:171]\n"
    "ds_read_b64_tr_b16 v[114:115], v38 offset:49152\n"
    "v_mfma_f32_16x16x32_bf16 v[184:187], v[140:143], v[128:131], v[184:187]\n"
    "ds_read_b64_tr_b16 v[116:117], v39 offset:43008\n"
    "v_mfma_f32_16x16x32_bf16 v[200:203], v[128:131], v[144:147], v[200:203]\n"
    "global_load_lds_dwordx4 v33, s[42:43] offset:2048\n"
    "s_add_u32 s42, s42, 12288\n"
    "s_addc_u32 s43, s43, 0\n"
    "ds_read_b64_tr_b16 v[118:119], v39 offset:49152\n"
    "v_mfma_f32_16x16x32_bf16 v[216:219], v[128:131], v[148:151], v[216:219]\n"
    "ds_read_b128 v[92:95], v36 offset:44032\n"
    "v_mfma_f32_16x16x32_bf16 v[232:235], v[128:131], v[152:155], v[232:235]\n"
    "v_mfma_f32_16x16x32_bf16 v[248:251], v[128:131], v[156:159], v[248:251]\n"
    "v_mfma_f32_16x16x32_bf16 v[172:175], v[136:139], v[132:135], v[172:175]\n"
    "v_mfma_f32_16x16x32_bf16 v[188:191], v[140:143], v[132:135], v[188:191]\n"
    "v_mfma_f32_16x16x32_bf16 v[204:207], v[132:135], v[144:147], v[204:207]\n"
    "v_mfma_f32_16x16x32_bf16 v[220:223], v[132:135], v[148:151], v[220:223]\n"
    "v_mfma_f32_16x16x32_bf16 v[236:239], v[132:135], v[152:155], v[236:239]\n"
    "s_waitcnt vmcnt(14) lgkmcnt(0)\n"
    "s_barrier\n"
    "v_mfma_f32_16x16x32_bf16 v[252:255], v[132:135], v[156:159], v[252:255]\n"
    "v_mfma_f32_16x16x32_bf16 v[160:163], v[96:99], v[80:83], v[160:163]\n"
    "s_add_u32 m0, s44, 40960\n"
    "ds_read_b64_tr_b16 v[136:137], v40 offset:0\n"
    "v_mfma_f32_16x16x32_bf16 v[176:179], v[100:103], v[80:83], v[176:179]\n"
    "global_load_lds_dwordx4 v32, s[40:41] offset:0 sc1\n"
    "ds_read_b64_tr_b16 v[138:139], v40 offset:6144\n"
    "v_mfma_f32_16x16x32_bf16 v[192:195], v[80:83], v[104:107], v[192:195]\n"
    "ds_read_b64_tr_b16 v[140:141], v41 offset:0\n"
    "v_mfma_f32_16x16x32_bf16 v[208:211], v[80:83], v[108:111], v[208:211]\n"
    "ds_read_b128 v[120:123], v37 offset:0\n"
    "v_mfma_f32_16x16x32_bf16 v[224:227], v[80:83], v[112:115], v[224:227]\n"
    "global_load_lds_dwordx4 v32, s[40:41] offset:1024 sc1\n"
    "s_add_u32 s40, s40, 8192\n"
    "s_addc_u32 s41, s41, 0\n"
    "ds_read_b64_tr_b16 v[142:143], v41 offset:6144\n"
    "v_mfma_f32_16x16x32_bf16 v[240:243], v[80:83], v[116:119], v[240:243]\n"
    "ds_read_b64_tr_b16 v[144:145], v40 offset:1024\n"
    "v_mfma_f32_16x16x32_bf16 v[164:167], v[96:99], v[84:87], v[164:167]\n"
    "s_add_u32 m0, s45, 40960\n"
    "ds_read_b64_tr_b16 v[146:147], v40 offset:7168\n"
    "v_mfma_f32_16x16x32_bf16 v[180:183], v[100:103], v[84:87], v[180:183]\n"
    "global_load_lds_dwordx4 v33, s[42:43] offset:0\n"
    "ds_read_b128 v[124:127], v37 offset:1024\n"
    "v_mfma_f32_16x16x32_bf16 v[196:199], v[84:87], v[104:107], v[196:199]\n"
    "ds_read_b64_tr_b16 v[148:149], v41 offset:1024\n"
    "v_mfma_f32_16x16x32_bf16 v[212:215], v[84:87], v[108:111], v[212:215]\n"
    "ds_read_b64_tr_b16 v[150:151], v41 offset:7168\n"
    "v_mfma_f32_16x16x32_bf16 v[228:231], v[84:87], v[112:115], v[228:231]\n"
    "ds_read_b64_tr_b16 v[152:153], v40 offset:2048\n"
    "v_mfma_f32_16x16x32_bf16 v[244:247], v[84:87], v[116:119], v[244:247]\n"
    "global_load_lds_dwordx4 v33, s[42:43] offset:1024\n"
    "ds_read_b128 v[128:131], v37 offset:2048\n"
    "v_mfma_f32_16x16x32_bf16 v[168:171], v[96:99], v[88:91], v[168:171]\n"
    "ds_read_b64_tr_b16 v[154:155], v40 offset:8192\n"
    "v_mfma_f32_16x16x32_bf16 v[184:187], v[100:103], v[88:91], v[184:187]\n"
    "ds_read_b64_tr_b16 v[156:157], v41 offset:2048\n"
    "v_mfma_f32_16x16x32_bf16 v[200:203], v[88:91], v[104:107], v[200:203]\n"
    "global_load_lds_dwordx4 v33, s[42:43] offset:2048\n"
    "s_add_u32 s42, s42, 12288\n"
    "s_addc_u32 s43, s43, 0\n"
    "ds_read_b64_tr_b16 v[158:159], v41 offset:8192\n"
    "v_mfma_f32_16x16x32_bf16 v[216:219], v[88:91], v[108:111], v[216:219]\n"
    "ds_read_b128 v[132:135], v37 offset:3072\n"
    "v_mfma_f32_16x16x32_bf16 v[232:235], v[88:91], v[112:115], v[232:235]\n"
    "v_mfma_f32_16x16x32_bf16 v[248:251], v[88:91], v[116:119], v[248:251]\n"
    "v_mfma_f32_16x16x32_bf16 v[172:175], v[96:99], v[92:95], v[172:175]\n"
    "v_mfma_f32_16x16x32_bf16 v[188:191], v[100:103], v[92:95], v[188:191]\n"
    "v_mfma_f32_16x16x32_bf16 v[204:207], v[92:95], v[104:107], v[204:207]\n"
    "v_mfma_f32_16x16x32_bf16 v[220:223], v[92:95], v[108:111], v[220:223]\n"
    "v_mfma_f32_16x16x32_bf16 v[236:239], v[92:95], v[112:115], v[236:239]\n"
    "s_waitcnt vmcnt(17) lgkmcnt(0)\n"
    "s_barrier\n"
    "v_mfma_f32_16x16x32_bf16 v[252:255], v[92:95], v[116:119], v[252:255]\n"
    "v_mfma_f32_16x16x32_bf16 v[160:163], v[136:139], v[120:123], v[160:163]\n"
    "s_add_u32 m0, s44, 61440\n"
    "ds_read_b64_tr_b16 v[96:97], v40 offset:20480\n"
    "v_mfma_f32_16x16x32_bf16 v[176:179], v[140:143], v[120:123], v[176:179]\n"
    "global_load_lds_dwordx4 v32, s[40:41] offset:0 sc1\n"
    "ds_read_b64_tr_b16 v[98:99], v40 offset:26624\n"
    "v_mfma_f32_16x16x32_bf16 v[192:195], v[120:123], v[144:147], v[192:195]\n"
    "ds_read_b64_tr_b16 v[100:101], v41 offset:20480\n"
    "v_mfma_f32_16x16x32_bf16 v[208:211], v[120:123], v[148:151], v[208:211]\n"
    "ds_read_b128 v[80:83], v37 offset:20480\n"
    "v_mfma_f32_16x16x32_bf16 v[224:227], v[120:123], v[152:155], v[224:227]\n"
    "global_load_lds_dwordx4 v32, s[40:41] offset:1024 sc1\n"
    "s_add_u32 s40, s40, 8192\n"
    "s_addc_u32 s41, s41, 0\n"
    "ds_read_b64_tr_b16 v[102:103], v41 offset:26624\n"
    "v_mfma_f32_16x16x32_bf16 v[240:243], v[120:123], v[156:159], v[240:243]\n"
    "ds_read_b64_tr_b16 v[104:105], v40 offset:21504\n"
    "v_mfma_f32_16x16x32_bf16 v[164:167], v[136:139], v[124:127], v[164:167]\n"
    "s_add_u32 m0, s45, 61440\n"
    "ds_read_b64_tr_b16 v[106:107], v40 offset:27648\n"
    "v_mfma_f32_16x16x32_bf16 v[180:183], v[140:143], v[124:127], v[180:183]\n"
    "global_load_lds_dwordx4 v33, s[42:43] offset:0\n"
    "ds_read_b128 v[84:87], v37 offset:21504\n"
    "v_mfma_f32_16x16x32_bf16 v[196:199], v[124:127], v[144:147], v[196:199]\n"
    "ds_read_b64_tr_b16 v[108:109], v41 offset:21504\n"
    "v_mfma_f32_16x16x32_bf16 v[212:215], v[124:127], v[148:151], v[212:215]\n"
    "ds_read_b64_tr_b16 v[110:111], v41 offset:27648\n"
    "v_mfma_f32_16x16x32_bf16 v[228:231], v[124:127], v[152:155], v[228:231]\n"
    "ds_read_b64_tr_b16 v[112:113], v40 offset:22528\n"
    "v_mfma_f32_16x16x32_bf16 v[244:247], v[124:127], v[156:159], v[244:247]\n"
    "global_load_lds_dwordx4 v33, s[42:43] offset:1024\n"
    "ds_read_b128 v[88:91], v37 offset:22528\n"
    "v_mfma_f32_16x16x32_bf16 v[168:171], v[136:139], v[128:131], v[168:171]\n"
    "ds_read_b64_tr_b16 v[114:115], v40 offset:28672\n"
    "v_mfma_f32_16x16x32_bf16 v[184:187], v[140:143], v[128:131], v[184:187]\n"
    "ds_read_b64_tr_b16 v[116:117], v41 offset:22528\n"
    "v_mfma_f32_16x16x32_bf16 v[200:203], v[128:131], v[144:147], v[200:203]\n"
    "global_load_lds_dwordx4 v33, s[42:43] offset:2048\n"
    "s_add_u32 s42, s42, 12288\n"
    "s_addc_u32 s43, s43, 0\n"
    "ds_read_b64_tr_b16 v[118:119], v41 offset:28672\n"
    "v_mfma_f32_16x16x32_bf16 v[216:219], v[128:131], v[148:151], v[216:219]\n"
    "ds_read_b128 v[92:95], v37 offset:23552\n"
    "v_mfma_f32_16x16x32_bf16 v[232:235], v[128:131], v[152:155], v[232:235]\n"
    "v_mfma_f32_16x16x32_bf16 v[248:251], v[128:131], v[156:159], v[248:251]\n"
    "v_mfma_f32_16x16x32_bf16 v[172:175], v[136:139], v[132:135], v[172:175]\n"
    "v_mfma_f32_16x16x32_bf16 v[188:191], v[140:143], v[132:135], v[188:191]\n"
    "v_mfma_f32_16x16x32_bf16 v[204:207], v[132:135], v[144:147], v[204:207]\n"
    "v_mfma_f32_16x16x32_bf16 v[220:223], v[132:135], v[148:151], v[220:223]\n"
    "v_mfma_f32_16x16x32_bf16 v[236:239], v[132:135], v[152:155], v[236:239]\n"
    "s_waitcnt vmcnt(20) lgkmcnt(0)\n"
    "s_barrier\n"
    "v_mfma_f32_16x16x32_bf16 v[252:255], v[132:135], v[156:159], v[252:255]\n"
    "s_mov_b32 s46, 3\n"
    "1:\n"
    "v_mfma_f32_16x16x32_bf16 v[160:163], v[96:99], v[80:83], v[160:163]\n"
    "s_add_u32 m0, s44, 81920\n"
    "ds_read_b64_tr_b16 v[136:137], v40 offset:40960\n"
    "v_mfma_f32_16x16x32_bf16 v[176:179], v[100:103], v[80:83], v[176:179]\n"
    "global_load_lds_dwordx4 v32, s[40:41] offset:0 sc1\n"
    "ds_read_b64_tr_b16 v[138:139], v40 offset:47104\n"
    "v_mfma_f32_16x16x32_bf16 v[192:195], v[80:83], v[104:107], v[192:195]\n"
    "ds_read_b64_tr_b16 v[140:141], v41 offset:40960\n"
    "v_mfma_f32_16x16x32_bf16 v[208:211], v[80:83], v[108:111], v[208:211]\n"
    "ds_read_b128 v[120:123], v37 offset:40960\n"
    "v_mfma_f32_16x16x32_bf16 v[224:227], v[80:83], v[112:115], v[224:227]\n"
    "global_load_lds_dwordx4 v32, s[40:41] offset:1024 sc1\n"
    "s_add_u32 s40, s40, 8192\n"
    "s_addc_u32 s41, s41, 0\n"
    "ds_read_b64_tr_b16 v[142:143], v41 offset:47104\n"
    "v_mfma_f32_16x16x32_bf16 v[240:243], v[80:83], v[116:119], v[240:243]\n"
    "ds_read_b64_tr_b16 v[144:145], v40 offset:41984\n"
    "v_mfma_f32_16x16x32_bf16 v[164:167], v[96:99], v[84:87], v[164:167]\n"
    "s_add_u32 m0, s45, 81920\n"
    "ds_read_b64_tr_b16 v[146:147], v40 offset:48128\n"
    "v_mfma_f32_16x16x32_bf16 v[180:183], v[100:103], v[84:87], v[180:183]\n"
    "global_load_lds_dwordx4 v33, s[42:43] offset:0\n"
    "ds_read_b128 v[124:127], v37 offset:41984\n"
    "v_mfma_f32_16x16x32_bf16 v[196:199], v[84:87], v[104:107], v[196:199]\n"
    "ds_read_b64_tr_b16 v[148:149], v41 offset:41984\n"
    "v_mfma_f32_16x16x32_bf16 v[212:215], v[84:87], v[108:111], v[212:215]\n"
    "ds_read_b64_tr_b16 v[150:151], v41 offset:48128\n"
    "v_mfma_f32_16x16x32_bf16 v[228:231], v[84:87], v[112:115], v[228:231]\n"
    "ds_read_b64_tr_b16 v[152:153], v40 offset:43008\n"
    "v_mfma_f32_16x16x32_bf16 v[244:247], v[84:87], v[116:119], v[244:247]\n"
    "global_load_lds_dwordx4 v33, s[42:43] offset:1024\n"
    "ds_read_b128 v[128:131], v37 offset:43008\n"
    "v_mfma_f32_16x16x32_bf16 v[168:171], v[96:99], v[88:91], v[168:171]\n"
    "ds_read_b64_tr_b16 v[154:155], v40 offset:49152\n"
    "v_mfma_f32_16x16x32_bf16 v[184:187], v[100:103], v[88:91], v[184:187]\n"
    "ds_read_b64_tr_b16 v[156:157], v41 offset:43008\n"
    "v_mfma_f32_16x16x32_bf16 v[200:203], v[88:91], v[104:107], v[200:203]\n"
    "global_load_lds_dwordx4 v33, s[42:43] offset:2048\n"
    "s_add_u32 s42, s42, 12288\n"
    "s_addc_u32 s43, s43, 0\n"
    "ds_read_b64_tr_b16 v[158:159], v41 offset:49152\n"
    "v_mfma_f32_16x16x32_bf16 v[216:219], v[88:91], v[108:111], v[216:219]\n"
    "ds_read_b128 v[132:135], v37 offset:44032\n"
    "v_mfma_f32_16x16x32_bf16 v[232:235], v[88:91], v[112:115], v[232:235]\n"
    "v_mfma_f32_16x16x32_bf16 v[248:251], v[88:91], v[116:119], v[248:251]\n"
    "v_mfma_f32_16x16x32_bf16 v[172:175], v[96:99], v[92:95], v[172:175]\n"
    "v_mfma_f32_16x16x32_bf16 v[188:191], v[100:103], v[92:95], v[188:191]\n"
    "v_mfma_f32_16x16x32_bf16 v[204:207], v[92:95], v[104:107], v[204:207]\n"
    "v_mfma_f32_16x16x32_bf16 v[220:223], v[92:95], v[108:111], v[220:223]\n"
    "v_mfma_f32_16x16x32_bf16 v[236:239], v[92:95], v[112:115], v[236:239]\n"
    "s_waitcnt vmcnt(20) lgkmcnt(0)\n"
    "s_barrier\n"
    "v_mfma_f32_16x16x32_bf16 v[252:255], v[92:95], v[116:119], v[252:255]\n"
    "v_mfma_f32_16x16x32_bf16 v[160:163], v[136:139], v[120:123], v[160:163]\n"
    "s_add_u32 m0, s44, 102400\n"
    "ds_read_b64_tr_b16 v[96:97], v38 offset:0\n"
    "v_mfma_f32_16x16x32_bf16 v[176:179], v[140:143], v[120:123], v[176:179]\n"
    "global_load_lds_dwordx4 v32, s[40:41] offset:0 sc1\n"
    "ds_read_b64_tr_b16 v[98:99], v38 offset:6144\n"
    "v_mfma_f32_16x16x32_bf16 v[192:195], v[120:123], v[144:147], v[192:195]\n"
    "ds_read_b64_tr_b16 v[100:101], v39 offset:0\n"
    "v_mfma_f32_16x16x32_bf16 v[208:211], v[120:123], v[148:151], v[208:211]\n"
    "ds_read_b128 v[80:83], v36 offset:0\n"
    "v_mfma_f32_16x16x32_bf16 v[224:227], v[120:123], v[152:155], v[224:227]\n"
    "global_load_lds_dwordx4 v32, s[40:41] offset:1024 sc1\n"
    "s_add_u32 s40, s40, 8192\n"
    "s_addc_u32 s41, s41, 0\n"
    "ds_read_b64_tr_b16 v[102:103], v39 offset:6144\n"
    "v_mfma_f32_16x16x32_bf16 v[240:243], v[120:123], v[156:159], v[240:243]\n"
    "ds_read_b64_tr_b16 v[104:105], v38 offset:1024\n"
    "v_mfma_f32_16x16x32_bf16 v[164:167], v[136:139], v[124:127], v[164:167]\n"
    "s_add_u32 m0, s45, 102400\n"
    "ds_read_b64_tr_b16 v[106:107], v38 offset:7168\n"
    "v_mfma_f32_16x16x32_bf16 v[180:183], v[140:143], v[124:127], v[180:183]\n"
    "global_load_lds_dwordx4 v33, s[42:43] offset:0\n"
    "ds_read_b128 v[84:87], v36 offset:1024\n"
    "v_mfma_f32_16x16x32_bf16 v[196:199], v[124:127], v[144:147], v[196:199]\n"
    "ds_read_b64_tr_b16 v[108:109], v39 offset:1024\n"
    "v_mfma_f32_16x16x32_bf16 v[212:215], v[124:127], v[148:151], v[212:215]\n"
    "ds_read_b64_tr_b16 v[110:111], v39 offset:7168\n"
    "v_mfma_f32_16x16x32_bf16 v[228:231], v[124:127], v[152:155], v[228:231]\n"
    "ds_read_b64_tr_b16 v[112:113], v38 offset:2048\n"
    "v_mfma_f32_16x16x32_bf16 v[244:247], v[124:127], v[156:159], v[244:247]\n"
    "global_load_lds_dwordx4 v33, s[42:43] offset:1024\n"
    "ds_read_b128 v[88:91], v36 offset:2048\n"
    "v_mfma_f32_16x16x32_bf16 v[168:171], v[136:139], v[128:131], v[168:171]\n"
    "ds_read_b64_tr_b16 v[114:115], v38 offset:8192\n"
    "v_mfma_f32_16x16x32_bf16 v[184:187], v[140:143], v[128:131], v[184:187]\n"
    "ds_read_b64_tr_b16 v[116:117], v39 offset:2048\n"
    "v_mfma_f32_16x16x32_bf16 v[200:203], v[128:131], v[144:147], v[200:203]\n"
    "global_load_lds_dwordx4 v33, s[42:43] offset:2048\n"
    "s_add_u32 s42, s42, 12288\n"
    "s_addc_u32 s43, s43, 0\n"
    "ds_read_b64_tr_b16 v[118:119], v39 offset:8192\n"
    "v_mfma_f32_16x16x32_bf16 v[216:219], v[128:131], v[148:151], v[216:219]\n"
    "ds_read_b128 v[92:95], v36 offset:3072\n"
    "v_mfma_f32_16x16x32_bf16 v[232:235], v[128:131], v[152:155], v[232:235]\n"
    "v_mfma_f32_16x16x32_bf16 v[248:251], v[128:131], v[156:159], v[248:251]\n"
    "v_mfma_f32_16x16x32_bf16 v[172:175], v[136:139], v[132:135], v[172:175]\n"
    "v_mfma_f32_16x16x32_bf16 v[188:191], v[140:143], v[132:135], v[188:191]\n"
    "v_mfma_f32_16x16x32_bf16 v[204:207], v[132:135], v[144:147], v[204:207]\n"
    "v_mfma_f32_16x16x32_bf16 v[220:223], v[132:135], v[148:151], v[220:223]\n"
    "v_mfma_f32_16x16x32_bf16 v[236:239], v[132:135], v[152:155], v[236:239]\n"
    "s_waitcnt vmcnt(20) lgkmcnt(0)\n"
    "s_barrier\n"
    "v_mfma_f32_16x16x32_bf16 v[252:255], v[132:135], v[156:159], v[252:255]\n"
    "v_mfma_f32_16x16x32_bf16 v[160:163], v[96:99], v[80:83], v[160:163]\n"
    "s_add_u32 m0, s44, 0\n"
    "ds_read_b64_tr_b16 v[136:137], v38 offset:20480\n"
    "v_mfma_f32_16x16x32_bf16 v[176:179], v[100:103], v[80:83], v[176:179]\n"
    "global_load_lds_dwordx4 v32, s[40:41] offset:0 sc1\n"
    "ds_read_b64_tr_b16 v[138:139], v38 offset:26624\n"
    "v_mfma_f32_16x16x32_bf16 v[192:195], v[80:83], v[104:107], v[192:195]\n"
    "ds_read_b64_tr_b16 v[140:141], v39 offset:20480\n"
    "v_mfma_f32_16x16x32_bf16 v[208:211], v[80:83], v[108:111], v[208:211]\n"
    "ds_read_b128 v[120:123], v36 offset:20480\n"
    "v_mfma_f32_16x16x32_bf16 v[224:227], v[80:83], v[112:115], v[224:227]\n"
    "global_load_lds_dwordx4 v32, s[40:41] offset:1024 sc1\n"
    "s_add_u32 s40, s40, 8192\n"
    "s_addc_u32 s41, s41, 0\n"
    "ds_read_b64_tr_b16 v[142:143], v39 offset:26624\n"
    "v_mfma_f32_16x16x32_bf16 v[240:243], v[80:83], v[116:119], v[240:243]\n"
    "ds_read_b64_tr_b16 v[144:145], v38 offset:21504\n"
    "v_mfma_f32_16x16x32_bf16 v[164:167], v[96:99], v[84:87], v[164:167]\n"
    "s_add_u32 m0, s45, 0\n"
    "ds_read_b64_tr_b16 v[146:147], v38 offset:27648\n"
    "v_mfma_f32_16x16x32_bf16 v[180:183], v[100:103], v[84:87], v[180:183]\n"
    "global_load_lds_dwordx4 v33, s[42:43] offset:0\n"
    "ds_read_b128 v[124:127], v36 offset:21504\n"
    "v_mfma_f32_16x16x32_bf16 v[196:199], v[84:87], v[104:107], v[196:199]\n"
    "ds_read_b64_tr_b16 v[148:149], v39 offset:21504\n"
    "v_mfma_f32_16x16x32_bf16 v[212:215], v[84:87], v[108:111], v[212:215]\n"
    "ds_read_b64_tr_b16 v[150:151], v39 offset:27648\n"
    "v_mfma_f32_16x16x32_bf16 v[228:231], v[84:87], v[112:115], v[228:231]\n"
    "ds_read_b64_tr_b16 v[152:153], v38 offset:22528\n"
    "v_mfma_f32_16x16x32_bf16 v[244:247], v[84:87], v[116:119], v[244:247]\n"
    "global_load_lds_dwordx4 v33, s[42:43] offset:1024\n"
    "ds_read_b128 v[128:131], v36 offset:22528\n"
    "v_mfma_f32_16x16x32_bf16 v[168:171], v[96:99], v[88:91], v[168:171]\n"
    "ds_read_b64_tr_b16 v[154:155], v38 offset:28672\n"
    "v_mfma_f32_16x16x32_bf16 v[184:187], v[100:103], v[88:91], v[184:187]\n"
    "ds_read_b64_tr_b16 v[156:157], v39 offset:22528\n"
    "v_mfma_f32_16x16x32_bf16 v[200:203], v[88:91], v[104:107], v[200:203]\n"
    "global_load_lds_dwordx4 v33, s[42:43] offset:2048\n"
    "s_add_u32 s42, s42, 12288\n"
    "s_addc_u32 s43, s43, 0\n"
    "ds_read_b64_tr_b16 v[158:159], v39 offset:28672\n"
    "v_mfma_f32_16x16x32_bf16 v[216:219], v[88:91], v[108:111], v[216:219]\n"
    "ds_read_b128 v[132:135], v36 offset:23552\n"
    "v_mfma_f32_16x16x32_bf16 v[232:235], v[88:91], v[112:115], v[232:235]\n"
    "v_mfma_f32_16x16x32_bf16 v[248:251], v[88:91], v[116:119], v[248:251]\n"
    "v_mfma_f32_16x16x32_bf16 v[172:175], v[96:99], v[92:95], v[172:175]\n"
    "v_mfma_f32_16x16x32_bf16 v[188:191], v[100:103], v[92:95], v[188:191]\n"
    "v_mfma_f32_16x16x32_bf16 v[204:207], v[92:95], v[104:107], v[204:207]\n"
    "v_mfma_f32_16x16x32_bf16 v[220:223], v[92:95], v[108:111], v[220:223]\n"
    "v_mfma_f32_16x16x32_bf16 v[236:239], v[92:95], v[112:115], v[236:239]\n"
    "s_waitcnt vmcnt(20) lgkmcnt(0)\n"
    "s_barrier\n"
    "v_mfma_f32_16x16x32_bf16 v[252:255], v[92:95], v[116:119], v[252:255]\n"
    "v_mfma_f32_16x16x32_bf16 v[160:163], v[136:139], v[120:123], v[160:163]\n"
    "s_add_u32 m0, s44, 20480\n"
    "ds_read_b64_tr_b16 v[96:97], v38 offset:40960\n"
    "v_mfma_f32_16x16x32_bf16 v[176:179], v[140:143], v[120:123], v[176:179]\n"
    "global_load_lds_dwordx4 v32, s[40:41] offset:0 sc1\n"
    "ds_read_b64_tr_b16 v[98:99], v38 offset:47104\n"
    "v_mfma_f32_16x16x32_bf16 v[192:195], v[120:123], v[144:147], v[192:195]\n"
    "ds_read_b64_tr_b16 v[100:101], v39 offset:40960\n"
    "v_mfma_f32_16x16x32_bf16 v[208:211], v[120:123], v[148:151], v[208:211]\n"
    "ds_read_b128 v[80:83], v36 offset:40960\n"
    "v_mfma_f32_16x16x32_bf16 v[224:227], v[120:123], v[152:155], v[224:227]\n"
    "global_load_lds_dwordx4 v32, s[40:41] offset:1024 sc1\n"
    "s_add_u32 s40, s40, 8192\n"
    "s_addc_u32 s41, s41, 0\n"
    "ds_read_b64_tr_b16 v[102:103], v39 offset:47104\n"
    "v_mfma_f32_16x16x32_bf16 v[240:243], v[120:123], v[156:159], v[240:243]\n"
    "ds_read_b64_tr_b16 v[104:105], v38 offset:41984\n"
    "v_mfma_f32_16x16x32_bf16 v[164:167], v[136:139], v[124:127], v[164:167]\n"
    "s_add_u32 m0, s45, 20480\n"
    "ds_read_b64_tr_b16 v[106:107], v38 offset:48128\n"
    "v_mfma_f32_16x16x32_bf16 v[180:183], v[140:143], v[124:127], v[180:183]\n"
    "global_load_lds_dwordx4 v33, s[42:43] offset:0\n"
    "ds_read_b128 v[84:87], v36 offset:41984\n"
    "v_mfma_f32_16x16x32_bf16 v[196:199], v[124:127], v[144:147], v[196:199]\n"
    "ds_read_b64_tr_b16 v[108:109], v39 offset:41984\n"
    "v_mfma_f32_16x16x32_bf16 v[212:215], v[124:127], v[148:151], v[212:215]\n"
    "ds_read_b64_tr_b16 v[110:111], v39 offset:48128\n"
    "v_mfma_f32_16x16x32_bf16 v[228:231], v[124:127], v[152:155], v[228:231]\n"
    "ds_read_b64_tr_b16 v[112:113], v38 offset:43008\n"
    "v_mfma_f32_16x16x32_bf16 v[244:247], v[124:127], v[156:159], v[244:247]\n"
    "global_load_lds_dwordx4 v33, s[42:43] offset:1024\n"
    "ds_read_b128 v[88:91], v36 offset:43008\n"
    "v_mfma_f32_16x16x32_bf16 v[168:171], v[136:139], v[128:131], v[168:171]\n"
    "ds_read_b64_tr_b16 v[114:115], v38 offset:49152\n"
    "v_mfma_f32_16x16x32_bf16 v[184:187], v[140:143], v[128:131], v[184:187]\n"
    "ds_read_b64_tr_b16 v[116:117], v39 offset:43008\n"
    "v_mfma_f32_16x16x32_bf16 v[200:203], v[128:131], v[144:147], v[200:203]\n"
    "global_load_lds_dwordx4 v33, s[42:43] offset:2048\n"
    "s_add_u32 s42, s42, 12288\n"
    "s_addc_u32 s43, s43, 0\n"
    "ds_read_b64_tr_b16 v[118:119], v39 offset:49152\n"
    "v_mfma_f32_16x16x32_bf16 v[216:219], v[128:131], v[148:151], v[216:219]\n"
    "ds_read_b128 v[92:95], v36 offset:44032\n"
    "v_mfma_f32_16x16x32_bf16 v[232:235], v[128:131], v[152:155], v[232:235]\n"
    "v_mfma_f32_16x16x32_bf16 v[248:251], v[128:131], v[156:159], v[248:251]\n"
    "v_mfma_f32_16x16x32_bf16 v[172:175], v[136:139], v[132:135], v[172:175]\n"
    "v_mfma_f32_16x16x32_bf16 v[188:191], v[140:143], v[132:135], v[188:191]\n"
    "v_mfma_f32_16x16x32_bf16 v[204:207], v[132:135], v[144:147], v[204:207]\n"
    "v_mfma_f32_16x16x32_bf16 v[220:223], v[132:135], v[148:151], v[220:223]\n"
    "v_mfma_f32_16x16x32_bf16 v[236:239], v[132:135], v[152:155], v[236:239]\n"
    "s_waitcnt vmcnt(20) lgkmcnt(0)\n"
    "s_barrier\n"
    "v_mfma_f32_16x16x32_bf16 v[252:255], v[132:135], v[156:159], v[252:255]\n"
    "v_mfma_f32_16x16x32_bf16 v[160:163], v[96:99], v[80:83], v[160:163]\n"
    "s_add_u32 m0, s44, 40960\n"
    "ds_read_b64_tr_b16 v[136:137], v40 offset:0\n"
    "v_mfma_f32_16x16x32_bf16 v[176:179], v[100:103], v[80:83], v[176:179]\n"
    "global_load_lds_dwordx4 v32, s[40:41] offset:0 sc1\n"
    "ds_read_b64_tr_b16 v[138:139], v40 offset:6144\n"
    "v_mfma_f32_16x16x32_bf16 v[192:195], v[80:83], v[104:107], v[192:195]\n"
    "ds_read_b64_tr_b16 v[140:141], v41 offset:0\n"
    "v_mfma_f32_16x16x32_bf16 v[208:211], v[80:83], v[108:111], v[208:211]\n"
    "ds_read_b128 v[120:123], v37 offset:0\n"
    "v_mfma_f32_16x16x32_bf16 v[224:227], v[80:83], v[112:115], v[224:227]\n"
    "global_load_lds_dwordx4 v32, s[40:41] offset:1024 sc1\n"
    "s_add_u32 s40, s40, 8192\n"
    "s_addc_u32 s41, s41, 0\n"
    "ds_read_b64_tr_b16 v[142:143], v41 offset:6144\n"
    "v_mfma_f32_16x16x32_bf16 v[240:243], v[80:83], v[116:119], v[240:243]\n"
    "ds_read_b64_tr_b16 v[144:145], v40 offset:1024\n"
    "v_mfma_f32_16x16x32_bf16 v[164:167], v[96:99], v[84:87], v[164:167]\n"
    "s_add_u32 m0, s45, 40960\n"
    "ds_read_b64_tr_b16 v[146:147], v40 offset:7168\n"
    "v_mfma_f32_16x16x32_bf16 v[180:183], v[100:103], v[84:87], v[180:183]\n"
    "global_load_lds_dwordx4 v33, s[42:43] offset:0\n"
    "ds_read_b128 v[124:127], v37 offset:1024\n"
    "v_mfma_f32_16x16x32_bf16 v[196:199], v[84:87], v[104:107], v[196:199]\n"
    "ds_read_b64_tr_b16 v[148:149], v41 offset:1024\n"
    "v_mfma_f32_16x16x32_bf16 v[212:215], v[84:87], v[108:111], v[212:215]\n"
    "ds_read_b64_tr_b16 v[150:151], v41 offset:7168\n"
    "v_mfma_f32_16x16x32_bf16 v[228:231], v[84:87], v[112:115], v[228:231]\n"
    "ds_read_b64_tr_b16 v[152:153], v40 offset:2048\n"
    "v_mfma_f32_16x16x32_bf16 v[244:247], v[84:87], v[116:119], v[244:247]\n"
    "global_load_lds_dwordx4 v33, s[42:43] offset:1024\n"
    "ds_read_b128 v[128:131], v37 offset:2048\n"
    "v_mfma_f32_16x16x32_bf16 v[168:171], v[96:99], v[88:91], v[168:171]\n"
    "ds_read_b64_tr_b16 v[154:155], v40 offset:8192\n"
    "v_mfma_f32_16x16x32_bf16 v[184:187], v[100:103], v[88:91], v[184:187]\n"
    "ds_read_b64_tr_b16 v[156:157], v41 offset:2048\n"
    "v_mfma_f32_16x16x32_bf16 v[200:203], v[88:91], v[104:107], v[200:203]\n"
    "global_load_lds_dwordx4 v33, s[42:43] offset:2048\n"
    "s_add_u32 s42, s42, 12288\n"
    "s_addc_u32 s43, s43, 0\n"
    "ds_read_b64_tr_b16 v[158:159], v41 offset:8192\n"
    "v_mfma_f32_16x16x32_bf16 v[216:219], v[88:91], v[108:111], v[216:219]\n"
    "ds_read_b128 v[132:135], v37 offset:3072\n"
    "v_mfma_f32_16x16x32_bf16 v[232:235], v[88:91], v[112:115], v[232:235]\n"
    "v_mfma_f32_16x16x32_bf16 v[248:251], v[88:91], v[116:119], v[248:251]\n"
    "v_mfma_f32_16x16x32_bf16 v[172:175], v[96:99], v[92:95], v[172:175]\n"
    "v_mfma_f32_16x16x32_bf16 v[188:191], v[100:103], v[92:95], v[188:191]\n"
    "v_mfma_f32_16x16x32_bf16 v[204:207], v[92:95], v[104:107], v[204:207]\n"
    "v_mfma_f32_16x16x32_bf16 v[220:223], v[92:95], v[108:111], v[220:223]\n"
    "v_mfma_f32_16x16x32_bf16 v[236:239], v[92:95], v[112:115], v[236:239]\n"
    "s_waitcnt vmcnt(20) lgkmcnt(0)\n"
    "s_barrier\n"
    "v_mfma_f32_16x16x32_bf16 v[252:255], v[92:95], v[116:119], v[252:255]\n"
    "v_mfma_f32_16x16x32_bf16 v[160:163], v[136:139], v[120:123], v[160:163]\n"
    "s_add_u32 m0, s44, 61440\n"
    "ds_read_b64_tr_b16 v[96:97], v40 offset:20480\n"
    "v_mfma_f32_16x16x32_bf16 v[176:179], v[140:143], v[120:123], v[176:179]\n"
    "global_load_lds_dwordx4 v32, s[40:41] offset:0 sc1\n"
    "ds_read_b64_tr_b16 v[98:99], v40 offset:26624\n"
    "v_mfma_f32_16x16x32_bf16 v[192:195], v[120:123], v[144:147], v[192:195]\n"
    "ds_read_b64_tr_b16 v[100:101], v41 offset:20480\n"
    "v_mfma_f32_16x16x32_bf16 v[208:211], v[120:123], v[148:151], v[208:211]\n"
    "ds_read_b128 v[80:83], v37 offset:20480\n"
    "v_mfma_f32_16x16x32_bf16 v[224:227], v[120:123], v[152:155], v[224:227]\n"
    "global_load_lds_dwordx4 v32, s[40:41] offset:1024 sc1\n"
    "s_add_u32 s40, s40, 8192\n"
    "s_addc_u32 s41, s41, 0\n"
    "ds_read_b64_tr_b16 v[102:103], v41 offset:26624\n"
    "v_mfma_f32_16x16x32_bf16 v[240:243], v[120:123], v[156:159], v[240:243]\n"
    "ds_read_b64_tr_b16 v[104:105], v40 offset:21504\n"
    "v_mfma_f32_16x16x32_bf16 v[164:167], v[136:139], v[124:127], v[164:167]\n"
    "s_add_u32 m0, s45, 61440\n"
    "ds_read_b64_tr_b16 v[106:107], v40 offset:27648\n"
    "v_mfma_f32_16x16x32_bf16 v[180:183], v[140:143], v[124:127], v[180:183]\n"
    "global_load_lds_dwordx4 v33, s[42:43] offset:0\n"
    "ds_read_b128 v[84:87], v37 offset:21504\n"
    "v_mfma_f32_16x16x32_bf16 v[196:199], v[124:127], v[144:147], v[196:199]\n"
    "ds_read_b64_tr_b16 v[108:109], v41 offset:21504\n"
    "v_mfma_f32_16x16x32_bf16 v[212:215], v[124:127], v[148:151], v[212:215]\n"
    "ds_read_b64_tr_b16 v[110:111], v41 offset:27648\n"
    "v_mfma_f32_16x16x32_bf16 v[228:231], v[124:127], v[152:155], v[228:231]\n"
    "ds_read_b64_tr_b16 v[112:113], v40 offset:22528\n"
    "v_mfma_f32_16x16x32_bf16 v[244:247], v[124:127], v[156:159], v[244:247]\n"
    "global_load_lds_dwordx4 v33, s[42:43] offset:1024\n"
    "ds_read_b128 v[88:91], v37 offset:22528\n"
    "v_mfma_f32_16x16x32_bf16 v[168:171], v[136:139], v[128:131], v[168:171]\n"
    "ds_read_b64_tr_b16 v[114:115], v40 offset:28672\n"
    "v_mfma_f32_16x16x32_bf16 v[184:187], v[140:143], v[128:131], v[184:187]\n"
    "ds_read_b64_tr_b16 v[116:117], v41 offset:22528\n"
    "v_mfma_f32_16x16x32_bf16 v[200:203], v[128:131], v[144:147], v[200:203]\n"
    "global_load_lds_dwordx4 v33, s[42:43] offset:2048\n"
    "s_add_u32 s42, s42, 12288\n"
    "s_addc_u32 s43, s43, 0\n"
    "ds_read_b64_tr_b16 v[118:119], v41 offset:28672\n"
    "v_mfma_f32_16x16x32_bf16 v[216:219], v[128:131], v[148:151], v[216:219]\n"
    "ds_read_b128 v[92:95], v37 offset:23552\n"
    "v_mfma_f32_16x16x32_bf16 v[232:235], v[128:131], v[152:155], v[232:235]\n"
    "v_mfma_f32_16x16x32_bf16 v[248:251], v[128:131], v[156:159], v[248:251]\n"
    "v_mfma_f32_16x16x32_bf16 v[172:175], v[136:139], v[132:135], v[172:175]\n"
    "v_mfma_f32_16x16x32_bf16 v[188:191], v[140:143], v[132:135], v[188:191]\n"
    "v_mfma_f32_16x16x32_bf16 v[204:207], v[132:135], v[144:147], v[204:207]\n"
    "v_mfma_f32_16x16x32_bf16 v[220:223], v[132:135], v[148:151], v[220:223]\n"
    "v_mfma_f32_16x16x32_bf16 v[236:239], v[132:135], v[152:155], v[236:239]\n"
    "s_waitcnt vmcnt(20) lgkmcnt(0)\n"
    "s_barrier\n"
    "v_mfma_f32_16x16x32_bf16 v[252:255], v[132:135], v[156:159], v[252:255]\n"
    "s_sub_u32 s46, s46, 1\n"
    "s_cmp_lg_u32 s46, 0\n"
    "s_cbranch_scc1 1b\n"
    "v_mfma_f32_16x16x32_bf16 v[160:163], v[96:99], v[80:83], v[160:163]\n"
    "s_add_u32 m0, s44, 81920\n"
    "ds_read_b64_tr_b16 v[136:137], v40 offset:40960\n"
    "v_mfma_f32_16x16x32_bf16 v[176:179], v[100:103], v[80:83], v[176:179]\n"
    "global_load_lds_dwordx4 v32, s[40:41] offset:0 sc1\n"
    "ds_read_b64_tr_b16 v[138:139], v40 offset:47104\n"
    "v_mfma_f32_16x16x32_bf16 v[192:195], v[80:83], v[104:107], v[192:195]\n"
    "ds_read_b64_tr_b16 v[140:141], v41 offset:40960\n"
    "v_mfma_f32_16x16x32_bf16 v[208:211], v[80:83], v[108:111], v[208:211]\n"
    "ds_read_b128 v[120:123], v37 offset:40960\n"
    "v_mfma_f32_16x16x32_bf16 v[224:227], v[80:83], v[112:115], v[224:227]\n"
    "global_load_lds_dwordx4 v32, s[40:41] offset:1024 sc1\n"
    "s_add_u32 s40, s40, 8192\n"
    "s_addc_u32 s41, s41, 0\n"
    "ds_read_b64_tr_b16 v[142:143], v41 offset:47104\n"
    "v_mfma_f32_16x16x32_bf16 v[240:243], v[80:83], v[116:119], v[240:243]\n"
    "ds_read_b64_tr_b16 v[144:145], v40 offset:41984\n"
    "v_mfma_f32_16x16x32_bf16 v[164:167], v[96:99], v[84:87], v[164:167]\n"
    "s_add_u32 m0, s45, 81920\n"
    "ds_read_b64_tr_b16 v[146:147], v40 offset:48128\n"
    "v_mfma_f32_16x16x32_bf16 v[180:183], v[100:103], v[84:87], v[180:183]\n"
    "global_load_lds_dwordx4 v33, s[42:43] offset:0\n"
    "ds_read_b128 v[124:127], v37 offset:41984\n"
    "v_mfma_f32_16x16x32_bf16 v[196:199], v[84:87], v[104:107], v[196:199]\n"
    "ds_read_b64_tr_b16 v[148:149], v41 offset:41984\n"
    "v_mfma_f32_16x16x32_bf16 v[212:215], v[84:87], v[108:111], v[212:215]\n"
    "ds_read_b64_tr_b16 v[150:151], v41 offset:48128\n"
    "v_mfma_f32_16x16x32_bf16 v[228:231], v[84:87], v[112:115], v[228:231]\n"
    "ds_read_b64_tr_b16 v[152:153], v40 offset:43008\n"
    "v_mfma_f32_16x16x32_bf16 v[244:247], v[84:87], v[116:119], v[244:247]\n"
    "global_load_lds_dwordx4 v33, s[42:43] offset:1024\n"
    "ds_read_b128 v[128:131], v37 offset:43008\n"
    "v_mfma_f32_16x16x32_bf16 v[168:171], v[96:99], v[88:91], v[168:171]\n"
    "ds_read_b64_tr_b16 v[154:155], v40 offset:49152\n"
    "v_mfma_f32_16x16x32_bf16 v[184:187], v[100:103], v[88:91], v[184:187]\n"
    "ds_read_b64_tr_b16 v[156:157], v41 offset:43008\n"
    "v_mfma_f32_16x16x32_bf16 v[200:203], v[88:91], v[104:107], v[200:203]\n"
    "global_load_lds_dwordx4 v33, s[42:43] offset:2048\n"
    "s_add_u32 s42, s42, 12288\n"
    "s_addc_u32 s43, s43, 0\n"
    "ds_read_b64_tr_b16 v[158:159], v41 offset:49152\n"
    "v_mfma_f32_16x16x32_bf16 v[216:219], v[88:91], v[108:111], v[216:219]\n"
    "ds_read_b128 v[132:135], v37 offset:44032\n"
    "v_mfma_f32_16x16x32_bf16 v[232:235], v[88:91], v[112:115], v[232:235]\n"
    "v_mfma_f32_16x16x32_bf16 v[248:251], v[88:91], v[116:119], v[248:251]\n"
    "v_mfma_f32_16x16x32_bf16 v[172:175], v[96:99], v[92:95], v[172:175]\n"
    "v_mfma_f32_16x16x32_bf16 v[188:191], v[100:103], v[92:95], v[188:191]\n"
    "v_mfma_f32_16x16x32_bf16 v[204:207], v[92:95], v[104:107], v[204:207]\n"
    "v_mfma_f32_16x16x32_bf16 v[220:223], v[92:95], v[108:111], v[220:223]\n"
    "v_mfma_f32_16x16x32_bf16 v[236:239], v[92:95], v[112:115], v[236:239]\n"
    "s_waitcnt vmcnt(20) lgkmcnt(0)\n"
    "s_barrier\n"
    "v_mfma_f32_16x16x32_bf16 v[252:255], v[92:95], v[116:119], v[252:255]\n"
    "v_mfma_f32_16x16x32_bf16 v[160:163], v[136:139], v[120:123], v[160:163]\n"
    "s_add_u32 m0, s44, 102400\n"
    "ds_read_b64_tr_b16 v[96:97], v38 offset:0\n"
    "v_mfma_f32_16x16x32_bf16 v[176:179], v[140:143], v[120:123], v[176:179]\n"
    "global_load_lds_dwordx4 v32, s[40:41] offset:0 sc1\n"
    "ds_read_b64_tr_b16 v[98:99], v38 offset:6144\n"
    "v_mfma_f32_16x16x32_bf16 v[192:195], v[120:123], v[144:147], v[192:195]\n"
    "ds_read_b64_tr_b16 v[100:101], v39 offset:0\n"
    "v_mfma_f32_16x16x32_bf16 v[208:211], v[120:123], v[148:151], v[208:211]\n"
    "ds_read_b128 v[80:83], v36 offset:0\n"
    "v_mfma_f32_16x16x32_bf16 v[224:227], v[120:123], v[152:155], v[224:227]\n"
    "global_load_lds_dwordx4 v32, s[40:41] offset:1024 sc1\n"
    "s_add_u32 s40, s40, 8192\n"
    "s_addc_u32 s41, s41, 0\n"
    "ds_read_b64_tr_b16 v[102:103], v39 offset:6144\n"
    "v_mfma_f32_16x16x32_bf16 v[240:243], v[120:123], v[156:159], v[240:243]\n"
    "ds_read_b64_tr_b16 v[104:105], v38 offset:1024\n"
    "v_mfma_f32_16x16x32_bf16 v[164:167], v[136:139], v[124:127], v[164:167]\n"
    "s_add_u32 m0, s45, 102400\n"
    "ds_read_b64_tr_b16 v[106:107], v38 offset:7168\n"
    "v_mfma_f32_16x16x32_bf16 v[180:183], v[140:143], v[124:127], v[180:183]\n"
    "global_load_lds_dwordx4 v33, s[42:43] offset:0\n"
    "ds_read_b128 v[84:87], v36 offset:1024\n"
    "v_mfma_f32_16x16x32_bf16 v[196:199], v[124:127], v[144:147], v[196:199]\n"
    "ds_read_b64_tr_b16 v[108:109], v39 offset:1024\n"
    "v_mfma_f32_16x16x32_bf16 v[212:215], v[124:127], v[148:151], v[212:215]\n"
    "ds_read_b64_tr_b16 v[110:111], v39 offset:7168\n"
    "v_mfma_f32_16x16x32_bf16 v[228:231], v[124:127], v[152:155], v[228:231]\n"
    "ds_read_b64_tr_b16 v[112:113], v38 offset:2048\n"
    "v_mfma_f32_16x16x32_bf16 v[244:247], v[124:127], v[156:159], v[244:247]\n"
    "global_load_lds_dwordx4 v33, s[42:43] offset:1024\n"
    "ds_read_b128 v[88:91], v36 offset:2048\n"
    "v_mfma_f32_16x16x32_bf16 v[168:171], v[136:139], v[128:131], v[168:171]\n"
    "ds_read_b64_tr_b16 v[114:115], v38 offset:8192\n"
    "v_mfma_f32_16x16x32_bf16 v[184:187], v[140:143], v[128:131], v[184:187]\n"
    "ds_read_b64_tr_b16 v[116:117], v39 offset:2048\n"
    "v_mfma_f32_16x16x32_bf16 v[200:203], v[128:131], v[144:147], v[200:203]\n"
    "global_load_lds_dwordx4 v33, s[42:43] offset:2048\n"
    "s_add_u32 s42, s42, 12288\n"
    "s_addc_u32 s43, s43, 0\n"
    "ds_read_b64_tr_b16 v[118:119], v39 offset:8192\n"
    "v_mfma_f32_16x16x32_bf16 v[216:219], v[128:131], v[148:151], v[216:219]\n"
    "ds_read_b128 v[92:95], v36 offset:3072\n"
    "v_mfma_f32_16x16x32_bf16 v[232:235], v[128:131], v[152:155], v[232:235]\n"
    "v_mfma_f32_16x16x32_bf16 v[248:251], v[128:131], v[156:159], v[248:251]\n"
    "v_mfma_f32_16x16x32_bf16 v[172:175], v[136:139], v[132:135], v[172:175]\n"
    "v_mfma_f32_16x16x32_bf16 v[188:191], v[140:143], v[132:135], v[188:191]\n"
    "v_mfma_f32_16x16x32_bf16 v[204:207], v[132:135], v[144:147], v[204:207]\n"
    "v_mfma_f32_16x16x32_bf16 v[220:223], v[132:135], v[148:151], v[220:223]\n"
    "v_mfma_f32_16x16x32_bf16 v[236:239], v[132:135], v[152:155], v[236:239]\n"
    "s_waitcnt vmcnt(20) lgkmcnt(0)\n"
    "s_barrier\n"
    "v_mfma_f32_16x16x32_bf16 v[252:255], v[132:135], v[156:159], v[252:255]\n"
    "v_mfma_f32_16x16x32_bf16 v[160:163], v[96:99], v[80:83], v[160:163]\n"
    "s_add_u32 m0, s44, 0\n"
    "ds_read_b64_tr_b16 v[136:137], v38 offset:20480\n"
    "v_mfma_f32_16x16x32_bf16 v[176:179], v[100:103], v[80:83], v[176:179]\n"
    "global_load_lds_dwordx4 v32, s[40:41] offset:0 sc1\n"
    "ds_read_b64_tr_b16 v[138:139], v38 offset:26624\n"
    "v_mfma_f32_16x16x32_bf16 v[192:195], v[80:83], v[104:107], v[192:195]\n"
    "ds_read_b64_tr_b16 v[140:141], v39 offset:20480\n"
    "v_mfma_f32_16x16x32_bf16 v[208:211], v[80:83], v[108:111], v[208:211]\n"
    "ds_read_b128 v[120:123], v36 offset:20480\n"
    "v_mfma_f32_16x16x32_bf16 v[224:227], v[80:83], v[112:115], v[224:227]\n"
    "global_load_lds_dwordx4 v32, s[40:41] offset:1024 sc1\n"
    "s_add_u32 s40, s40, 8192\n"
    "s_addc_u32 s41, s41, 0\n"
    "ds_read_b64_tr_b16 v[142:143], v39 offset:26624\n"
    "v_mfma_f32_16x16x32_bf16 v[240:243], v[80:83], v[116:119], v[240:243]\n"
    "ds_read_b64_tr_b16 v[144:145], v38 offset:21504\n"
    "v_mfma_f32_16x16x32_bf16 v[164:167], v[96:99], v[84:87], v[164:167]\n"
    "s_add_u32 m0, s45, 0\n"
    "ds_read_b64_tr_b16 v[146:147], v38 offset:27648\n"
    "v_mfma_f32_16x16x32_bf16 v[180:183], v[100:103], v[84:87], v[180:183]\n"
    "global_load_lds_dwordx4 v33, s[42:43] offset:0\n"
    "ds_read_b128 v[124:127], v36 offset:21504\n"
    "v_mfma_f32_16x16x32_bf16 v[196:199], v[84:87], v[104:107], v[196:199]\n"
    "ds_read_b64_tr_b16 v[148:149], v39 offset:21504\n"
    "v_mfma_f32_16x16x32_bf16 v[212:215], v[84:87], v[108:111], v[212:215]\n"
    "ds_read_b64_tr_b16 v[150:151], v39 offset:27648\n"
    "v_mfma_f32_16x16x32_bf16 v[228:231], v[84:87], v[112:115], v[228:231]\n"
    "ds_read_b64_tr_b16 v[152:153], v38 offset:22528\n"
    "v_mfma_f32_16x16x32_bf16 v[244:247], v[84:87], v[116:119], v[244:247]\n"
    "global_load_lds_dwordx4 v33, s[42:43] offset:1024\n"
    "ds_read_b128 v[128:131], v36 offset:22528\n"
    "v_mfma_f32_16x16x32_bf16 v[168:171], v[96:99], v[88:91], v[168:171]\n"
    "ds_read_b64_tr_b16 v[154:155], v38 offset:28672\n"
    "v_mfma_f32_16x16x32_bf16 v[184:187], v[100:103], v[88:91], v[184:187]\n"
    "ds_read_b64_tr_b16 v[156:157], v39 offset:22528\n"
    "v_mfma_f32_16x16x32_bf16 v[200:203], v[88:91], v[104:107], v[200:203]\n"
    "global_load_lds_dwordx4 v33, s[42:43] offset:2048\n"
    "s_add_u32 s42, s42, 12288\n"
    "s_addc_u32 s43, s43, 0\n"
    "ds_read_b64_tr_b16 v[158:159], v39 offset:28672\n"
    "v_mfma_f32_16x16x32_bf16 v[216:219], v[88:91], v[108:111], v[216:219]\n"
    "ds_read_b128 v[132:135], v36 offset:23552\n"
    "v_mfma_f32_16x16x32_bf16 v[232:235], v[88:91], v[112:115], v[232:235]\n"
    "v_mfma_f32_16x16x32_bf16 v[248:251], v[88:91], v[116:119], v[248:251]\n"
    "v_mfma_f32_16x16x32_bf16 v[172:175], v[96:99], v[92:95], v[172:175]\n"
    "v_mfma_f32_16x16x32_bf16 v[188:191], v[100:103], v[92:95], v[188:191]\n"
    "v_mfma_f32_16x16x32_bf16 v[204:207], v[92:95], v[104:107], v[204:207]\n"
    "v_mfma_f32_16x16x32_bf16 v[220:223], v[92:95], v[108:111], v[220:223]\n"
    "v_mfma_f32_16x16x32_bf16 v[236:239], v[92:95], v[112:115], v[236:239]\n"
    "s_waitcnt vmcnt(20) lgkmcnt(0)\n"
    "s_barrier\n"
    "v_mfma_f32_16x16x32_bf16 v[252:255], v[92:95], v[116:119], v[252:255]\n"
    "v_mfma_f32_16x16x32_bf16 v[160:163], v[136:139], v[120:123], v[160:163]\n"
    "s_add_u32 m0, s44, 20480\n"
    "ds_read_b64_tr_b16 v[96:97], v38 offset:40960\n"
    "v_mfma_f32_16x16x32_bf16 v[176:179], v[140:143], v[120:123], v[176:179]\n"
    "global_load_lds_dwordx4 v32, s[40:41] offset:0 sc1\n"
    "ds_read_b64_tr_b16 v[98:99], v38 offset:47104\n"
    "v_mfma_f32_16x16x32_bf16 v[192:195], v[120:123], v[144:147], v[192:195]\n"
    "ds_read_b64_tr_b16 v[100:101], v39 offset:40960\n"
    "v_mfma_f32_16x16x32_bf16 v[208:211], v[120:123], v[148:151], v[208:211]\n"
    "ds_read_b128 v[80:83], v36 offset:40960\n"
    "v_mfma_f32_16x16x32_bf16 v[224:227], v[120:123], v[152:155], v[224:227]\n"
    "global_load_lds_dwordx4 v32, s[40:41] offset:1024 sc1\n"
    "s_add_u32 s40, s40, 8192\n"
    "s_addc_u32 s41, s41, 0\n"
    "ds_read_b64_tr_b16 v[102:103], v39 offset:47104\n"
    "v_mfma_f32_16x16x32_bf16 v[240:243], v[120:123], v[156:159], v[240:243]\n"
    "ds_read_b64_tr_b16 v[104:105], v38 offset:41984\n"
    "v_mfma_f32_16x16x32_bf16 v[164:167], v[136:139], v[124:127], v[164:167]\n"
    "s_add_u32 m0, s45, 20480\n"
    "ds_read_b64_tr_b16 v[106:107], v38 offset:48128\n"
    "v_mfma_f32_16x16x32_bf16 v[180:183], v[140:143], v[124:127], v[180:183]\n"
    "global_load_lds_dwordx4 v33, s[42:43] offset:0\n"
    "ds_read_b128 v[84:87], v36 offset:41984\n"
    "v_mfma_f32_16x16x32_bf16 v[196:199], v[124:127], v[144:147], v[196:199]\n"
    "ds_read_b64_tr_b16 v[108:109], v39 offset:41984\n"
    "v_mfma_f32_16x16x32_bf16 v[212:215], v[124:127], v[148:151], v[212:215]\n"
    "ds_read_b64_tr_b16 v[110:111], v39 offset:48128\n"
    "v_mfma_f32_16x16x32_bf16 v[228:231], v[124:127], v[152:155], v[228:231]\n"
    "ds_read_b64_tr_b16 v[112:113], v38 offset:43008\n"
    "v_mfma_f32_16x16x32_bf16 v[244:247], v[124:127], v[156:159], v[244:247]\n"
    "global_load_lds_dwordx4 v33, s[42:43] offset:1024\n"
    "ds_read_b128 v[88:91], v36 offset:43008\n"
    "v_mfma_f32_16x16x32_bf16 v[168:171], v[136:139], v[128:131], v[168:171]\n"
    "ds_read_b64_tr_b16 v[114:115], v38 offset:49152\n"
    "v_mfma_f32_16x16x32_bf16 v[184:187], v[140:143], v[128:131], v[184:187]\n"
    "ds_read_b64_tr_b16 v[116:117], v39 offset:43008\n"
    "v_mfma_f32_16x16x32_bf16 v[200:203], v[128:131], v[144:147], v[200:203]\n"
    "global_load_lds_dwordx4 v33, s[42:43] offset:2048\n"
    "s_add_u32 s42, s42, 12288\n"
    "s_addc_u32 s43, s43, 0\n"
    "ds_read_b64_tr_b16 v[118:119], v39 offset:49152\n"
    "v_mfma_f32_16x16x32_bf16 v[216:219], v[128:131], v[148:151], v[216:219]\n"
    "ds_read_b128 v[92:95], v36 offset:44032\n"
    "v_mfma_f32_16x16x32_bf16 v[232:235], v[128:131], v[152:155], v[232:235]\n"
    "v_mfma_f32_16x16x32_bf16 v[248:251], v[128:131], v[156:159], v[248:251]\n"
    "v_mfma_f32_16x16x32_bf16 v[172:175], v[136:139], v[132:135], v[172:175]\n"
    "v_mfma_f32_16x16x32_bf16 v[188:191], v[140:143], v[132:135], v[188:191]\n"
    "v_mfma_f32_16x16x32_bf16 v[204:207], v[132:135], v[144:147], v[204:207]\n"
    "v_mfma_f32_16x16x32_bf16 v[220:223], v[132:135], v[148:151], v[220:223]\n"
    "v_mfma_f32_16x16x32_bf16 v[236:239], v[132:135], v[152:155], v[236:239]\n"
    "s_waitcnt vmcnt(20) lgkmcnt(0)\n"
    "s_barrier\n"
    "v_mfma_f32_16x16x32_bf16 v[252:255], v[132:135], v[156:159], v[252:255]\n"
    "v_mfma_f32_16x16x32_bf16 v[160:163], v[96:99], v[80:83], v[160:163]\n"
    "ds_read_b64_tr_b16 v[136:137], v40 offset:0\n"
    "v_mfma_f32_16x16x32_bf16 v[176:179], v[100:103], v[80:83], v[176:179]\n"
    "global_load_dwordx2 v[48:49], v42, s[48:49] sc1\n"
    "s_add_u32 s48, s48, 16384\n"
    "s_addc_u32 s49, s49, 0\n"
    "ds_read_b64_tr_b16 v[138:139], v40 offset:6144\n"
    "v_mfma_f32_16x16x32_bf16 v[192:195], v[80:83], v[104:107], v[192:195]\n"
    "global_load_dwordx2 v[50:51], v42, s[48:49] sc1\n"
    "s_add_u32 s48, s48, 16384\n"
    "s_addc_u32 s49, s49, 0\n"
    "ds_read_b64_tr_b16 v[140:141], v41 offset:0\n"
    "v_mfma_f32_16x16x32_bf16 v[208:211], v[80:83], v[108:111], v[208:211]\n"
    "global_load_dwordx2 v[52:53], v42, s[48:49] sc1\n"
    "s_add_u32 s48, s48, 16384\n"
    "s_addc_u32 s49, s49, 0\n"
    "ds_read_b128 v[120:123], v37 offset:0\n"
    "v_mfma_f32_16x16x32_bf16 v[224:227], v[80:83], v[112:115], v[224:227]\n"
    "ds_read_b64_tr_b16 v[142:143], v41 offset:6144\n"
    "v_mfma_f32_16x16x32_bf16 v[240:243], v[80:83], v[116:119], v[240:243]\n"
    "global_load_dwordx2 v[54:55], v42, s[48:49] sc1\n"
    "s_add_u32 s48, s48, 16384\n"
    "s_addc_u32 s49, s49, 0\n"
    "ds_read_b64_tr_b16 v[144:145], v40 offset:1024\n"
    "v_mfma_f32_16x16x32_bf16 v[164:167], v[96:99], v[84:87], v[164:167]\n"
    "global_load_dwordx2 v[56:57], v42, s[48:49] sc1\n"
    "s_add_u32 s48, s48, 16384\n"
    "s_addc_u32 s49, s49, 0\n"
    "ds_read_b64_tr_b16 v[146:147], v40 offset:7168\n"
    "v_mfma_f32_16x16x32_bf16 v[180:183], v[100:103], v[84:87], v[180:183]\n"
    "ds_read_b128 v[124:127], v37 offset:1024\n"
    "v_mfma_f32_16x16x32_bf16 v[196:199], v[84:87], v[104:107], v[196:199]\n"
    "global_load_dwordx2 v[58:59], v42, s[48:49] sc1\n"
    "s_add_u32 s48, s48, 16384\n"
    "s_addc_u32 s49, s49, 0\n"
    "ds_read_b64_tr_b16 v[148:149], v41 offset:1024\n"
    "v_mfma_f32_16x16x32_bf16 v[212:215], v[84:87], v[108:111], v[212:215]\n"
    "global_load_dwordx2 v[60:61], v42, s[48:49] sc1\n"
    "s_add_u32 s48, s48, 16384\n"
    "s_addc_u32 s49, s49, 0\n"
    "ds_read_b64_tr_b16 v[150:151], v41 offset:7168\n"
    "v_mfma_f32_16x16x32_bf16 v[228:231], v[84:87], v[112:115], v[228:231]\n"
    "global_load_dwordx2 v[62:63], v42, s[48:49] sc1\n"
    "ds_read_b64_tr_b16 v[152:153], v40 offset:2048\n"
    "v_mfma_f32_16x16x32_bf16 v[244:247], v[84:87], v[116:119], v[244:247]\n"
    "ds_read_b128 v[128:131], v37 offset:2048\n"
    "v_mfma_f32_16x16x32_bf16 v[168:171], v[96:99], v[88:91], v[168:171]\n"
    "global_load_dword v64, v43, s[50:51] sc1\n"
    "s_add_u32 s50, s50, 12288\n"
    "s_addc_u32 s51, s51, 0\n"
    "ds_read_b64_tr_b16 v[154:155], v40 offset:8192\n"
    "v_mfma_f32_16x16x32_bf16 v[184:187], v[100:103], v[88:91], v[184:187]\n"
    "global_load_dword v65, v43, s[50:51] sc1\n"
    "s_add_u32 s50, s50, 12288\n"
    "s_addc_u32 s51, s51, 0\n"
    "ds_read_b64_tr_b16 v[156:157], v41 offset:2048\n"
    "v_mfma_f32_16x16x32_bf16 v[200:203], v[88:91], v[104:107], v[200:203]\n"
    "ds_read_b64_tr_b16 v[158:159], v41 offset:8192\n"
    "v_mfma_f32_16x16x32_bf16 v[216:219], v[88:91], v[108:111], v[216:219]\n"
    "global_load_dword v66, v43, s[50:51] sc1\n"
    "s_add_u32 s50, s50, 12288\n"
    "s_addc_u32 s51, s51, 0\n"
    "ds_read_b128 v[132:135], v37 offset:3072\n"
    "v_mfma_f32_16x16x32_bf16 v[232:235], v[88:91], v[112:115], v[232:235]\n"
    "global_load_dword v67, v43, s[50:51] sc1\n"
    "s_add_u32 s50, s50, 12288\n"
    "s_addc_u32 s51, s51, 0\n"
    "v_mfma_f32_16x16x32_bf16 v[248:251], v[88:91], v[116:119], v[248:251]\n"
    "v_mfma_f32_16x16x32_bf16 v[172:175], v[96:99], v[92:95], v[172:175]\n"
    "v_mfma_f32_16x16x32_bf16 v[188:191], v[100:103], v[92:95], v[188:191]\n"
    "v_mfma_f32_16x16x32_bf16 v[204:207], v[92:95], v[104:107], v[204:207]\n"
    "v_mfma_f32_16x16x32_bf16 v[220:223], v[92:95], v[108:111], v[220:223]\n"
    "v_mfma_f32_16x16x32_bf16 v[236:239], v[92:95], v[112:115], v[236:239]\n"
    "s_waitcnt vmcnt(27) lgkmcnt(0)\n"
    "s_barrier\n"
    "v_mfma_f32_16x16x32_bf16 v[252:255], v[92:95], v[116:119], v[252:255]\n"
    "v_mfma_f32_16x16x32_bf16 v[160:163], v[136:139], v[120:123], v[160:163]\n"
    "ds_read_b64_tr_b16 v[96:97], v40 offset:20480\n"
    "v_mfma_f32_16x16x32_bf16 v[176:179], v[140:143], v[120:123], v[176:179]\n"
    "global_load_dword v68, v43, s[50:51] sc1\n"
    "s_add_u32 s50, s50, 12288\n"
    "s_addc_u32 s51, s51, 0\n"
    "ds_read_b64_tr_b16 v[98:99], v40 offset:26624\n"
    "v_mfma_f32_16x16x32_bf16 v[192:195], v[120:123], v[144:147], v[192:195]\n"
    "global_load_dword v69, v43, s[50:51] sc1\n"
    "s_add_u32 s50, s50, 12288\n"
    "s_addc_u32 s51, s51, 0\n"
    "ds_read_b64_tr_b16 v[100:101], v41 offset:20480\n"
    "v_mfma_f32_16x16x32_bf16 v[208:211], v[120:123], v[148:151], v[208:211]\n"
    "global_load_dword v70, v43, s[50:51] sc1\n"
    "s_add_u32 s50, s50, 12288\n"
    "s_addc_u32 s51, s51, 0\n"
    "ds_read_b128 v[80:83], v37 offset:20480\n"
    "v_mfma_f32_16x16x32_bf16 v[224:227], v[120:123], v[152:155], v[224:227]\n"
    "global_load_dword v71, v43, s[50:51] sc1\n"
    "s_add_u32 s50, s50, 12288\n"
    "s_addc_u32 s51, s51, 0\n"
    "ds_read_b64_tr_b16 v[102:103], v41 offset:26624\n"
    "v_mfma_f32_16x16x32_bf16 v[240:243], v[120:123], v[156:159], v[240:243]\n"
    "global_load_dword v72, v43, s[50:51] sc1\n"
    "s_add_u32 s50, s50, 12288\n"
    "s_addc_u32 s51, s51, 0\n"
    "ds_read_b64_tr_b16 v[104:105], v40 offset:21504\n"
    "v_mfma_f32_16x16x32_bf16 v[164:167], v[136:139], v[124:127], v[164:167]\n"
    "global_load_dword v73, v43, s[50:51] sc1\n"
    "s_add_u32 s50, s50, 12288\n"
    "s_addc_u32 s51, s51, 0\n"
    "ds_read_b64_tr_b16 v[106:107], v40 offset:27648\n"
    "v_mfma_f32_16x16x32_bf16 v[180:183], v[140:143], v[124:127], v[180:183]\n"
    "global_load_dword v74, v43, s[50:51] sc1\n"
    "s_add_u32 s50, s50, 12288\n"
    "s_addc_u32 s51, s51, 0\n"
    "ds_read_b128 v[84:87], v37 offset:21504\n"
    "v_mfma_f32_16x16x32_bf16 v[196:199], v[124:127], v[144:147], v[196:199]\n"
    "global_load_dword v75, v43, s[50:51] sc1\n"
    "s_add_u32 s50, s50, 12288\n"
    "s_addc_u32 s51, s51, 0\n"
    "ds_read_b64_tr_b16 v[108:109], v41 offset:21504\n"
    "v_mfma_f32_16x16x32_bf16 v[212:215], v[124:127], v[148:151], v[212:215]\n"
    "ds_read_b64_tr_b16 v[110:111], v41 offset:27648\n"
    "v_mfma_f32_16x16x32_bf16 v[228:231], v[124:127], v[152:155], v[228:231]\n"
    "global_load_dword v76, v43, s[50:51] sc1\n"
    "s_add_u32 s50, s50, 12288\n"
    "s_addc_u32 s51, s51, 0\n"
    "ds_read_b64_tr_b16 v[112:113], v40 offset:22528\n"
    "v_mfma_f32_16x16x32_bf16 v[244:247], v[124:127], v[156:159], v[244:247]\n"
    "global_load_dword v77, v43, s[50:51] sc1\n"
    "s_add_u32 s50, s50, 12288\n"
    "s_addc_u32 s51, s51, 0\n"
    "ds_read_b128 v[88:91], v37 offset:22528\n"
    "v_mfma_f32_16x16x32_bf16 v[168:171], v[136:139], v[128:131], v[168:171]\n"
    "global_load_dword v78, v43, s[50:51] sc1\n"
    "s_add_u32 s50, s50, 12288\n"
    "s_addc_u32 s51, s51, 0\n"
    "ds_read_b64_tr_b16 v[114:115], v40 offset:28672\n"
    "v_mfma_f32_16x16x32_bf16 v[184:187], v[140:143], v[128:131], v[184:187]\n"
    "global_load_dword v79, v43, s[50:51] sc1\n"
    "s_getpc_b64 s[70:71]\n"
    "2:\n"
    "v_min_u32 v15, lin_end-2b-4, v46\n"
    "s_mov_b32 m0, s47\n"
    "ds_read_b64_tr_b16 v[116:117], v41 offset:22528\n"
    "v_mfma_f32_16x16x32_bf16 v[200:203], v[128:131], v[144:147], v[200:203]\n"
    "global_load_lds_dword v15, s[70:71]\n"
    "ds_read_b64_tr_b16 v[118:119], v41 offset:28672\n"
    "v_mfma_f32_16x16x32_bf16 v[216:219], v[128:131], v[148:151], v[216:219]\n"
    "global_load_lds_dword v47, s[72:73]\n"
    "ds_read_b128 v[92:95], v37 offset:23552\n"
    "v_mfma_f32_16x16x32_bf16 v[232:235], v[128:131], v[152:155], v[232:235]\n"
    "global_load_lds_dword v34, s[74:75]\n"
    "v_mfma_f32_16x16x32_bf16 v[248:251], v[128:131], v[156:159], v[248:251]\n"
    "v_mfma_f32_16x16x32_bf16 v[172:175], v[136:139], v[132:135], v[172:175]\n"
    "v_mfma_f32_16x16x32_bf16 v[188:191], v[140:143], v[132:135], v[188:191]\n"
    "v_mfma_f32_16x16x32_bf16 v[204:207], v[132:135], v[144:147], v[204:207]\n"
    "v_mfma_f32_16x16x32_bf16 v[220:223], v[132:135], v[148:151], v[220:223]\n"
    "v_mfma_f32_16x16x32_bf16 v[236:239], v[132:135], v[152:155], v[236:239]\n"
    "s_waitcnt vmcnt(37) lgkmcnt(0)\n"
    "s_barrier\n"
    "v_mfma_f32_16x16x32_bf16 v[252:255], v[132:135], v[156:159], v[252:255]\n"
    "v_mfma_f32_16x16x32_bf16 v[160:163], v[96:99], v[80:83], v[160:163]\n"
    "ds_read_b64_tr_b16 v[136:137], v40 offset:40960\n"
    "v_mfma_f32_16x16x32_bf16 v[176:179], v[100:103], v[80:83], v[176:179]\n"
    "ds_read_b64_tr_b16 v[138:139], v40 offset:47104\n"
    "v_mfma_f32_16x16x32_bf16 v[192:195], v[80:83], v[104:107], v[192:195]\n"
    "ds_read_b64_tr_b16 v[140:141], v41 offset:40960\n"
    "v_mfma_f32_16x16x32_bf16 v[208:211], v[80:83], v[108:111], v[208:211]\n"
    "ds_read_b128 v[120:123], v37 offset:40960\n"
    "v_mfma_f32_16x16x32_bf16 v[224:227], v[80:83], v[112:115], v[224:227]\n"
    "ds_read_b64_tr_b16 v[142:143], v41 offset:47104\n"
    "v_mfma_f32_16x16x32_bf16 v[240:243], v[80:83], v[116:119], v[240:243]\n"
    "ds_read_b64_tr_b16 v[144:145], v40 offset:41984\n"
    "v_mfma_f32_16x16x32_bf16 v[164:167], v[96:99], v[84:87], v[164:167]\n"
    "ds_read_b64_tr_b16 v[146:147], v40 offset:48128\n"
    "v_mfma_f32_16x16x32_bf16 v[180:183], v[100:103], v[84:87], v[180:183]\n"
    "ds_read_b128 v[124:127], v37 offset:41984\n"
    "v_mfma_f32_16x16x32_bf16 v[196:199], v[84:87], v[104:107], v[196:199]\n"
    "ds_read_b64_tr_b16 v[148:149], v41 offset:41984\n"
    "v_mfma_f32_16x16x32_bf16 v[212:215], v[84:87], v[108:111], v[212:215]\n"
    "ds_read_b64_tr_b16 v[150:151], v41 offset:48128\n"
    "v_mfma_f32_16x16x32_bf16 v[228:231], v[84:87], v[112:115], v[228:231]\n"
    "ds_read_b64_tr_b16 v[152:153], v40 offset:43008\n"
    "v_mfma_f32_16x16x32_bf16 v[244:247], v[84:87], v[116:119], v[244:247]\n"
    "ds_read_b128 v[128:131], v37 offset:43008\n"
    "v_mfma_f32_16x16x32_bf16 v[168:171], v[96:99], v[88:91], v[168:171]\n"
    "ds_read_b64_tr_b16 v[154:155], v40 offset:49152\n"
    "v_mfma_f32_16x16x32_bf16 v[184:187], v[100:103], v[88:91], v[184:187]\n"
    "ds_read_b64_tr_b16 v[156:157], v41 offset:43008\n"
    "v_mfma_f32_16x16x32_bf16 v[200:203], v[88:91], v[104:107], v[200:203]\n"
    "ds_read_b64_tr_b16 v[158:159], v41 offset:49152\n"
    "v_mfma_f32_16x16x32_bf16 v[216:219], v[88:91], v[108:111], v[216:219]\n"
    "ds_read_b128 v[132:135], v37 offset:44032\n"
    "v_mfma_f32_16x16x32_bf16 v[232:235], v[88:91], v[112:115], v[232:235]\n"
    "v_mfma_f32_16x16x32_bf16 v[248:251], v[88:91], v[116:119], v[248:251]\n"
    "v_mfma_f32_16x16x32_bf16 v[172:175], v[96:99], v[92:95], v[172:175]\n"
    "v_mfma_f32_16x16x32_bf16 v[188:191], v[100:103], v[92:95], v[188:191]\n"
    "v_mfma_f32_16x16x32_bf16 v[204:207], v[92:95], v[104:107], v[204:207]\n"
    "v_mfma_f32_16x16x32_bf16 v[220:223], v[92:95], v[108:111], v[220:223]\n"
    "v_mfma_f32_16x16x32_bf16 v[236:239], v[92:95], v[112:115], v[236:239]\n"
    "s_waitcnt vmcnt(32) lgkmcnt(0)\n"
    "s_barrier\n"
    "v_mfma_f32_16x16x32_bf16 v[252:255], v[92:95], v[116:119], v[252:255]\n"
    "v_mfma_f32_16x16x32_bf16 v[160:163], v[136:139], v[120:123], v[160:163]\n"
    "ds_read_b64_tr_b16 v[96:97], v38 offset:0\n"
    "v_mfma_f32_16x16x32_bf16 v[176:179], v[140:143], v[120:123], v[176:179]\n"
    "ds_read_b64_tr_b16 v[98:99], v38 offset:6144\n"
    "v_mfma_f32_16x16x32_bf16 v[192:195], v[120:123], v[144:147], v[192:195]\n"
    "ds_read_b64_tr_b16 v[100:101], v39 offset:0\n"
    "v_mfma_f32_16x16x32_bf16 v[208:211], v[120:123], v[148:151], v[208:211]\n"
    "ds_read_b128 v[80:83], v36 offset:0\n"
    "v_mfma_f32_16x16x32_bf16 v[224:227], v[120:123], v[152:155], v[224:227]\n"
    "ds_read_b64_tr_b16 v[102:103], v39 offset:6144\n"
    "v_mfma_f32_16x16x32_bf16 v[240:243], v[120:123], v[156:159], v[240:243]\n"
    "ds_read_b64_tr_b16 v[104:105], v38 offset:1024\n"
    "v_mfma_f32_16x16x32_bf16 v[164:167], v[136:139], v[124:127], v[164:167]\n"
    "ds_read_b64_tr_b16 v[106:107], v38 offset:7168\n"
    "v_mfma_f32_16x16x32_bf16 v[180:183], v[140:143], v[124:127], v[180:183]\n"
    "ds_read_b128 v[84:87], v36 offset:1024\n"
    "v_mfma_f32_16x16x32_bf16 v[196:199], v[124:127], v[144:147], v[196:199]\n"
    "ds_read_b64_tr_b16 v[108:109], v39 offset:1024\n"
    "v_mfma_f32_16x16x32_bf16 v[212:215], v[124:127], v[148:151], v[212:215]\n"
    "ds_read_b64_tr_b16 v[110:111], v39 offset:7168\n"
    "v_mfma_f32_16x16x32_bf16 v[228:231], v[124:127], v[152:155], v[228:231]\n"
    "ds_read_b64_tr_b16 v[112:113], v38 offset:2048\n"
    "v_mfma_f32_16x16x32_bf16 v[244:247], v[124:127], v[156:159], v[244:247]\n"
    "ds_read_b128 v[88:91], v36 offset:2048\n"
    "v_mfma_f32_16x16x32_bf16 v[168:171], v[136:139], v[128:131], v[168:171]\n"
    "ds_read_b64_tr_b16 v[114:115], v38 offset:8192\n"
    "v_mfma_f32_16x16x32_bf16 v[184:187], v[140:143], v[128:131], v[184:187]\n"
    "ds_read_b64_tr_b16 v[116:117], v39 offset:2048\n"
    "v_mfma_f32_16x16x32_bf16 v[200:203], v[128:131], v[144:147], v[200:203]\n"
    "ds_read_b64_tr_b16 v[118:119], v39 offset:8192\n"
    "v_mfma_f32_16x16x32_bf16 v[216:219], v[128:131], v[148:151], v[216:219]\n"
    "ds_read_b128 v[92:95], v36 offset:3072\n"
    "v_mfma_f32_16x16x32_bf16 v[232:235], v[128:131], v[152:155], v[232:235]\n"
    "v_mfma_f32_16x16x32_bf16 v[248:251], v[128:131], v[156:159], v[248:251]\n"
    "v_mfma_f32_16x16x32_bf16 v[172:175], v[136:139], v[132:135], v[172:175]\n"
    "v_mfma_f32_16x16x32_bf16 v[188:191], v[140:143], v[132:135], v[188:191]\n"
    "v_mfma_f32_16x16x32_bf16 v[204:207], v[132:135], v[144:147], v[204:207]\n"
    "v_mfma_f32_16x16x32_bf16 v[220:223], v[132:135], v[148:151], v[220:223]\n"
    "v_mfma_f32_16x16x32_bf16 v[236:239], v[132:135], v[152:155], v[236:239]\n"
    "s_waitcnt vmcnt(27) lgkmcnt(0)\n"
    "s_barrier\n"
    "v_mfma_f32_16x16x32_bf16 v[252:255], v[132:135], v[156:159], v[252:255]\n"
    "v_mfma_f32_16x16x32_bf16 v[160:163], v[96:99], v[80:83], v[160:163]\n"
    "ds_read_b64_tr_b16 v[136:137], v38 offset:20480\n"
    "v_mfma_f32_16x16x32_bf16 v[176:179], v[100:103], v[80:83], v[176:179]\n"
    "ds_read_b64_tr_b16 v[138:139], v38 offset:26624\n"
    "v_mfma_f32_16x16x32_bf16 v[192:195], v[80:83], v[104:107], v[192:195]\n"
    "ds_read_b64_tr_b16 v[140:141], v39 offset:20480\n"
    "v_mfma_f32_16x16x32_bf16 v[208:211], v[80:83], v[108:111], v[208:211]\n"
    "ds_read_b128 v[120:123], v36 offset:20480\n"
    "v_mfma_f32_16x16x32_bf16 v[224:227], v[80:83], v[112:115], v[224:227]\n"
    "ds_read_b64_tr_b16 v[142:143], v39 offset:26624\n"
    "v_mfma_f32_16x16x32_bf16 v[240:243], v[80:83], v[116:119], v[240:243]\n"
    "ds_read_b64_tr_b16 v[144:145], v38 offset:21504\n"
    "v_mfma_f32_16x16x32_bf16 v[164:167], v[96:99], v[84:87], v[164:167]\n"
    "ds_read_b64_tr_b16 v[146:147], v38 offset:27648\n"
    "v_mfma_f32_16x16x32_bf16 v[180:183], v[100:103], v[84:87], v[180:183]\n"
    "ds_read_b128 v[124:127], v36 offset:21504\n"
    "v_mfma_f32_16x16x32_bf16 v[196:199], v[84:87], v[104:107], v[196:199]\n"
    "ds_read_b64_tr_b16 v[148:149], v39 offset:21504\n"
    "v_mfma_f32_16x16x32_bf16 v[212:215], v[84:87], v[108:111], v[212:215]\n"
    "ds_read_b64_tr_b16 v[150:151], v39 offset:27648\n"
    "v_mfma_f32_16x16x32_bf16 v[228:231], v[84:87], v[112:115], v[228:231]\n"
    "ds_read_b64_tr_b16 v[152:153], v38 offset:22528\n"
    "v_mfma_f32_16x16x32_bf16 v[244:247], v[84:87], v[116:119], v[244:247]\n"
    "ds_read_b128 v[128:131], v36 offset:22528\n"
    "v_mfma_f32_16x16x32_bf16 v[168:171], v[96:99], v[88:91], v[168:171]\n"
    "ds_read_b64_tr_b16 v[154:155], v38 offset:28672\n"
    "v_mfma_f32_16x16x32_bf16 v[184:187], v[100:103], v[88:91], v[184:187]\n"
    "ds_read_b64_tr_b16 v[156:157], v39 offset:22528\n"
    "v_mfma_f32_16x16x32_bf16 v[200:203], v[88:91], v[104:107], v[200:203]\n"
    "ds_read_b64_tr_b16 v[158:159], v39 offset:28672\n"
    "v_mfma_f32_16x16x32_bf16 v[216:219], v[88:91], v[108:111], v[216:219]\n"
    "ds_read_b128 v[132:135], v36 offset:23552\n"
    "v_mfma_f32_16x16x32_bf16 v[232:235], v[88:91], v[112:115], v[232:235]\n"
    "v_mfma_f32_16x16x32_bf16 v[248:251], v[88:91], v[116:119], v[248:251]\n"
    "v_mfma_f32_16x16x32_bf16 v[172:175], v[96:99], v[92:95], v[172:175]\n"
    "v_mfma_f32_16x16x32_bf16 v[188:191], v[100:103], v[92:95], v[188:191]\n"
    "v_mfma_f32_16x16x32_bf16 v[204:207], v[92:95], v[104:107], v[204:207]\n"
    "v_mfma_f32_16x16x32_bf16 v[220:223], v[92:95], v[108:111], v[220:223]\n"
    "v_mfma_f32_16x16x32_bf16 v[236:239], v[92:95], v[112:115], v[236:239]\n"
    "s_waitcnt lgkmcnt(0)\n"
    "v_mfma_f32_16x16x32_bf16 v[252:255], v[92:95], v[116:119], v[252:255]\n"
    "v_mfma_f32_16x16x32_bf16 v[160:163], v[136:139], v[120:123], v[160:163]\n"
    "v_mfma_f32_16x16x32_bf16 v[176:179], v[140:143], v[120:123], v[176:179]\n"
    "v_mfma_f32_16x16x32_bf16 v[192:195], v[120:123], v[144:147], v[192:195]\n"
    "v_mfma_f32_16x16x32_bf16 v[208:211], v[120:123], v[148:151], v[208:211]\n"
    "v_mfma_f32_16x16x32_bf16 v[224:227], v[120:123], v[152:155], v[224:227]\n"
    "v_mfma_f32_16x16x32_bf16 v[240:243], v[120:123], v[156:159], v[240:243]\n"
    "v_mfma_f32_16x16x32_bf16 v[164:167], v[136:139], v[124:127], v[164:167]\n"
    "v_mfma_f32_16x16x32_bf16 v[180:183], v[140:143], v[124:127], v[180:183]\n"
    "v_mfma_f32_16x16x32_bf16 v[196:199], v[124:127], v[144:147], v[196:199]\n"
    "v_mfma_f32_16x16x32_bf16 v[212:215], v[124:127], v[148:151], v[212:215]\n"
    "v_mfma_f32_16x16x32_bf16 v[228:231], v[124:127], v[152:155], v[228:231]\n"
    "v_mfma_f32_16x16x32_bf16 v[244:247], v[124:127], v[156:159], v[244:247]\n"
    "v_mfma_f32_16x16x32_bf16 v[168:171], v[136:139], v[128:131], v[168:171]\n"
    "v_mfma_f32_16x16x32_bf16 v[184:187], v[140:143], v[128:131], v[184:187]\n"
    "v_mfma_f32_16x16x32_bf16 v[200:203], v[128:131], v[144:147], v[200:203]\n"
    "v_mfma_f32_16x16x32_bf16 v[216:219], v[128:131], v[148:151], v[216:219]\n"
    "v_mfma_f32_16x16x32_bf16 v[232:235], v[128:131], v[152:155], v[232:235]\n"
    "v_mfma_f32_16x16x32_bf16 v[248:251], v[128:131], v[156:159], v[248:251]\n"
    "v_mfma_f32_16x16x32_bf16 v[172:175], v[136:139], v[132:135], v[172:175]\n"
    "v_mfma_f32_16x16x32_bf16 v[188:191], v[140:143], v[132:135], v[188:191]\n"
    "v_mfma_f32_16x16x32_bf16 v[204:207], v[132:135], v[144:147], v[204:207]\n"
    "v_mfma_f32_16x16x32_bf16 v[220:223], v[132:135], v[148:151], v[220:223]\n"
    "v_mfma_f32_16x16x32_bf16 v[236:239], v[132:135], v[152:155], v[236:239]\n"
    "v_mfma_f32_16x16x32_bf16 v[252:255], v[132:135], v[156:159], v[252:255]\n"
    "s_waitcnt vmcnt(3)\n"
    "s_nop 7\n"
    :
      "=&{v[160:191]}"(aq), "=&{v[192:223]}"(ak), "=&{v[224:255]}"(av), "=&{v[48:63]}"(stt), "=&{v[64:79]}"(cp),
      "+{s[40:41]}"(pA), "+{s[42:43]}"(pB), "+{s[48:49]}"(pS), "+{s[50:51]}"(pC)
    : "{v32}"(voffA), "{v33}"(voffB), "{v36}"(dsA), "{v37}"(dsA + 61440),
      "{v38}"(dsB0), "{v39}"(dsB1), "{v40}"(dsB0 + 61440), "{v41}"(dsB1 + 61440), "{v42}"(voffS), "{v43}"(voffC),
      "{v46}"(tid * 128), "{v47}"(layer ? 0 : min(bx * 256 + tid, 57343u) * 128), "{s44}"(m0A), "{s45}"(m0B), "{s47}"(lb + L_SCR), "{s[72:73]}"(A.wg),
      "{v34}"(layer ? 0 : min((bx * 256 + tid) >> 1, 28671u) * 128), "{s[74:75]}"(A.wu)
    : "memory", "scc", "v15", "v80", "v81", "v82", "v83", "v84", "v85", "v86", "v87", "v88", "v89", "v90", "v91", "v92", "v93", "v94", "v95", "v96", "v97", "v98", "v99", "v100", "v101", "v102", "v103", "v104", "v105", "v106", "v107", "v108", "v109", "v110", "v111", "v112", "v113", "v114", "v115", "v116", "v117", "v118", "v119", "v120", "v121", "v122", "v123", "v124", "v125", "v126", "v127", "v128", "v129", "v130", "v131", "v132", "v133", "v134", "v135", "v136", "v137", "v138", "v139", "v140", "v141", "v142", "v143", "v144", "v145", "v146", "v147", "v148", "v149", "v150", "v151", "v152", "v153", "v154", "v155", "v156", "v157", "v158", "v159", "s46", "s70", "s71");
  {
  LANE_IDS
  {
    float sm = 0.f;
#pragma unroll
    for (int t = 0; t < 8; ++t) sm += stt[2 * t];
    const float mean = sum_x32(sm) * (1.f / 16.f);
    float m2 = 0.f;
#pragma unroll
    for (int t = 0; t < 8; ++t) { const float d = stt[2 * t] - mean; m2 += stt[2 * t + 1] + 64.f * d * d; }
    m2 = sum_x32(m2);
    const float rstd = __builtin_amdgcn_rsqf(m2 * (1.f / 1024.f) + 1e-5f);
    ((LAS float*)(lds + L_TAB))[(lane >> 5) * 128 + 32 * w + (lane & 31)] = lane < 32 ? mean : rstd;
    float sc = 0.f;
#pragma unroll
    for (int t = 0; t < 16; ++t) sc += cp[t];
    ((LAS float*)(lds + L_TAB))[256 + tid] = sc;
  }
  wg_barrier();
  float cs[12];
  {
    const LAS float* tc = (const LAS float*)(lds + L_TAB) + 256 + wn * 32;
    const f32x4 c0 = *(const LAS f32x4*)(tc + 4 * g), c1 = *(const LAS f32x4*)(tc + 16 + 4 * g);
#pragma unroll
    for (int j = 0; j < 4; ++j) { cs[j] = c0[j]; cs[4 + j] = c1[j]; cs[8 + j] = tc[64 + (j >> 1) * 64 + (j & 1) * 16 + r]; }
  }
  const LAS float* tm = (const LAS float*)(lds + L_TAB); const LAS float* tr = tm + 128;
  f32x4 kv[2][2] = {}, zz[2] = {};
  unsigned one2 = 0x3f803f80u;
  asm volatile("" : "+v"(one2));
  const bf16x8 ones = __builtin_bit_cast(bf16x8, (u32x4){one2, one2, one2, one2});
#pragma unroll
  for (int a = 0; a < 2; ++a) {
    u32x4 kf[2], vf[2];
#pragma unroll
    for (int h = 0; h < 2; ++h) {
      const int m = 2 * a + h;
      const f32x4 mean = *(const LAS f32x4*)(tm + 64 * wm + 16 * m + 4 * g), rstd = *(const LAS f32x4*)(tr + 64 * wm + 16 * m + 4 * g);
#pragma unroll
      for (int f = 0; f < 2; ++f) {
        float k4[4], v4[4];
#pragma unroll
        for (int j = 0; j < 4; ++j) {
          k4[j] = phi((ak[(f * 4 + m) * 4 + j] - mean[j] * cs[8 + f]) * rstd[j]);
          v4[j] = (av[(f * 4 + m) * 4 + j] - mean[j] * cs[10 + f]) * rstd[j];
        }
        kf[f][2 * h] = pk_bf16(k4[0], k4[1]); kf[f][2 * h + 1] = pk_bf16(k4[2], k4[3]);
        vf[f][2 * h] = pk_bf16(v4[0], v4[1]); vf[f][2 * h + 1] = pk_bf16(v4[2], v4[3]);
      }
    }
#pragma unroll
    for (int f = 0; f < 2; ++f) {
#pragma unroll
      for (int e = 0; e < 2; ++e) kv[f][e] = __builtin_amdgcn_mfma_f32_16x16x32_bf16(__builtin_bit_cast(bf16x8, kf[f]), __builtin_bit_cast(bf16x8, vf[e]), kv[f][e], 0, 0, 0);
      zz[f] = __builtin_amdgcn_mfma_f32_16x16x32_bf16(__builtin_bit_cast(bf16x8, kf[f]), ones, zz[f], 0, 0, 0);
    }
  }
  LAS float* rec = (LAS float*)(lds + L_KV) + (wn * 2 + wm) * KVREC;
#pragma unroll
  for (int e = 0; e < 2; ++e) { *(LAS f32x4*)(rec + e * 512 + lane * 4) = kv[0][e]; *(LAS f32x4*)(rec + e * 512 + 256 + lane * 4) = kv[1][e]; }
  if (r == 0) { *(LAS f32x4*)(rec + 1024 + g * 8) = zz[0]; *(LAS f32x4*)(rec + 1024 + g * 8 + 4) = zz[1]; }
  wg_barrier();
  const rsrc_t rw = make_rsrc(ws, WS_END);
  const unsigned kp = WS_KP + (layer * 256 + pn * 16) * 8448;
  const LAS f32x4* red = (const LAS f32x4*)(lds + L_KV);
#pragma unroll
  for (int k = 0; k < 3; ++k) {
    const int i = min(tid + 256 * k, 527), o = i + (i >= 264 ? 264 : 0);
    if (k < 2 || tid < 16) st16(rw, kp + pm * 8448 + i * 16, __builtin_bit_cast(u32x4, red[o] + red[o + 264]));
  }
  __builtin_amdgcn_sched_barrier(0);
#pragma unroll
  for (int m = 0; m < 4; ++m) {
    const float mean = tm[64 * wm + 16 * m + r], rstd = tr[64 * wm + 16 * m + r];
    float o[8];
#pragma unroll
    for (int j = 0; j < 8; ++j) o[j] = phi((aq[((j >> 2) * 4 + m) * 4 + (j & 3)] - mean * cs[j]) * rstd);
    st16(rw, WS_PQ + layer * XB + ((pm * 32 + hd) * 8 + 4 * wm + m) * 1024 + lane * 16, (u32x4){pk_bf16(o[0], o[1]), pk_bf16(o[2], o[3]), pk_bf16(o[4], o[5]), pk_bf16(o[6], o[7])});
  }
  {
    const int un = 16 * pm + r;
    const unsigned src = kp + (tid >> 4) * 8448;
    const unsigned uo = (un >> 7) * (KVREC * 4) + ((un >> 6) & 1) * 2048 + (un & 63) * 16, zo = (r >> 3) * (KVREC * 4) + 4096 + (r & 7) * 16;
    u32x4 pv[3];
    fetch(rw, pv, [&](int i) { return src + (i == 0 ? uo : i == 1 ? uo + 1024 : zo); });
    drain();
    f32x4 a0 = __builtin_bit_cast(f32x4, pv[0]), a1 = __builtin_bit_cast(f32x4, pv[1]), az = __builtin_bit_cast(f32x4, pv[2]);
#pragma unroll
    for (int j = 0; j < 4; ++j) { a0[j] = sum_x32(sum_x16(a0[j])); a1[j] = sum_x32(sum_x16(a1[j])); az[j] = sum_x32(sum_x16(az[j])); }
    LAS f32x4* fin = (LAS f32x4*)lds;
    fin[(w * 4 + g) * 16 + r] = g == 0 ? a0 : g == 1 ? a1 : az;
    wg_barrier();
    if (tid < 48) {
      const f32x4 v = (fin[tid] + fin[64 + tid]) + (fin[128 + tid] + fin[192 + tid]);
      const u32x2 pkd = {pk_bf16(v[0], v[1]), pk_bf16(v[2], v[3])};
      if (tid < 32) st8(rw, WS_KB + layer * 65536 + pn * 4096 + un * 16 + g * 8, pkd);
      else if (pm == 0) st8(rw, WS_ZB + layer * 2048 + pn * 128 + r * 8, pkd);
    }
  }
  if (w == 1 && lane < 4) set_word(ws, flag(layer, FL_Q + pm * 4 + lane, pn));
  stage_attn(A, lds);
  }
}

DI void phase_attn(const Args& A, LAS char* lds, int layer) {
  int w = A.w; unsigned bx = blockIdx.x; char* ws = A.ws;
  asm volatile("" : "+s"(w), "+s"(bx), "+s"(ws));
  LANE_IDS
  const unsigned lb = (unsigned)(size_t)lds;
  const int xcd = bx & 7, bi = bx >> 3, pm = 8 * (xcd & 3) + (bi & 7), pn = 4 * (xcd >> 2) + (bi >> 3);
  const int gs = w >> 1, wl = w & 1;
  const char* pK = ws + WS_KB + layer * 65536 + 32768;
  const char* pA = ws + WS_PQ + layer * XB + ((pm >> 1) * 32 + gs) * 8192 + (pm & 1) * 4096;
  const char* pB = (const char*)A.wo + gs * 65536 + pn * 256 + 5 * 131072;
  const char* pX = ws + WS_XT + layer * 2 * XB + ((pm >> 1) * 32 + 4 * pn + 2 * gs) * 8192 + (pm & 1) * 4096;
  const unsigned rb = lb + gs * 61440;
  const unsigned voffA = lane * 16 + wl * 2048;
  const unsigned voffB = attn_voffB(lane, wl);
  const unsigned sw = 2 * (g & 1) + (q >> 1), rowo = rb + 4096 + (4 * g + q) * 128 + p * 8;
  const unsigned bk = rowo + gs * 4096, bs = rowo + (gs ^ 1) * 4096;
  f32x32 keep, send; f32x16 ex;
  {
    const rsrc_t rw = make_rsrc(ws, WS_END);
    u32x4 kv[9];
#pragma unroll 1
    for (int spin = 0; spin < (1 << 16); ++spin) {
      const unsigned f = __builtin_amdgcn_raw_buffer_load_b32(rw, w ? WS_CS : flag(layer, FL_Q + (pm >> 1) * 4 + (pn & 3), r), 0, 16);
#pragma unroll
      for (int i = 0; i < 9; ++i)
        kv[i] = __builtin_amdgcn_raw_buffer_load_b128(rw, (i < 8 ? WS_KB + layer * 65536 + w * 8192 + i * 1024 : WS_ZB + layer * 2048 + wl * 1024) + lane * 16, 0, 16);
      bool ok = (w != 0) | (f == 1u);
#pragma unroll
      for (int i = 0; i < 9; ++i) ok &= filled(kv[i]);
      if (__all(ok)) break;
      asm volatile("s_sleep 2" ::: "memory");
    }
    asm volatile("s_barrier" ::: "memory");
    asm volatile(
    "s_nop 4\n"
    "s_add_u32 m0, s44, 49152\n"
    "s_nop 0\n"
    "global_load_lds_dwordx4 v32, s[40:41] offset:0 sc1\n"
    "global_load_lds_dwordx4 v32, s[40:41] offset:1024 sc1\n"
    "s_add_u32 s40, s40, 16384\n"
    "s_addc_u32 s41, s41, 0\n"
    "s_add_u32 m0, s44, 0\n"
    "s_nop 0\n"
    "global_load_lds_dwordx4 v32, s[40:41] offset:0 sc1\n"
    "global_load_lds_dwordx4 v32, s[40:41] offset:1024 sc1\n"
    "s_add_u32 s40, s40, 16384\n"
    "s_addc_u32 s41, s41, 0\n"
    "s_add_u32 m0, s44, 12288\n"
    "s_nop 0\n"
    "global_load_lds_dwordx4 v32, s[40:41] offset:0 sc1\n"
    "global_load_lds_dwordx4 v32, s[40:41] offset:1024 sc1\n"
    "s_add_u32 s40, s40, 16384\n"
    "s_addc_u32 s41, s41, 0\n"
    "s_add_u32 m0, s44, 24576\n"
    "s_nop 0\n"
    "global_load_lds_dwordx4 v32, s[40:41] offset:0 sc1\n"
    "global_load_lds_dwordx4 v32, s[40:41] offset:1024 sc1\n"
    "s_add_u32 s40, s40, 16384\n"
    "s_addc_u32 s41, s41, 0\n"
    "s_add_u32 m0, s44, 36864\n"
    "s_nop 0\n"
    "global_load_lds_dwordx4 v32, s[40:41] offset:0 sc1\n"
    "global_load_lds_dwordx4 v32, s[40:41] offset:1024 sc1\n"
    "s_add_u32 s40, s40, 16384\n"
    "s_addc_u32 s41, s41, 0\n"
      : "+{s[40:41]}"(pA) : "{v32}"(voffA), "{s44}"(rb + wl * 2048) : "memory", "scc");
#pragma unroll
    for (int i = 0; i < 9; ++i) *(LAS u32x4*)(lds + (i < 8 ? L_KV + w * 8192 + i * 1024 : L_Z + wl * 1024) + lane * 16) = kv[i];
  }
  asm volatile(
    "s_nop 4\n"
    "s_mov_b32 s57, 0x10000\n"
    "global_load_dwordx4 v[8:11], v46, s[52:53] offset:0 sc1\n"
    "global_load_dwordx4 v[12:15], v46, s[52:53] offset:1024 sc1\n"
    "global_load_dwordx4 v[16:19], v46, s[52:53] offset:2048 sc1\n"
    "global_load_dwordx4 v[20:23], v46, s[52:53] offset:3072 sc1\n"
    "v_mov_b32 v4, 0x358637bd\n"
    "v_mov_b32 v5, 0x358637bd\n"
    "v_mov_b32 v6, 0x358637bd\n"
    "v_mov_b32 v7, 0x358637bd\n"
    "s_waitcnt vmcnt(10) lgkmcnt(0)\n"
    "s_barrier\n"
    "ds_read_b128 v[120:123], v45 offset:0\n"
    "ds_read_b128 v[112:115], v44 offset:0\n"
    "ds_read_b128 v[116:119], v44 offset:1024\n"
    "ds_read_b128 v[124:127], v34 offset:49152\n"
    "ds_read_b128 v[128:131], v34 offset:50176\n"
    "ds_read_b128 v[140:143], v45 offset:128\n"
    "ds_read_b128 v[132:135], v44 offset:4096\n"
    "ds_read_b128 v[136:139], v44 offset:5120\n"
    "ds_read_b128 v[144:147], v34 offset:0\n"
    "ds_read_b128 v[148:151], v34 offset:1024\n"
    "ds_read_b64_tr_b16 v[48:49], v36 offset:0\n"
    "ds_read_b64_tr_b16 v[50:51], v36 offset:2048\n"
    "ds_read_b64_tr_b16 v[52:53], v37 offset:0\n"
    "ds_read_b64_tr_b16 v[54:55], v37 offset:2048\n"
    "ds_read_b64_tr_b16 v[56:57], v38 offset:0\n"
    "s_waitcnt lgkmcnt(10)\n"
    "v_mfma_f32_16x16x32_bf16 v[160:163], v[120:123], v[124:127], v[4:7]\n"
    "v_mfma_f32_16x16x32_bf16 v[172:175], v[120:123], v[128:131], v[4:7]\n"
    "v_mfma_f32_16x16x32_bf16 v[152:155], v[112:115], v[124:127], 0\n"
    "v_mfma_f32_16x16x32_bf16 v[156:159], v[116:119], v[124:127], 0\n"
    "v_mfma_f32_16x16x32_bf16 v[164:167], v[112:115], v[128:131], 0\n"
    "v_mfma_f32_16x16x32_bf16 v[168:171], v[116:119], v[128:131], 0\n"
    "ds_read_b64_tr_b16 v[58:59], v38 offset:2048\n"
    "ds_read_b64_tr_b16 v[60:61], v39 offset:0\n"
    "ds_read_b64_tr_b16 v[62:63], v39 offset:2048\n"
    "ds_read_b64_tr_b16 v[64:65], v40 offset:0\n"
    "ds_read_b64_tr_b16 v[66:67], v40 offset:2048\n"
    "ds_read_b64_tr_b16 v[68:69], v41 offset:0\n"
    "ds_read_b64_tr_b16 v[70:71], v41 offset:2048\n"
    "ds_read_b64_tr_b16 v[72:73], v42 offset:0\n"
    "ds_read_b64_tr_b16 v[74:75], v42 offset:2048\n"
    "ds_read_b64_tr_b16 v[76:77], v43 offset:0\n"
    "ds_read_b64_tr_b16 v[78:79], v43 offset:2048\n"
    "v_rcp_f32 v24, v160\n"
    "v_rcp_f32 v25, v172\n"
    "v_mul_f32 v152, v24, v152\n"
    "v_mul_f32 v153, v24, v153\n"
    "v_mul_f32 v154, v24, v154\n"
    "v_mul_f32 v155, v24, v155\n"
    "v_mul_f32 v156, v24, v156\n"
    "v_mul_f32 v157, v24, v157\n"
    "v_mul_f32 v158, v24, v158\n"
    "v_mul_f32 v159, v24, v159\n"
    "v_cvt_pk_bf16_f32 v176, v152, v153\n"
    "v_cvt_pk_bf16_f32 v177, v154, v155\n"
    "v_cvt_pk_bf16_f32 v178, v156, v157\n"
    "v_cvt_pk_bf16_f32 v179, v158, v159\n"
    "v_mul_f32 v164, v25, v164\n"
    "v_mul_f32 v165, v25, v165\n"
    "v_mul_f32 v166, v25, v166\n"
    "v_mul_f32 v167, v25, v167\n"
    "v_mul_f32 v168, v25, v168\n"
    "v_mul_f32 v169, v25, v169\n"
    "v_mul_f32 v170, v25, v170\n"
    "v_mul_f32 v171, v25, v171\n"
    "v_cvt_pk_bf16_f32 v180, v164, v165\n"
    "v_cvt_pk_bf16_f32 v181, v166, v167\n"
    "v_cvt_pk_bf16_f32 v182, v168, v169\n"
    "v_cvt_pk_bf16_f32 v183, v170, v171\n"
    "s_waitcnt vmcnt(8) lgkmcnt(0)\n"
    "s_barrier\n"
    "v_mfma_f32_16x16x32_bf16 v[160:163], v[140:143], v[144:147], v[4:7]\n"
    "s_add_u32 m0, s44, 49152\n"
    "ds_read_b128 v[120:123], v45 offset:256\n"
    "ds_read_b128 v[112:115], v44 offset:8192\n"
    "v_mfma_f32_16x16x32_bf16 v[172:175], v[140:143], v[148:151], v[4:7]\n"
    "global_load_lds_dwordx4 v32, s[40:41] offset:0 sc1\n"
    "ds_read_b128 v[116:119], v44 offset:9216\n"
    "v_mfma_f32_16x16x32_bf16 v[152:155], v[132:135], v[144:147], 0\n"
    "ds_read_b128 v[124:127], v34 offset:12288\n"
    "ds_read_b128 v[128:131], v34 offset:13312\n"
    "v_mfma_f32_16x16x32_bf16 v[156:159], v[136:139], v[144:147], 0\n"
    "global_load_lds_dwordx4 v32, s[40:41] offset:1024 sc1\n"
    "s_add_u32 s40, s40, 16384\n"
    "s_addc_u32 s41, s41, 0\n"
    "ds_read_b64_tr_b16 v[80:81], v36 offset:12288\n"
    "v_mfma_f32_16x16x32_bf16 v[164:167], v[132:135], v[148:151], 0\n"
    "s_add_u32 m0, s44, 0\n"
    "ds_read_b64_tr_b16 v[82:83], v36 offset:14336\n"
    "ds_read_b64_tr_b16 v[84:85], v37 offset:12288\n"
    "v_mfma_f32_16x16x32_bf16 v[168:171], v[136:139], v[148:151], 0\n"
    "global_load_lds_dwordx4 v32, s[40:41] offset:0 sc1\n"
    "ds_read_b64_tr_b16 v[86:87], v37 offset:14336\n"
    "v_rcp_f32 v24, v160\n"
    "v_mfma_f32_16x16x32_bf16 v[192:195], v[48:51], v[176:179], 0\n"
    "ds_read_b64_tr_b16 v[88:89], v38 offset:12288\n"
    "ds_read_b64_tr_b16 v[90:91], v38 offset:14336\n"
    "v_rcp_f32 v25, v172\n"
    "v_mfma_f32_16x16x32_bf16 v[200:203], v[52:55], v[176:179], 0\n"
    "global_load_lds_dwordx4 v32, s[40:41] offset:1024 sc1\n"
    "s_add_u32 s40, s40, 16384\n"
    "s_addc_u32 s41, s41, 0\n"
    "ds_read_b64_tr_b16 v[92:93], v39 offset:12288\n"
    "v_mfma_f32_16x16x32_bf16 v[208:211], v[56:59], v[176:179], 0\n"
    "ds_read_b64_tr_b16 v[94:95], v39 offset:14336\n"
    "ds_read_b64_tr_b16 v[96:97], v40 offset:12288\n"
    "v_mul_f32 v152, v24, v152\n"
    "v_mul_f32 v153, v24, v153\n"
    "v_mfma_f32_16x16x32_bf16 v[216:219], v[60:63], v[176:179], 0\n"
    "s_add_u32 m0, s45, 0\n"
    "ds_read_b64_tr_b16 v[98:99], v40 offset:14336\n"
    "v_mul_f32 v154, v24, v154\n"
    "v_mul_f32 v155, v24, v155\n"
    "v_mfma_f32_16x16x32_bf16 v[224:227], v[64:67], v[176:179], 0\n"
    "global_load_lds_dwordx4 v33, s[42:43] offset:0\n"
    "ds_read_b64_tr_b16 v[100:101], v41 offset:12288\n"
    "ds_read_b64_tr_b16 v[102:103], v41 offset:14336\n"
    "v_mul_f32 v156, v24, v156\n"
    "v_mul_f32 v157, v24, v157\n"
    "v_mfma_f32_16x16x32_bf16 v[232:235], v[68:71], v[176:179], 0\n"
    "ds_read_b64_tr_b16 v[104:105], v42 offset:12288\n"
    "v_mul_f32 v158, v24, v158\n"
    "v_mfma_f32_16x16x32_bf16 v[240:243], v[72:75], v[176:179], 0\n"
    "global_load_lds_dwordx4 v27, s[42:43] offset:1024\n"
    "ds_read_b64_tr_b16 v[106:107], v42 offset:14336\n"
    "ds_read_b64_tr_b16 v[108:109], v43 offset:12288\n"
    "v_mul_f32 v159, v24, v159\n"
    "v_cvt_pk_bf16_f32 v184, v152, v153\n"
    "v_mfma_f32_16x16x32_bf16 v[248:251], v[76:79], v[176:179], 0\n"
    "ds_read_b64_tr_b16 v[110:111], v43 offset:14336\n"
    "v_cvt_pk_bf16_f32 v185, v154, v155\n"
    "v_cvt_pk_bf16_f32 v186, v156, v157\n"
    "v_mfma_f32_16x16x32_bf16 v[196:199], v[48:51], v[180:183], 0\n"
    "global_load_lds_dwordx4 v29, s[42:43] offset:2048\n"
    "v_cvt_pk_bf16_f32 v187, v158, v159\n"
    "v_mfma_f32_16x16x32_bf16 v[204:207], v[52:55], v[180:183], 0\n"
    "v_mul_f32 v164, v25, v164\n"
    "v_mul_f32 v165, v25, v165\n"
    "v_mfma_f32_16x16x32_bf16 v[212:215], v[56:59], v[180:183], 0\n"
    "global_load_lds_dwordx4 v3, s[42:43] offset:3072\n"
    "s_add_u32 s42, s42, 131072\n"
    "s_addc_u32 s43, s43, 0\n"
    "v_mul_f32 v166, v25, v166\n"
    "v_mul_f32 v167, v25, v167\n"
    "v_mfma_f32_16x16x32_bf16 v[220:223], v[60:63], v[180:183], 0\n"
    "v_mul_f32 v168, v25, v168\n"
    "v_mul_f32 v169, v25, v169\n"
    "v_mfma_f32_16x16x32_bf16 v[228:231], v[64:67], v[180:183], 0\n"
    "v_mul_f32 v170, v25, v170\n"
    "v_mfma_f32_16x16x32_bf16 v[236:239], v[68:71], v[180:183], 0\n"
    "v_mul_f32 v171, v25, v171\n"
    "v_cvt_pk_bf16_f32 v188, v164, v165\n"
    "s_waitcnt vmcnt(14) lgkmcnt(0)\n"
    "s_barrier\n"
    "v_mfma_f32_16x16x32_bf16 v[244:247], v[72:75], v[180:183], 0\n"
    "v_cvt_pk_bf16_f32 v189, v166, v167\n"
    "v_cvt_pk_bf16_f32 v190, v168, v169\n"
    "v_mfma_f32_16x16x32_bf16 v[252:255], v[76:79], v[180:183], 0\n"
    "v_cvt_pk_bf16_f32 v191, v170, v171\n"
    "v_mfma_f32_16x16x32_bf16 v[160:163], v[120:123], v[124:127], v[4:7]\n"
    "s_add_u32 m0, s44, 12288\n"
    "ds_read_b128 v[140:143], v45 offset:384\n"
    "ds_read_b128 v[132:135], v44 offset:12288\n"
    "v_mfma_f32_16x16x32_bf16 v[172:175], v[120:123], v[128:131], v[4:7]\n"
    "global_load_lds_dwordx4 v32, s[40:41] offset:0 sc1\n"
    "ds_read_b128 v[136:139], v44 offset:13312\n"
    "v_mfma_f32_16x16x32_bf16 v[152:155], v[112:115], v[124:127], 0\n"
    "ds_read_b128 v[144:147], v34 offset:24576\n"
    "ds_read_b128 v[148:151], v34 offset:25600\n"
    "v_mfma_f32_16x16x32_bf16 v[156:159], v[116:119], v[124:127], 0\n"
    "ds_read_b64_tr_b16 v[48:49], v36 offset:24576\n"
    "v_mfma_f32_16x16x32_bf16 v[164:167], v[112:115], v[128:131], 0\n"
    "global_load_lds_dwordx4 v32, s[40:41] offset:1024 sc1\n"
    "s_add_u32 s40, s40, 16384\n"
    "s_addc_u32 s41, s41, 0\n"
    "ds_read_b64_tr_b16 v[50:51], v36 offset:26624\n"
    "ds_read_b64_tr_b16 v[52:53], v37 offset:24576\n"
    "v_mfma_f32_16x16x32_bf16 v[168:171], v[116:119], v[128:131], 0\n"
    "ds_read_b64_tr_b16 v[54:55], v37 offset:26624\n"
    "v_rcp_f32 v24, v160\n"
    "v_mfma_f32_16x16x32_bf16 v[192:195], v[80:83], v[184:187], v[192:195]\n"
    "s_add_u32 m0, s45, 12288\n"
    "ds_read_b64_tr_b16 v[56:57], v38 offset:24576\n"
    "ds_read_b64_tr_b16 v[58:59], v38 offset:26624\n"
    "v_rcp_f32 v25, v172\n"
    "v_mfma_f32_16x16x32_bf16 v[200:203], v[84:87], v[184:187], v[200:203]\n"
    "global_load_lds_dwordx4 v33, s[42:43] offset:0\n"
    "ds_read_b64_tr_b16 v[60:61], v39 offset:24576\n"
    "v_mfma_f32_16x16x32_bf16 v[208:211], v[88:91], v[184:187], v[208:211]\n"
    "ds_read_b64_tr_b16 v[62:63], v39 offset:26624\n"
    "ds_read_b64_tr_b16 v[64:65], v40 offset:24576\n"
    "v_mul_f32 v152, v24, v152\n"
    "v_mul_f32 v153, v24, v153\n"
    "v_mfma_f32_16x16x32_bf16 v[216:219], v[92:95], v[184:187], v[216:219]\n"
    "ds_read_b64_tr_b16 v[66:67], v40 offset:26624\n"
    "v_mul_f32 v154, v24, v154\n"
    "v_mul_f32 v155, v24, v155\n"
    "v_mfma_f32_16x16x32_bf16 v[224:227], v[96:99], v[184:187], v[224:227]\n"
    "global_load_lds_dwordx4 v27, s[42:43] offset:1024\n"
    "ds_read_b64_tr_b16 v[68:69], v41 offset:24576\n"
    "ds_read_b64_tr_b16 v[70:71], v41 offset:26624\n"
    "v_mul_f32 v156, v24, v156\n"
    "v_mul_f32 v157, v24, v157\n"
    "v_mfma_f32_16x16x32_bf16 v[232:235], v[100:103], v[184:187], v[232:235]\n"
    "ds_read_b64_tr_b16 v[72:73], v42 offset:24576\n"
    "v_mul_f32 v158, v24, v158\n"
    "v_mfma_f32_16x16x32_bf16 v[240:243], v[104:107], v[184:187], v[240:243]\n"
    "ds_read_b64_tr_b16 v[74:75], v42 offset:26624\n"
    "ds_read_b64_tr_b16 v[76:77], v43 offset:24576\n"
    "v_mul_f32 v159, v24, v159\n"
    "v_cvt_pk_bf16_f32 v176, v152, v153\n"
    "v_mfma_f32_16x16x32_bf16 v[248:251], v[108:111], v[184:187], v[248:251]\n"
    "global_load_lds_dwordx4 v29, s[42:43] offset:2048\n"
    "ds_read_b64_tr_b16 v[78:79], v43 offset:26624\n"
    "v_cvt_pk_bf16_f32 v177, v154, v155\n"
    "v_cvt_pk_bf16_f32 v178, v156, v157\n"
    "v_mfma_f32_16x16x32_bf16 v[196:199], v[80:83], v[188:191], v[196:199]\n"
    "v_cvt_pk_bf16_f32 v179, v158, v159\n"
    "v_mfma_f32_16x16x32_bf16 v[204:207], v[84:87], v[188:191], v[204:207]\n"
    "v_mul_f32 v164, v25, v164\n"
    "v_mul_f32 v165, v25, v165\n"
    "v_mfma_f32_16x16x32_bf16 v[212:215], v[88:91], v[188:191], v[212:215]\n"
    "global_load_lds_dwordx4 v3, s[42:43] offset:3072\n"
    "s_add_u32 s42, s42, 131072\n"
    "s_addc_u32 s43, s43, 0\n"
    "v_mul_f32 v166, v25, v166\n"
    "v_mul_f32 v167, v25, v167\n"
    "v_mfma_f32_16x16x32_bf16 v[220:223], v[92:95], v[188:191], v[220:223]\n"
    "v_mul_f32 v168, v25, v168\n"
    "v_mul_f32 v169, v25, v169\n"
    "v_mfma_f32_16x16x32_bf16 v[228:231], v[96:99], v[188:191], v[228:231]\n"
    "v_mul_f32 v170, v25, v170\n"
    "v_mfma_f32_16x16x32_bf16 v[236:239], v[100:103], v[188:191], v[236:239]\n"
    "v_mul_f32 v171, v25, v171\n"
    "v_cvt_pk_bf16_f32 v180, v164, v165\n"
    "s_waitcnt vmcnt(18) lgkmcnt(0)\n"
    "s_barrier\n"
    "v_mfma_f32_16x16x32_bf16 v[244:247], v[104:107], v[188:191], v[244:247]\n"
    "v_cvt_pk_bf16_f32 v181, v166, v167\n"
    "v_cvt_pk_bf16_f32 v182, v168, v169\n"
    "v_mfma_f32_16x16x32_bf16 v[252:255], v[108:111], v[188:191], v[252:255]\n"
    "v_cvt_pk_bf16_f32 v183, v170, v171\n"
    "s_waitcnt vmcnt(14)\n"
    "12:\n"
    "v_max3_u32 v24, v8, v9, v10\n"
    "v_max3_u32 v25, v11, v12, v13\n"
    "v_max3_u32 v24, v24, v14, v15\n"
    "v_max3_u32 v25, v25, v16, v17\n"
    "v_max3_u32 v24, v24, v18, v19\n"
    "v_max3_u32 v25, v25, v20, v21\n"
    "v_max3_u32 v24, v24, v22, v23\n"
    "v_max_u32 v24, v24, v25\n"
    "v_cmp_eq_u32 vcc, -1, v24\n"
    "s_and_b64 vcc, exec, vcc\n"
    "s_cbranch_vccnz 22f\n"
    "32:\n"
    "v_mfma_f32_16x16x32_bf16 v[160:163], v[140:143], v[144:147], v[4:7]\n"
    "s_add_u32 m0, s46, 0\n"
    "ds_read_b128 v[120:123], v45 offset:512\n"
    "ds_read_b128 v[112:115], v44 offset:16384\n"
    "v_mfma_f32_16x16x32_bf16 v[172:175], v[140:143], v[148:151], v[4:7]\n"
    "global_load_lds_dwordx4 v46, s[52:53] offset:0 sc1\n"
    "ds_read_b128 v[116:119], v44 offset:17408\n"
    "v_mfma_f32_16x16x32_bf16 v[152:155], v[132:135], v[144:147], 0\n"
    "global_load_lds_dwordx4 v46, s[52:53] offset:1024 sc1\n"
    "ds_read_b128 v[124:127], v34 offset:36864\n"
    "ds_read_b128 v[128:131], v34 offset:37888\n"
    "v_mfma_f32_16x16x32_bf16 v[156:159], v[136:139], v[144:147], 0\n"
    "ds_read_b64_tr_b16 v[80:81], v36 offset:36864\n"
    "v_mfma_f32_16x16x32_bf16 v[164:167], v[132:135], v[148:151], 0\n"
    "global_load_lds_dwordx4 v46, s[52:53] offset:2048 sc1\n"
    "ds_read_b64_tr_b16 v[82:83], v36 offset:38912\n"
    "ds_read_b64_tr_b16 v[84:85], v37 offset:36864\n"
    "v_mfma_f32_16x16x32_bf16 v[168:171], v[136:139], v[148:151], 0\n"
    "ds_read_b64_tr_b16 v[86:87], v37 offset:38912\n"
    "v_rcp_f32 v24, v160\n"
    "v_mfma_f32_16x16x32_bf16 v[192:195], v[48:51], v[176:179], v[192:195]\n"
    "global_load_lds_dwordx4 v46, s[52:53] offset:3072 sc1\n"
    "s_add_u32 s52, s52, 16384\n"
    "s_addc_u32 s53, s53, 0\n"
    "ds_read_b64_tr_b16 v[88:89], v38 offset:36864\n"
    "ds_read_b64_tr_b16 v[90:91], v38 offset:38912\n"
    "v_rcp_f32 v25, v172\n"
    "v_mfma_f32_16x16x32_bf16 v[200:203], v[52:55], v[176:179], v[200:203]\n"
    "s_add_u32 m0, s44, 24576\n"
    "ds_read_b64_tr_b16 v[92:93], v39 offset:36864\n"
    "v_mfma_f32_16x16x32_bf16 v[208:211], v[56:59], v[176:179], v[208:211]\n"
    "global_load_lds_dwordx4 v32, s[40:41] offset:0 sc1\n"
    "ds_read_b64_tr_b16 v[94:95], v39 offset:38912\n"
    "ds_read_b64_tr_b16 v[96:97], v40 offset:36864\n"
    "v_mul_f32 v152, v24, v152\n"
    "v_mul_f32 v153, v24, v153\n"
    "v_mfma_f32_16x16x32_bf16 v[216:219], v[60:63], v[176:179], v[216:219]\n"
    "ds_read_b64_tr_b16 v[98:99], v40 offset:38912\n"
    "v_mul_f32 v154, v24, v154\n"
    "v_mul_f32 v155, v24, v155\n"
    "v_mfma_f32_16x16x32_bf16 v[224:227], v[64:67], v[176:179], v[224:227]\n"
    "global_load_lds_dwordx4 v32, s[40:41] offset:1024 sc1\n"
    "s_add_u32 s40, s40, 16384\n"
    "s_addc_u32 s41, s41, 0\n"
    "s_add_u32 m0, s45, 24576\n"
    "ds_read_b64_tr_b16 v[100:101], v41 offset:36864\n"
    "ds_read_b64_tr_b16 v[102:103], v41 offset:38912\n"
    "v_mul_f32 v156, v24, v156\n"
    "v_mul_f32 v157, v24, v157\n"
    "v_mfma_f32_16x16x32_bf16 v[232:235], v[68:71], v[176:179], v[232:235]\n"
    "global_load_lds_dwordx4 v33, s[42:43] offset:0\n"
    "ds_read_b64_tr_b16 v[104:105], v42 offset:36864\n"
    "v_mul_f32 v158, v24, v158\n"
    "v_mfma_f32_16x16x32_bf16 v[240:243], v[72:75], v[176:179], v[240:243]\n"
    "ds_read_b64_tr_b16 v[106:107], v42 offset:38912\n"
    "ds_read_b64_tr_b16 v[108:109], v43 offset:36864\n"
    "v_mul_f32 v159, v24, v159\n"
    "v_cvt_pk_bf16_f32 v184, v152, v153\n"
    "v_mfma_f32_16x16x32_bf16 v[248:251], v[76:79], v[176:179], v[248:251]\n"
    "global_load_lds_dwordx4 v27, s[42:43] offset:1024\n"
    "ds_read_b64_tr_b16 v[110:111], v43 offset:38912\n"
    "v_cvt_pk_bf16_f32 v185, v154, v155\n"
    "v_cvt_pk_bf16_f32 v186, v156, v157\n"
    "v_mfma_f32_16x16x32_bf16 v[196:199], v[48:51], v[180:183], v[196:199]\n"
    "v_cvt_pk_bf16_f32 v187, v158, v159\n"
    "v_mfma_f32_16x16x32_bf16 v[204:207], v[52:55], v[180:183], v[204:207]\n"
    "global_load_lds_dwordx4 v29, s[42:43] offset:2048\n"
    "v_mul_f32 v164, v25, v164\n"
    "v_mul_f32 v165, v25, v165\n"
    "v_mfma_f32_16x16x32_bf16 v[212:215], v[56:59], v[180:183], v[212:215]\n"
    "v_mul_f32 v166, v25, v166\n"
    "v_mul_f32 v167, v25, v167\n"
    "v_mfma_f32_16x16x32_bf16 v[220:223], v[60:63], v[180:183], v[220:223]\n"
    "global_load_lds_dwordx4 v3, s[42:43] offset:3072\n"
    "s_add_u32 s42, s42, 131072\n"
    "s_addc_u32 s43, s43, 0\n"
    "v_mul_f32 v168, v25, v168\n"
    "v_mul_f32 v169, v25, v169\n"
    "v_mfma_f32_16x16x32_bf16 v[228:231], v[64:67], v[180:183], v[228:231]\n"
    "v_mul_f32 v170, v25, v170\n"
    "v_mfma_f32_16x16x32_bf16 v[236:239], v[68:71], v[180:183], v[236:239]\n"
    "v_mul_f32 v171, v25, v171\n"
    "v_cvt_pk_bf16_f32 v188, v164, v165\n"
    "s_waitcnt vmcnt(22) lgkmcnt(0)\n"
    "s_barrier\n"
    "v_mfma_f32_16x16x32_bf16 v[244:247], v[72:75], v[180:183], v[244:247]\n"
    "v_cvt_pk_bf16_f32 v189, v166, v167\n"
    "v_cvt_pk_bf16_f32 v190, v168, v169\n"
    "v_mfma_f32_16x16x32_bf16 v[252:255], v[76:79], v[180:183], v[252:255]\n"
    "v_cvt_pk_bf16_f32 v191, v170, v171\n"
    "v_mfma_f32_16x16x32_bf16 v[160:163], v[120:123], v[124:127], v[4:7]\n"
    "ds_read_b128 v[140:143], v45 offset:640\n"
    "ds_read_b128 v[132:135], v44 offset:20480\n"
    "v_mfma_f32_16x16x32_bf16 v[172:175], v[120:123], v[128:131], v[4:7]\n"
    "global_load_dwordx4 v[8:11], v46, s[52:53] offset:0 sc1\n"
    "ds_read_b128 v[136:139], v44 offset:21504\n"
    "v_mfma_f32_16x16x32_bf16 v[152:155], v[112:115], v[124:127], 0\n"
    "global_load_dwordx4 v[12:15], v46, s[52:53] offset:1024 sc1\n"
    "ds_read_b128 v[144:147], v34 offset:49152\n"
    "ds_read_b128 v[148:151], v34 offset:50176\n"
    "v_mfma_f32_16x16x32_bf16 v[156:159], v[116:119], v[124:127], 0\n"
    "ds_read_b64_tr_b16 v[48:49], v36 offset:49152\n"
    "v_mfma_f32_16x16x32_bf16 v[164:167], v[112:115], v[128:131], 0\n"
    "global_load_dwordx4 v[16:19], v46, s[52:53] offset:2048 sc1\n"
    "ds_read_b64_tr_b16 v[50:51], v36 offset:51200\n"
    "ds_read_b64_tr_b16 v[52:53], v37 offset:49152\n"
    "v_mfma_f32_16x16x32_bf16 v[168:171], v[116:119], v[128:131], 0\n"
    "ds_read_b64_tr_b16 v[54:55], v37 offset:51200\n"
    "v_rcp_f32 v24, v160\n"
    "v_mfma_f32_16x16x32_bf16 v[192:195], v[80:83], v[184:187], v[192:195]\n"
    "global_load_dwordx4 v[20:23], v46, s[52:53] offset:3072 sc1\n"
    "ds_read_b64_tr_b16 v[56:57], v38 offset:49152\n"
    "ds_read_b64_tr_b16 v[58:59], v38 offset:51200\n"
    "v_rcp_f32 v25, v172\n"
    "v_mfma_f32_16x16x32_bf16 v[200:203], v[84:87], v[184:187], v[200:203]\n"
    "s_add_u32 m0, s44, 36864\n"
    "ds_read_b64_tr_b16 v[60:61], v39 offset:49152\n"
    "v_mfma_f32_16x16x32_bf16 v[208:211], v[88:91], v[184:187], v[208:211]\n"
    "global_load_lds_dwordx4 v32, s[40:41] offset:0 sc1\n"
    "ds_read_b64_tr_b16 v[62:63], v39 offset:51200\n"
    "ds_read_b64_tr_b16 v[64:65], v40 offset:49152\n"
    "v_mul_f32 v152, v24, v152\n"
    "v_mul_f32 v153, v24, v153\n"
    "v_mfma_f32_16x16x32_bf16 v[216:219], v[92:95], v[184:187], v[216:219]\n"
    "ds_read_b64_tr_b16 v[66:67], v40 offset:51200\n"
    "v_mul_f32 v154, v24, v154\n"
    "v_mul_f32 v155, v24, v155\n"
    "v_mfma_f32_16x16x32_bf16 v[224:227], v[96:99], v[184:187], v[224:227]\n"
    "global_load_lds_dwordx4 v32, s[40:41] offset:1024 sc1\n"
    "s_add_u32 s40, s40, 16384\n"
    "s_addc_u32 s41, s41, 0\n"
    "s_add_u32 m0, s45, 36864\n"
    "ds_read_b64_tr_b16 v[68:69], v41 offset:49152\n"
    "ds_read_b64_tr_b16 v[70:71], v41 offset:51200\n"
    "v_mul_f32 v156, v24, v156\n"
    "v_mul_f32 v157, v24, v157\n"
    "v_mfma_f32_16x16x32_bf16 v[232:235], v[100:103], v[184:187], v[232:235]\n"
    "global_load_lds_dwordx4 v33, s[42:43] offset:0\n"
    "ds_read_b64_tr_b16 v[72:73], v42 offset:49152\n"
    "v_mul_f32 v158, v24, v158\n"
    "v_mfma_f32_16x16x32_bf16 v[240:243], v[104:107], v[184:187], v[240:243]\n"
    "ds_read_b64_tr_b16 v[74:75], v42 offset:51200\n"
    "ds_read_b64_tr_b16 v[76:77], v43 offset:49152\n"
    "v_mul_f32 v159, v24, v159\n"
    "v_cvt_pk_bf16_f32 v176, v152, v153\n"
    "v_mfma_f32_16x16x32_bf16 v[248:251], v[108:111], v[184:187], v[248:251]\n"
    "global_load_lds_dwordx4 v27, s[42:43] offset:1024\n"
    "ds_read_b64_tr_b16 v[78:79], v43 offset:51200\n"
    "v_cvt_pk_bf16_f32 v177, v154, v155\n"
    "v_cvt_pk_bf16_f32 v178, v156, v157\n"
    "v_mfma_f32_16x16x32_bf16 v[196:199], v[80:83], v[188:191], v[196:199]\n"
    "v_cvt_pk_bf16_f32 v179, v158, v159\n"
    "v_mfma_f32_16x16x32_bf16 v[204:207], v[84:87], v[188:191], v[204:207]\n"
    "global_load_lds_dwordx4 v29, s[42:43] offset:2048\n"
    "v_mul_f32 v164, v25, v164\n"
    "v_mul_f32 v165, v25, v165\n"
    "v_mfma_f32_16x16x32_bf16 v[212:215], v[88:91], v[188:191], v[212:215]\n"
    "v_mul_f32 v166, v25, v166\n"
    "v_mul_f32 v167, v25, v167\n"
    "v_mfma_f32_16x16x32_bf16 v[220:223], v[92:95], v[188:191], v[220:223]\n"
    "global_load_lds_dwordx4 v3, s[42:43] offset:3072\n"
    "s_add_u32 s42, s42, 131072\n"
    "s_addc_u32 s43, s43, 0\n"
    "v_mul_f32 v168, v25, v168\n"
    "v_mul_f32 v169, v25, v169\n"
    "v_mfma_f32_16x16x32_bf16 v[228:231], v[96:99], v[188:191], v[228:231]\n"
    "v_mul_f32 v170, v25, v170\n"
    "v_mfma_f32_16x16x32_bf16 v[236:239], v[100:103], v[188:191], v[236:239]\n"
    "v_mul_f32 v171, v25, v171\n"
    "v_cvt_pk_bf16_f32 v180, v164, v165\n"
    "s_waitcnt vmcnt(26) lgkmcnt(0)\n"
    "s_barrier\n"
    "v_mfma_f32_16x16x32_bf16 v[244:247], v[104:107], v[188:191], v[244:247]\n"
    "v_cvt_pk_bf16_f32 v181, v166, v167\n"
    "v_cvt_pk_bf16_f32 v182, v168, v169\n"
    "v_mfma_f32_16x16x32_bf16 v[252:255], v[108:111], v[188:191], v[252:255]\n"
    "v_cvt_pk_bf16_f32 v183, v170, v171\n"
    "v_mfma_f32_16x16x32_bf16 v[160:163], v[140:143], v[144:147], v[4:7]\n"
    "s_add_u32 m0, s44, 49152\n"
    "ds_read_b128 v[120:123], v45 offset:768\n"
    "ds_read_b128 v[112:115], v44 offset:24576\n"
    "v_mfma_f32_16x16x32_bf16 v[172:175], v[140:143], v[148:151], v[4:7]\n"
    "global_load_lds_dwordx4 v32, s[40:41] offset:0 sc1\n"
    "ds_read_b128 v[116:119], v44 offset:25600\n"
    "v_mfma_f32_16x16x32_bf16 v[152:155], v[132:135], v[144:147], 0\n"
    "ds_read_b128 v[124:127], v34 offset:0\n"
    "ds_read_b128 v[128:131], v34 offset:1024\n"
    "v_mfma_f32_16x16x32_bf16 v[156:159], v[136:139], v[144:147], 0\n"
    "ds_read_b64_tr_b16 v[80:81], v36 offset:0\n"
    "v_mfma_f32_16x16x32_bf16 v[164:167], v[132:135], v[148:151], 0\n"
    "global_load_lds_dwordx4 v32, s[40:41] offset:1024 sc1\n"
    "s_add_u32 s40, s40, 16384\n"
    "s_addc_u32 s41, s41, 0\n"
    "ds_read_b64_tr_b16 v[82:83], v36 offset:2048\n"
    "ds_read_b64_tr_b16 v[84:85], v37 offset:0\n"
    "v_mfma_f32_16x16x32_bf16 v[168:171], v[136:139], v[148:151], 0\n"
    "ds_read_b64_tr_b16 v[86:87], v37 offset:2048\n"
    "v_rcp_f32 v24, v160\n"
    "v_mfma_f32_16x16x32_bf16 v[192:195], v[48:51], v[176:179], v[192:195]\n"
    "s_add_u32 m0, s45, 49152\n"
    "ds_read_b64_tr_b16 v[88:89], v38 offset:0\n"
    "ds_read_b64_tr_b16 v[90:91], v38 offset:2048\n"
    "v_rcp_f32 v25, v172\n"
    "v_mfma_f32_16x16x32_bf16 v[200:203], v[52:55], v[176:179], v[200:203]\n"
    "global_load_lds_dwordx4 v33, s[42:43] offset:0\n"
    "ds_read_b64_tr_b16 v[92:93], v39 offset:0\n"
    "v_mfma_f32_16x16x32_bf16 v[208:211], v[56:59], v[176:179], v[208:211]\n"
    "ds_read_b64_tr_b16 v[94:95], v39 offset:2048\n"
    "ds_read_b64_tr_b16 v[96:97], v40 offset:0\n"
    "v_mul_f32 v152, v24, v152\n"
    "v_mul_f32 v153, v24, v153\n"
    "v_mfma_f32_16x16x32_bf16 v[216:219], v[60:63], v[176:179], v[216:219]\n"
    "ds_read_b64_tr_b16 v[98:99], v40 offset:2048\n"
    "v_mul_f32 v154, v24, v154\n"
    "v_mul_f32 v155, v24, v155\n"
    "v_mfma_f32_16x16x32_bf16 v[224:227], v[64:67], v[176:179], v[224:227]\n"
    "global_load_lds_dwordx4 v27, s[42:43] offset:1024\n"
    "ds_read_b64_tr_b16 v[100:101], v41 offset:0\n"
    "ds_read_b64_tr_b16 v[102:103], v41 offset:2048\n"
    "v_mul_f32 v156, v24, v156\n"
    "v_mul_f32 v157, v24, v157\n"
    "v_mfma_f32_16x16x32_bf16 v[232:235], v[68:71], v[176:179], v[232:235]\n"
    "ds_read_b64_tr_b16 v[104:105], v42 offset:0\n"
    "v_mul_f32 v158, v24, v158\n"
    "v_mfma_f32_16x16x32_bf16 v[240:243], v[72:75], v[176:179], v[240:243]\n"
    "ds_read_b64_tr_b16 v[106:107], v42 offset:2048\n"
    "ds_read_b64_tr_b16 v[108:109], v43 offset:0\n"
    "v_mul_f32 v159, v24, v159\n"
    "v_cvt_pk_bf16_f32 v184, v152, v153\n"
    "v_mfma_f32_16x16x32_bf16 v[248:251], v[76:79], v[176:179], v[248:251]\n"
    "global_load_lds_dwordx4 v29, s[42:43] offset:2048\n"
    "ds_read_b64_tr_b16 v[110:111], v43 offset:2048\n"
    "v_cvt_pk_bf16_f32 v185, v154, v155\n"
    "v_cvt_pk_bf16_f32 v186, v156, v157\n"
    "v_mfma_f32_16x16x32_bf16 v[196:199], v[48:51], v[180:183], v[196:199]\n"
    "v_cvt_pk_bf16_f32 v187, v158, v159\n"
    "v_mfma_f32_16x16x32_bf16 v[204:207], v[52:55], v[180:183], v[204:207]\n"
    "v_mul_f32 v164, v25, v164\n"
    "v_mul_f32 v165, v25, v165\n"
    "v_mfma_f32_16x16x32_bf16 v[212:215], v[56:59], v[180:183], v[212:215]\n"
    "global_load_lds_dwordx4 v3, s[42:43] offset:3072\n"
    "s_add_u32 s42, s42, 131072\n"
    "s_addc_u32 s43, s43, 0\n"
    "v_mul_f32 v166, v25, v166\n"
    "v_mul_f32 v167, v25, v167\n"
    "v_mfma_f32_16x16x32_bf16 v[220:223], v[60:63], v[180:183], v[220:223]\n"
    "v_mul_f32 v168, v25, v168\n"
    "v_mul_f32 v169, v25, v169\n"
    "v_mfma_f32_16x16x32_bf16 v[228:231], v[64:67], v[180:183], v[228:231]\n"
    "v_mul_f32 v170, v25, v170\n"
    "v_mfma_f32_16x16x32_bf16 v[236:239], v[68:71], v[180:183], v[236:239]\n"
    "v_mul_f32 v171, v25, v171\n"
    "v_cvt_pk_bf16_f32 v188, v164, v165\n"
    "s_waitcnt vmcnt(26) lgkmcnt(0)\n"
    "s_barrier\n"
    "v_mfma_f32_16x16x32_bf16 v[244:247], v[72:75], v[180:183], v[244:247]\n"
    "v_cvt_pk_bf16_f32 v189, v166, v167\n"
    "v_cvt_pk_bf16_f32 v190, v168, v169\n"
    "v_mfma_f32_16x16x32_bf16 v[252:255], v[76:79], v[180:183], v[252:255]\n"
    "v_cvt_pk_bf16_f32 v191, v170, v171\n"
    "v_mfma_f32_16x16x32_bf16 v[160:163], v[120:123], v[124:127], v[4:7]\n"
    "s_add_u32 m0, s44, 0\n"
    "ds_read_b128 v[140:143], v45 offset:896\n"
    "ds_read_b128 v[132:135], v44 offset:28672\n"
    "v_mfma_f32_16x16x32_bf16 v[172:175], v[120:123], v[128:131], v[4:7]\n"
    "global_load_lds_dwordx4 v32, s[40:41] offset:0 sc1\n"
    "ds_read_b128 v[136:139], v44 offset:29696\n"
    "v_mfma_f32_16x16x32_bf16 v[152:155], v[112:115], v[124:127], 0\n"
    "ds_read_b128 v[144:147], v34 offset:12288\n"
    "ds_read_b128 v[148:151], v34 offset:13312\n"
    "v_mfma_f32_16x16x32_bf16 v[156:159], v[116:119], v[124:127], 0\n"
    "ds_read_b64_tr_b16 v[48:49], v36 offset:12288\n"
    "v_mfma_f32_16x16x32_bf16 v[164:167], v[112:115], v[128:131], 0\n"
    "global_load_lds_dwordx4 v32, s[40:41] offset:1024 sc1\n"
    "s_add_u32 s40, s40, 16384\n"
    "s_addc_u32 s41, s41, 0\n"
    "ds_read_b64_tr_b16 v[50:51], v36 offset:14336\n"
    "ds_read_b64_tr_b16 v[52:53], v37 offset:12288\n"
    "v_mfma_f32_16x16x32_bf16 v[168:171], v[116:119], v[128:131], 0\n"
    "ds_read_b64_tr_b16 v[54:55], v37 offset:14336\n"
    "v_rcp_f32 v24, v160\n"
    "v_mfma_f32_16x16x32_bf16 v[192:195], v[80:83], v[184:187], v[192:195]\n"
    "s_add_u32 m0, s45, 0\n"
    "ds_read_b64_tr_b16 v[56:57], v38 offset:12288\n"
    "ds_read_b64_tr_b16 v[58:59], v38 offset:14336\n"
    "v_rcp_f32 v25, v172\n"
    "v_mfma_f32_16x16x32_bf16 v[200:203], v[84:87], v[184:187], v[200:203]\n"
    "global_load_lds_dwordx4 v33, s[42:43] offset:0\n"
    "ds_read_b64_tr_b16 v[60:61], v39 offset:12288\n"
    "v_mfma_f32_16x16x32_bf16 v[208:211], v[88:91], v[184:187], v[208:211]\n"
    "ds_read_b64_tr_b16 v[62:63], v39 offset:14336\n"
    "ds_read_b64_tr_b16 v[64:65], v40 offset:12288\n"
    "v_mul_f32 v152, v24, v152\n"
    "v_mul_f32 v153, v24, v153\n"
    "v_mfma_f32_16x16x32_bf16 v[216:219], v[92:95], v[184:187], v[216:219]\n"
    "ds_read_b64_tr_b16 v[66:67], v40 offset:14336\n"
    "v_mul_f32 v154, v24, v154\n"
    "v_mul_f32 v155, v24, v155\n"
    "v_mfma_f32_16x16x32_bf16 v[224:227], v[96:99], v[184:187], v[224:227]\n"
    "global_load_lds_dwordx4 v27, s[42:43] offset:1024\n"
    "ds_read_b64_tr_b16 v[68:69], v41 offset:12288\n"
    "ds_read_b64_tr_b16 v[70:71], v41 offset:14336\n"
    "v_mul_f32 v156, v24, v156\n"
    "v_mul_f32 v157, v24, v157\n"
    "v_mfma_f32_16x16x32_bf16 v[232:235], v[100:103], v[184:187], v[232:235]\n"
    "ds_read_b64_tr_b16 v[72:73], v42 offset:12288\n"
    "v_mul_f32 v158, v24, v158\n"
    "v_mfma_f32_16x16x32_bf16 v[240:243], v[104:107], v[184:187], v[240:243]\n"
    "ds_read_b64_tr_b16 v[74:75], v42 offset:14336\n"
    "ds_read_b64_tr_b16 v[76:77], v43 offset:12288\n"
    "v_mul_f32 v159, v24, v159\n"
    "v_cvt_pk_bf16_f32 v176, v152, v153\n"
    "v_mfma_f32_16x16x32_bf16 v[248:251], v[108:111], v[184:187], v[248:251]\n"
    "global_load_lds_dwordx4 v29, s[42:43] offset:2048\n"
    "ds_read_b64_tr_b16 v[78:79], v43 offset:14336\n"
    "v_cvt_pk_bf16_f32 v177, v154, v155\n"
    "v_cvt_pk_bf16_f32 v178, v156, v157\n"
    "v_mfma_f32_16x16x32_bf16 v[196:199], v[80:83], v[188:191], v[196:199]\n"
    "v_cvt_pk_bf16_f32 v179, v158, v159\n"
    "v_mfma_f32_16x16x32_bf16 v[204:207], v[84:87], v[188:191], v[204:207]\n"
    "v_mul_f32 v164, v25, v164\n"
    "v_mul_f32 v165, v25, v165\n"
    "v_mfma_f32_16x16x32_bf16 v[212:215], v[88:91], v[188:191], v[212:215]\n"
    "global_load_lds_dwordx4 v3, s[42:43] offset:3072\n"
    "s_add_u32 s42, s42, 131072\n"
    "s_addc_u32 s43, s43, 0\n"
    "v_mul_f32 v166, v25, v166\n"
    "v_mul_f32 v167, v25, v167\n"
    "v_mfma_f32_16x16x32_bf16 v[220:223], v[92:95], v[188:191], v[220:223]\n"
    "v_mul_f32 v168, v25, v168\n"
    "v_mul_f32 v169, v25, v169\n"
    "v_mfma_f32_16x16x32_bf16 v[228:231], v[96:99], v[188:191], v[228:231]\n"
    "v_mul_f32 v170, v25, v170\n"
    "v_mfma_f32_16x16x32_bf16 v[236:239], v[100:103], v[188:191], v[236:239]\n"
    "v_mul_f32 v171, v25, v171\n"
    "v_cvt_pk_bf16_f32 v180, v164, v165\n"
    "s_waitcnt vmcnt(22) lgkmcnt(0)\n"
    "s_barrier\n"
    "v_mfma_f32_16x16x32_bf16 v[244:247], v[104:107], v[188:191], v[244:247]\n"
    "v_cvt_pk_bf16_f32 v181, v166, v167\n"
    "v_cvt_pk_bf16_f32 v182, v168, v169\n"
    "v_mfma_f32_16x16x32_bf16 v[252:255], v[108:111], v[188:191], v[252:255]\n"
    "v_cvt_pk_bf16_f32 v183, v170, v171\n"
    "s_waitcnt vmcnt(18)\n"
    "13:\n"
    "v_max3_u32 v24, v8, v9, v10\n"
    "v_max3_u32 v25, v11, v12, v13\n"
    "v_max3_u32 v24, v24, v14, v15\n"
    "v_max3_u32 v25, v25, v16, v17\n"
    "v_max3_u32 v24, v24, v18, v19\n"
    "v_max3_u32 v25, v25, v20, v21\n"
    "v_max3_u32 v24, v24, v22, v23\n"
    "v_max_u32 v24, v24, v25\n"
    "v_cmp_eq_u32 vcc, -1, v24\n"
    "s_and_b64 vcc, exec, vcc\n"
    "s_cbranch_vccnz 23f\n"
    "33:\n"
    "v_mfma_f32_16x16x32_bf16 v[160:163], v[140:143], v[144:147], v[4:7]\n"
    "s_add_u32 m0, s46, 16384\n"
    "ds_read_b128 v[120:123], v45 offset:1024\n"
    "ds_read_b128 v[112:115], v44 offset:0\n"
    "v_mfma_f32_16x16x32_bf16 v[172:175], v[140:143], v[148:151], v[4:7]\n"
    "global_load_lds_dwordx4 v46, s[52:53] offset:0 sc1\n"
    "ds_read_b128 v[116:119], v44 offset:1024\n"
    "v_mfma_f32_16x16x32_bf16 v[152:155], v[132:135], v[144:147], 0\n"
    "global_load_lds_dwordx4 v46, s[52:53] offset:1024 sc1\n"
    "ds_read_b128 v[124:127], v34 offset:24576\n"
    "ds_read_b128 v[128:131], v34 offset:25600\n"
    "v_mfma_f32_16x16x32_bf16 v[156:159], v[136:139], v[144:147], 0\n"
    "ds_read_b64_tr_b16 v[80:81], v36 offset:24576\n"
    "v_mfma_f32_16x16x32_bf16 v[164:167], v[132:135], v[148:151], 0\n"
    "global_load_lds_dwordx4 v46, s[52:53] offset:2048 sc1\n"
    "ds_read_b64_tr_b16 v[82:83], v36 offset:26624\n"
    "ds_read_b64_tr_b16 v[84:85], v37 offset:24576\n"
    "v_mfma_f32_16x16x32_bf16 v[168:171], v[136:139], v[148:151], 0\n"
    "ds_read_b64_tr_b16 v[86:87], v37 offset:26624\n"
    "v_rcp_f32 v24, v160\n"
    "v_mfma_f32_16x16x32_bf16 v[192:195], v[48:51], v[176:179], v[192:195]\n"
    "global_load_lds_dwordx4 v46, s[52:53] offset:3072 sc1\n"
    "s_add_u32 s52, s52, 16384\n"
    "s_addc_u32 s53, s53, 0\n"
    "ds_read_b64_tr_b16 v[88:89], v38 offset:24576\n"
    "ds_read_b64_tr_b16 v[90:91], v38 offset:26624\n"
    "v_rcp_f32 v25, v172\n"
    "v_mfma_f32_16x16x32_bf16 v[200:203], v[52:55], v[176:179], v[200:203]\n"
    "s_add_u32 m0, s44, 12288\n"
    "ds_read_b64_tr_b16 v[92:93], v39 offset:24576\n"
    "v_mfma_f32_16x16x32_bf16 v[208:211], v[56:59], v[176:179], v[208:211]\n"
    "global_load_lds_dwordx4 v32, s[40:41] offset:0 sc1\n"
    "ds_read_b64_tr_b16 v[94:95], v39 offset:26624\n"
    "ds_read_b64_tr_b16 v[96:97], v40 offset:24576\n"
    "v_mul_f32 v152, v24, v152\n"
    "v_mul_f32 v153, v24, v153\n"
    "v_mfma_f32_16x16x32_bf16 v[216:219], v[60:63], v[176:179], v[216:219]\n"
    "ds_read_b64_tr_b16 v[98:99], v40 offset:26624\n"
    "v_mul_f32 v154, v24, v154\n"
    "v_mul_f32 v155, v24, v155\n"
    "v_mfma_f32_16x16x32_bf16 v[224:227], v[64:67], v[176:179], v[224:227]\n"
    "global_load_lds_dwordx4 v32, s[40:41] offset:1024 sc1\n"
    "s_add_u32 s40, s40, 16384\n"
    "s_addc_u32 s41, s41, 0\n"
    "s_add_u32 m0, s45, 12288\n"
    "ds_read_b64_tr_b16 v[100:101], v41 offset:24576\n"
    "ds_read_b64_tr_b16 v[102:103], v41 offset:26624\n"
    "v_mul_f32 v156, v24, v156\n"
    "v_mul_f32 v157, v24, v157\n"
    "v_mfma_f32_16x16x32_bf16 v[232:235], v[68:71], v[176:179], v[232:235]\n"
    "global_load_lds_dwordx4 v33, s[42:43] offset:0\n"
    "ds_read_b64_tr_b16 v[104:105], v42 offset:24576\n"
    "v_mul_f32 v158, v24, v158\n"
    "v_mfma_f32_16x16x32_bf16 v[240:243], v[72:75], v[176:179], v[240:243]\n"
    "ds_read_b64_tr_b16 v[106:107], v42 offset:26624\n"
    "ds_read_b64_tr_b16 v[108:109], v43 offset:24576\n"
    "v_mul_f32 v159, v24, v159\n"
    "v_cvt_pk_bf16_f32 v184, v152, v153\n"
    "v_mfma_f32_16x16x32_bf16 v[248:251], v[76:79], v[176:179], v[248:251]\n"
    "global_load_lds_dwordx4 v27, s[42:43] offset:1024\n"
    "ds_read_b64_tr_b16 v[110:111], v43 offset:26624\n"
    "v_cvt_pk_bf16_f32 v185, v154, v155\n"
    "v_cvt_pk_bf16_f32 v186, v156, v157\n"
    "v_mfma_f32_16x16x32_bf16 v[196:199], v[48:51], v[180:183], v[196:199]\n"
    "v_cvt_pk_bf16_f32 v187, v158, v159\n"
    "v_mfma_f32_16x16x32_bf16 v[204:207], v[52:55], v[180:183], v[204:207]\n"
    "global_load_lds_dwordx4 v29, s[42:43] offset:2048\n"
    "v_mul_f32 v164, v25, v164\n"
    "v_mul_f32 v165, v25, v165\n"
    "v_mfma_f32_16x16x32_bf16 v[212:215], v[56:59], v[180:183], v[212:215]\n"
    "v_mul_f32 v166, v25, v166\n"
    "v_mul_f32 v167, v25, v167\n"
    "v_mfma_f32_16x16x32_bf16 v[220:223], v[60:63], v[180:183], v[220:223]\n"
    "global_load_lds_dwordx4 v3, s[42:43] offset:3072\n"
    "s_add_u32 s42, s42, 131072\n"
    "s_addc_u32 s43, s43, 0\n"
    "v_mul_f32 v168, v25, v168\n"
    "v_mul_f32 v169, v25, v169\n"
    "v_mfma_f32_16x16x32_bf16 v[228:231], v[64:67], v[180:183], v[228:231]\n"
    "v_mul_f32 v170, v25, v170\n"
    "v_mfma_f32_16x16x32_bf16 v[236:239], v[68:71], v[180:183], v[236:239]\n"
    "v_mul_f32 v171, v25, v171\n"
    "v_cvt_pk_bf16_f32 v188, v164, v165\n"
    "s_waitcnt vmcnt(22) lgkmcnt(0)\n"
    "s_barrier\n"
    "v_mfma_f32_16x16x32_bf16 v[244:247], v[72:75], v[180:183], v[244:247]\n"
    "v_cvt_pk_bf16_f32 v189, v166, v167\n"
    "v_cvt_pk_bf16_f32 v190, v168, v169\n"
    "v_mfma_f32_16x16x32_bf16 v[252:255], v[76:79], v[180:183], v[252:255]\n"
    "v_cvt_pk_bf16_f32 v191, v170, v171\n"
    "v_mfma_f32_16x16x32_bf16 v[160:163], v[120:123], v[124:127], v[4:7]\n"
    "s_add_u32 m0, s44, 24576\n"
    "ds_read_b128 v[140:143], v45 offset:1152\n"
    "ds_read_b128 v[132:135], v44 offset:4096\n"
    "v_mfma_f32_16x16x32_bf16 v[172:175], v[120:123], v[128:131], v[4:7]\n"
    "global_load_lds_dwordx4 v32, s[40:41] offset:0 sc1\n"
    "ds_read_b128 v[136:139], v44 offset:5120\n"
    "v_mfma_f32_16x16x32_bf16 v[152:155], v[112:115], v[124:127], 0\n"
    "ds_read_b128 v[144:147], v34 offset:36864\n"
    "ds_read_b128 v[148:151], v34 offset:37888\n"
    "v_mfma_f32_16x16x32_bf16 v[156:159], v[116:119], v[124:127], 0\n"
    "ds_read_b64_tr_b16 v[48:49], v36 offset:36864\n"
    "v_mfma_f32_16x16x32_bf16 v[164:167], v[112:115], v[128:131], 0\n"
    "global_load_lds_dwordx4 v32, s[40:41] offset:1024 sc1\n"
    "s_add_u32 s40, s40, 16384\n"
    "s_addc_u32 s41, s41, 0\n"
    "ds_read_b64_tr_b16 v[50:51], v36 offset:38912\n"
    "ds_read_b64_tr_b16 v[52:53], v37 offset:36864\n"
    "v_mfma_f32_16x16x32_bf16 v[168:171], v[116:119], v[128:131], 0\n"
    "ds_read_b64_tr_b16 v[54:55], v37 offset:38912\n"
    "v_rcp_f32 v24, v160\n"
    "v_mfma_f32_16x16x32_bf16 v[192:195], v[80:83], v[184:187], v[192:195]\n"
    "s_add_u32 m0, s45, 24576\n"
    "ds_read_b64_tr_b16 v[56:57], v38 offset:36864\n"
    "ds_read_b64_tr_b16 v[58:59], v38 offset:38912\n"
    "v_rcp_f32 v25, v172\n"
    "v_mfma_f32_16x16x32_bf16 v[200:203], v[84:87], v[184:187], v[200:203]\n"
    "global_load_lds_dwordx4 v33, s[42:43] offset:0\n"
    "ds_read_b64_tr_b16 v[60:61], v39 offset:36864\n"
    "v_mfma_f32_16x16x32_bf16 v[208:211], v[88:91], v[184:187], v[208:211]\n"
    "ds_read_b64_tr_b16 v[62:63], v39 offset:38912\n"
    "ds_read_b64_tr_b16 v[64:65], v40 offset:36864\n"
    "v_mul_f32 v152, v24, v152\n"
    "v_mul_f32 v153, v24, v153\n"
    "v_mfma_f32_16x16x32_bf16 v[216:219], v[92:95], v[184:187], v[216:219]\n"
    "ds_read_b64_tr_b16 v[66:67], v40 offset:38912\n"
    "v_mul_f32 v154, v24, v154\n"
    "v_mul_f32 v155, v24, v155\n"
    "v_mfma_f32_16x16x32_bf16 v[224:227], v[96:99], v[184:187], v[224:227]\n"
    "global_load_lds_dwordx4 v27, s[42:43] offset:1024\n"
    "ds_read_b64_tr_b16 v[68:69], v41 offset:36864\n"
    "ds_read_b64_tr_b16 v[70:71], v41 offset:38912\n"
    "v_mul_f32 v156, v24, v156\n"
    "v_mul_f32 v157, v24, v157\n"
    "v_mfma_f32_16x16x32_bf16 v[232:235], v[100:103], v[184:187], v[232:235]\n"
    "ds_read_b64_tr_b16 v[72:73], v42 offset:36864\n"
    "v_mul_f32 v158, v24, v158\n"
    "v_mfma_f32_16x16x32_bf16 v[240:243], v[104:107], v[184:187], v[240:243]\n"
    "ds_read_b64_tr_b16 v[74:75], v42 offset:38912\n"
    "ds_read_b64_tr_b16 v[76:77], v43 offset:36864\n"
    "v_mul_f32 v159, v24, v159\n"
    "v_cvt_pk_bf16_f32 v176, v152, v153\n"
    "v_mfma_f32_16x16x32_bf16 v[248:251], v[108:111], v[184:187], v[248:251]\n"
    "global_load_lds_dwordx4 v29, s[42:43] offset:2048\n"
    "ds_read_b64_tr_b16 v[78:79], v43 offset:38912\n"
    "v_cvt_pk_bf16_f32 v177, v154, v155\n"
    "v_cvt_pk_bf16_f32 v178, v156, v157\n"
    "v_mfma_f32_16x16x32_bf16 v[196:199], v[80:83], v[188:191], v[196:199]\n"
    "v_cvt_pk_bf16_f32 v179, v158, v159\n"
    "v_mfma_f32_16x16x32_bf16 v[204:207], v[84:87], v[188:191], v[204:207]\n"
    "v_mul_f32 v164, v25, v164\n"
    "v_mul_f32 v165, v25, v165\n"
    "v_mfma_f32_16x16x32_bf16 v[212:215], v[88:91], v[188:191], v[212:215]\n"
    "global_load_lds_dwordx4 v3, s[42:43] offset:3072\n"
    "s_add_u32 s42, s42, 131072\n"
    "s_addc_u32 s43, s43, 0\n"
    "v_mul_f32 v166, v25, v166\n"
    "v_mul_f32 v167, v25, v167\n"
    "v_mfma_f32_16x16x32_bf16 v[220:223], v[92:95], v[188:191], v[220:223]\n"
    "v_mul_f32 v168, v25, v168\n"
    "v_mul_f32 v169, v25, v169\n"
    "v_mfma_f32_16x16x32_bf16 v[228:231], v[96:99], v[188:191], v[228:231]\n"
    "v_mul_f32 v170, v25, v170\n"
    "v_mfma_f32_16x16x32_bf16 v[236:239], v[100:103], v[188:191], v[236:239]\n"
    "v_mul_f32 v171, v25, v171\n"
    "v_cvt_pk_bf16_f32 v180, v164, v165\n"
    "s_waitcnt vmcnt(22) lgkmcnt(0)\n"
    "s_barrier\n"
    "v_mfma_f32_16x16x32_bf16 v[244:247], v[104:107], v[188:191], v[244:247]\n"
    "v_cvt_pk_bf16_f32 v181, v166, v167\n"
    "v_cvt_pk_bf16_f32 v182, v168, v169\n"
    "v_mfma_f32_16x16x32_bf16 v[252:255], v[108:111], v[188:191], v[252:255]\n"
    "v_cvt_pk_bf16_f32 v183, v170, v171\n"
    "v_mfma_f32_16x16x32_bf16 v[160:163], v[140:143], v[144:147], v[4:7]\n"
    "s_add_u32 m0, s44, 36864\n"
    "ds_read_b128 v[120:123], v45 offset:1280\n"
    "ds_read_b128 v[112:115], v44 offset:8192\n"
    "v_mfma_f32_16x16x32_bf16 v[172:175], v[140:143], v[148:151], v[4:7]\n"
    "global_load_lds_dwordx4 v32, s[40:41] offset:0 sc1\n"
    "ds_read_b128 v[116:119], v44 offset:9216\n"
    "v_mfma_f32_16x16x32_bf16 v[152:155], v[132:135], v[144:147], 0\n"
    "ds_read_b128 v[124:127], v34 offset:49152\n"
    "ds_read_b128 v[128:131], v34 offset:50176\n"
    "v_mfma_f32_16x16x32_bf16 v[156:159], v[136:139], v[144:147], 0\n"
    "ds_read_b64_tr_b16 v[80:81], v36 offset:49152\n"
    "v_mfma_f32_16x16x32_bf16 v[164:167], v[132:135], v[148:151], 0\n"
    "global_load_lds_dwordx4 v32, s[40:41] offset:1024 sc1\n"
    "s_add_u32 s40, s40, 16384\n"
    "s_addc_u32 s41, s41, 0\n"
    "ds_read_b64_tr_b16 v[82:83], v36 offset:51200\n"
    "ds_read_b64_tr_b16 v[84:85], v37 offset:49152\n"
    "v_mfma_f32_16x16x32_bf16 v[168:171], v[136:139], v[148:151], 0\n"
    "ds_read_b64_tr_b16 v[86:87], v37 offset:51200\n"
    "v_rcp_f32 v24, v160\n"
    "v_mfma_f32_16x16x32_bf16 v[192:195], v[48:51], v[176:179], v[192:195]\n"
    "s_add_u32 m0, s45, 36864\n"
    "ds_read_b64_tr_b16 v[88:89], v38 offset:49152\n"
    "ds_read_b64_tr_b16 v[90:91], v38 offset:51200\n"
    "v_rcp_f32 v25, v172\n"
    "v_mfma_f32_16x16x32_bf16 v[200:203], v[52:55], v[176:179], v[200:203]\n"
    "global_load_lds_dwordx4 v33, s[42:43] offset:0\n"
    "ds_read_b64_tr_b16 v[92:93], v39 offset:49152\n"
    "v_mfma_f32_16x16x32_bf16 v[208:211], v[56:59], v[176:179], v[208:211]\n"
    "ds_read_b64_tr_b16 v[94:95], v39 offset:51200\n"
    "ds_read_b64_tr_b16 v[96:97], v40 offset:49152\n"
    "v_mul_f32 v152, v24, v152\n"
    "v_mul_f32 v153, v24, v153\n"
    "v_mfma_f32_16x16x32_bf16 v[216:219], v[60:63], v[176:179], v[216:219]\n"
    "ds_read_b64_tr_b16 v[98:99], v40 offset:51200\n"
    "v_mul_f32 v154, v24, v154\n"
    "v_mul_f32 v155, v24, v155\n"
    "v_mfma_f32_16x16x32_bf16 v[224:227], v[64:67], v[176:179], v[224:227]\n"
    "global_load_lds_dwordx4 v27, s[42:43] offset:1024\n"
    "ds_read_b64_tr_b16 v[100:101], v41 offset:49152\n"
    "ds_read_b64_tr_b16 v[102:103], v41 offset:51200\n"
    "v_mul_f32 v156, v24, v156\n"
    "v_mul_f32 v157, v24, v157\n"
    "v_mfma_f32_16x16x32_bf16 v[232:235], v[68:71], v[176:179], v[232:235]\n"
    "ds_read_b64_tr_b16 v[104:105], v42 offset:49152\n"
    "v_mul_f32 v158, v24, v158\n"
    "v_mfma_f32_16x16x32_bf16 v[240:243], v[72:75], v[176:179], v[240:243]\n"
    "ds_read_b64_tr_b16 v[106:107], v42 offset:51200\n"
    "ds_read_b64_tr_b16 v[108:109], v43 offset:49152\n"
    "v_mul_f32 v159, v24, v159\n"
    "v_cvt_pk_bf16_f32 v184, v152, v153\n"
    "v_mfma_f32_16x16x32_bf16 v[248:251], v[76:79], v[176:179], v[248:251]\n"
    "global_load_lds_dwordx4 v29, s[42:43] offset:2048\n"
    "ds_read_b64_tr_b16 v[110:111], v43 offset:51200\n"
    "v_cvt_pk_bf16_f32 v185, v154, v155\n"
    "v_cvt_pk_bf16_f32 v186, v156, v157\n"
    "v_mfma_f32_16x16x32_bf16 v[196:199], v[48:51], v[180:183], v[196:199]\n"
    "v_cvt_pk_bf16_f32 v187, v158, v159\n"
    "v_mfma_f32_16x16x32_bf16 v[204:207], v[52:55], v[180:183], v[204:207]\n"
    "v_mul_f32 v164, v25, v164\n"
    "v_mul_f32 v165, v25, v165\n"
    "v_mfma_f32_16x16x32_bf16 v[212:215], v[56:59], v[180:183], v[212:215]\n"
    "global_load_lds_dwordx4 v3, s[42:43] offset:3072\n"
    "s_add_u32 s42, s42, 131072\n"
    "s_addc_u32 s43, s43, 0\n"
    "v_mul_f32 v166, v25, v166\n"
    "v_mul_f32 v167, v25, v167\n"
    "v_mfma_f32_16x16x32_bf16 v[220:223], v[60:63], v[180:183], v[220:223]\n"
    "v_mul_f32 v168, v25, v168\n"
    "v_mul_f32 v169, v25, v169\n"
    "v_mfma_f32_16x16x32_bf16 v[228:231], v[64:67], v[180:183], v[228:231]\n"
    "v_mul_f32 v170, v25, v170\n"
    "v_mfma_f32_16x16x32_bf16 v[236:239], v[68:71], v[180:183], v[236:239]\n"
    "v_mul_f32 v171, v25, v171\n"
    "v_cvt_pk_bf16_f32 v188, v164, v165\n"
    "s_waitcnt vmcnt(22) lgkmcnt(0)\n"
    "s_barrier\n"
    "v_mfma_f32_16x16x32_bf16 v[244:247], v[72:75], v[180:183], v[244:247]\n"
    "v_cvt_pk_bf16_f32 v189, v166, v167\n"
    "v_cvt_pk_bf16_f32 v190, v168, v169\n"
    "v_mfma_f32_16x16x32_bf16 v[252:255], v[76:79], v[180:183], v[252:255]\n"
    "v_cvt_pk_bf16_f32 v191, v170, v171\n"
    "v_mfma_f32_16x16x32_bf16 v[160:163], v[120:123], v[124:127], v[4:7]\n"
    "s_add_u32 m0, s44, 49152\n"
    "ds_read_b128 v[140:143], v45 offset:1408\n"
    "ds_read_b128 v[132:135], v44 offset:12288\n"
    "v_mfma_f32_16x16x32_bf16 v[172:175], v[120:123], v[128:131], v[4:7]\n"
    "global_load_lds_dwordx4 v32, s[40:41] offset:0 sc1\n"
    "ds_read_b128 v[136:139], v44 offset:13312\n"
    "v_mfma_f32_16x16x32_bf16 v[152:155], v[112:115], v[124:127], 0\n"
    "ds_read_b128 v[144:147], v34 offset:0\n"
    "ds_read_b128 v[148:151], v34 offset:1024\n"
    "v_mfma_f32_16x16x32_bf16 v[156:159], v[116:119], v[124:127], 0\n"
    "ds_read_b64_tr_b16 v[48:49], v36 offset:0\n"
    "v_mfma_f32_16x16x32_bf16 v[164:167], v[112:115], v[128:131], 0\n"
    "global_load_lds_dwordx4 v32, s[40:41] offset:1024 sc1\n"
    "s_add_u32 s40, s40, 16384\n"
    "s_addc_u32 s41, s41, 0\n"
    "ds_read_b64_tr_b16 v[50:51], v36 offset:2048\n"
    "ds_read_b64_tr_b16 v[52:53], v37 offset:0\n"
    "v_mfma_f32_16x16x32_bf16 v[168:171], v[116:119], v[128:131], 0\n"
    "ds_read_b64_tr_b16 v[54:55], v37 offset:2048\n"
    "v_rcp_f32 v24, v160\n"
    "v_mfma_f32_16x16x32_bf16 v[192:195], v[80:83], v[184:187], v[192:195]\n"
    "s_add_u32 m0, s45, 49152\n"
    "ds_read_b64_tr_b16 v[56:57], v38 offset:0\n"
    "ds_read_b64_tr_b16 v[58:59], v38 offset:2048\n"
    "v_rcp_f32 v25, v172\n"
    "v_mfma_f32_16x16x32_bf16 v[200:203], v[84:87], v[184:187], v[200:203]\n"
    "global_load_lds_dwordx4 v33, s[42:43] offset:0\n"
    "ds_read_b64_tr_b16 v[60:61], v39 offset:0\n"
    "v_mfma_f32_16x16x32_bf16 v[208:211], v[88:91], v[184:187], v[208:211]\n"
    "ds_read_b64_tr_b16 v[62:63], v39 offset:2048\n"
    "ds_read_b64_tr_b16 v[64:65], v40 offset:0\n"
    "v_mul_f32 v152, v24, v152\n"
    "v_mul_f32 v153, v24, v153\n"
    "v_mfma_f32_16x16x32_bf16 v[216:219], v[92:95], v[184:187], v[216:219]\n"
    "ds_read_b64_tr_b16 v[66:67], v40 offset:2048\n"
    "v_mul_f32 v154, v24, v154\n"
    "v_mul_f32 v155, v24, v155\n"
    "v_mfma_f32_16x16x32_bf16 v[224:227], v[96:99], v[184:187], v[224:227]\n"
    "global_load_lds_dwordx4 v27, s[42:43] offset:1024\n"
    "ds_read_b64_tr_b16 v[68:69], v41 offset:0\n"
    "ds_read_b64_tr_b16 v[70:71], v41 offset:2048\n"
    "v_mul_f32 v156, v24, v156\n"
    "v_mul_f32 v157, v24, v157\n"
    "v_mfma_f32_16x16x32_bf16 v[232:235], v[100:103], v[184:187], v[232:235]\n"
    "ds_read_b64_tr_b16 v[72:73], v42 offset:0\n"
    "v_mul_f32 v158, v24, v158\n"
    "v_mfma_f32_16x16x32_bf16 v[240:243], v[104:107], v[184:187], v[240:243]\n"
    "ds_read_b64_tr_b16 v[74:75], v42 offset:2048\n"
    "ds_read_b64_tr_b16 v[76:77], v43 offset:0\n"
    "v_mul_f32 v159, v24, v159\n"
    "v_cvt_pk_bf16_f32 v176, v152, v153\n"
    "v_mfma_f32_16x16x32_bf16 v[248:251], v[108:111], v[184:187], v[248:251]\n"
    "global_load_lds_dwordx4 v29, s[42:43] offset:2048\n"
    "ds_read_b64_tr_b16 v[78:79], v43 offset:2048\n"
    "v_cvt_pk_bf16_f32 v177, v154, v155\n"
    "v_cvt_pk_bf16_f32 v178, v156, v157\n"
    "v_mfma_f32_16x16x32_bf16 v[196:199], v[80:83], v[188:191], v[196:199]\n"
    "v_cvt_pk_bf16_f32 v179, v158, v159\n"
    "v_mfma_f32_16x16x32_bf16 v[204:207], v[84:87], v[188:191], v[204:207]\n"
    "v_mul_f32 v164, v25, v164\n"
    "v_mul_f32 v165, v25, v165\n"
    "v_mfma_f32_16x16x32_bf16 v[212:215], v[88:91], v[188:191], v[212:215]\n"
    "global_load_lds_dwordx4 v3, s[42:43] offset:3072\n"
    "s_add_u32 s42, s42, 131072\n"
    "s_addc_u32 s43, s43, 0\n"
    "v_mul_f32 v166, v25, v166\n"
    "v_mul_f32 v167, v25, v167\n"
    "v_mfma_f32_16x16x32_bf16 v[220:223], v[92:95], v[188:191], v[220:223]\n"
    "v_mul_f32 v168, v25, v168\n"
    "v_mul_f32 v169, v25, v169\n"
    "v_mfma_f32_16x16x32_bf16 v[228:231], v[96:99], v[188:191], v[228:231]\n"
    "v_mul_f32 v170, v25, v170\n"
    "v_mfma_f32_16x16x32_bf16 v[236:239], v[100:103], v[188:191], v[236:239]\n"
    "v_mul_f32 v171, v25, v171\n"
    "v_cvt_pk_bf16_f32 v180, v164, v165\n"
    "s_waitcnt vmcnt(18) lgkmcnt(0)\n"
    "s_barrier\n"
    "v_mfma_f32_16x16x32_bf16 v[244:247], v[104:107], v[188:191], v[244:247]\n"
    "v_cvt_pk_bf16_f32 v181, v166, v167\n"
    "v_cvt_pk_bf16_f32 v182, v168, v169\n"
    "v_mfma_f32_16x16x32_bf16 v[252:255], v[108:111], v[188:191], v[252:255]\n"
    "v_cvt_pk_bf16_f32 v183, v170, v171\n"
    "v_mfma_f32_16x16x32_bf16 v[160:163], v[140:143], v[144:147], v[4:7]\n"
    "s_add_u32 m0, s45, 0\n"
    "ds_read_b128 v[120:123], v45 offset:1536\n"
    "ds_read_b128 v[112:115], v44 offset:16384\n"
    "v_mfma_f32_16x16x32_bf16 v[172:175], v[140:143], v[148:151], v[4:7]\n"
    "global_load_lds_dwordx4 v33, s[42:43] offset:0\n"
    "ds_read_b128 v[116:119], v44 offset:17408\n"
    "v_mfma_f32_16x16x32_bf16 v[152:155], v[132:135], v[144:147], 0\n"
    "ds_read_b128 v[124:127], v34 offset:12288\n"
    "ds_read_b128 v[128:131], v34 offset:13312\n"
    "v_mfma_f32_16x16x32_bf16 v[156:159], v[136:139], v[144:147], 0\n"
    "ds_read_b64_tr_b16 v[80:81], v36 offset:12288\n"
    "v_mfma_f32_16x16x32_bf16 v[164:167], v[132:135], v[148:151], 0\n"
    "ds_read_b64_tr_b16 v[82:83], v36 offset:14336\n"
    "ds_read_b64_tr_b16 v[84:85], v37 offset:12288\n"
    "v_mfma_f32_16x16x32_bf16 v[168:171], v[136:139], v[148:151], 0\n"
    "global_load_lds_dwordx4 v27, s[42:43] offset:1024\n"
    "ds_read_b64_tr_b16 v[86:87], v37 offset:14336\n"
    "v_rcp_f32 v24, v160\n"
    "v_mfma_f32_16x16x32_bf16 v[192:195], v[48:51], v[176:179], v[192:195]\n"
    "ds_read_b64_tr_b16 v[88:89], v38 offset:12288\n"
    "ds_read_b64_tr_b16 v[90:91], v38 offset:14336\n"
    "v_rcp_f32 v25, v172\n"
    "v_mfma_f32_16x16x32_bf16 v[200:203], v[52:55], v[176:179], v[200:203]\n"
    "ds_read_b64_tr_b16 v[92:93], v39 offset:12288\n"
    "v_mfma_f32_16x16x32_bf16 v[208:211], v[56:59], v[176:179], v[208:211]\n"
    "ds_read_b64_tr_b16 v[94:95], v39 offset:14336\n"
    "ds_read_b64_tr_b16 v[96:97], v40 offset:12288\n"
    "v_mul_f32 v152, v24, v152\n"
    "v_mul_f32 v153, v24, v153\n"
    "v_mfma_f32_16x16x32_bf16 v[216:219], v[60:63], v[176:179], v[216:219]\n"
    "ds_read_b64_tr_b16 v[98:99], v40 offset:14336\n"
    "v_mul_f32 v154, v24, v154\n"
    "v_mul_f32 v155, v24, v155\n"
    "v_mfma_f32_16x16x32_bf16 v[224:227], v[64:67], v[176:179], v[224:227]\n"
    "global_load_lds_dwordx4 v29, s[42:43] offset:2048\n"
    "ds_read_b64_tr_b16 v[100:101], v41 offset:12288\n"
    "ds_read_b64_tr_b16 v[102:103], v41 offset:14336\n"
    "v_mul_f32 v156, v24, v156\n"
    "v_mul_f32 v157, v24, v157\n"
    "v_mfma_f32_16x16x32_bf16 v[232:235], v[68:71], v[176:179], v[232:235]\n"
    "ds_read_b64_tr_b16 v[104:105], v42 offset:12288\n"
    "v_mul_f32 v158, v24, v158\n"
    "v_mfma_f32_16x16x32_bf16 v[240:243], v[72:75], v[176:179], v[240:243]\n"
    "ds_read_b64_tr_b16 v[106:107], v42 offset:14336\n"
    "ds_read_b64_tr_b16 v[108:109], v43 offset:12288\n"
    "v_mul_f32 v159, v24, v159\n"
    "v_cvt_pk_bf16_f32 v184, v152, v153\n"
    "v_mfma_f32_16x16x32_bf16 v[248:251], v[76:79], v[176:179], v[248:251]\n"
    "ds_read_b64_tr_b16 v[110:111], v43 offset:14336\n"
    "v_cvt_pk_bf16_f32 v185, v154, v155\n"
    "v_cvt_pk_bf16_f32 v186, v156, v157\n"
    "v_mfma_f32_16x16x32_bf16 v[196:199], v[48:51], v[180:183], v[196:199]\n"
    "global_load_lds_dwordx4 v3, s[42:43] offset:3072\n"
    "s_add_u32 s42, s42, 131072\n"
    "s_addc_u32 s43, s43, 0\n"
    "v_cvt_pk_bf16_f32 v187, v158, v159\n"
    "v_mfma_f32_16x16x32_bf16 v[204:207], v[52:55], v[180:183], v[204:207]\n"
    "v_mul_f32 v164, v25, v164\n"
    "v_mul_f32 v165, v25, v165\n"
    "v_mfma_f32_16x16x32_bf16 v[212:215], v[56:59], v[180:183], v[212:215]\n"
    "v_mul_f32 v166, v25, v166\n"
    "v_mul_f32 v167, v25, v167\n"
    "v_mfma_f32_16x16x32_bf16 v[220:223], v[60:63], v[180:183], v[220:223]\n"
    "v_mul_f32 v168, v25, v168\n"
    "v_mul_f32 v169, v25, v169\n"
    "v_mfma_f32_16x16x32_bf16 v[228:231], v[64:67], v[180:183], v[228:231]\n"
    "v_mul_f32 v170, v25, v170\n"
    "v_mfma_f32_16x16x32_bf16 v[236:239], v[68:71], v[180:183], v[236:239]\n"
    "v_mul_f32 v171, v25, v171\n"
    "v_cvt_pk_bf16_f32 v188, v164, v165\n"
    "s_waitcnt vmcnt(16) lgkmcnt(0)\n"
    "s_barrier\n"
    "v_mfma_f32_16x16x32_bf16 v[244:247], v[72:75], v[180:183], v[244:247]\n"
    "v_cvt_pk_bf16_f32 v189, v166, v167\n"
    "v_cvt_pk_bf16_f32 v190, v168, v169\n"
    "v_mfma_f32_16x16x32_bf16 v[252:255], v[76:79], v[180:183], v[252:255]\n"
    "v_cvt_pk_bf16_f32 v191, v170, v171\n"
    "v_mfma_f32_16x16x32_bf16 v[160:163], v[120:123], v[124:127], v[4:7]\n"
    "ds_read_b128 v[140:143], v45 offset:1664\n"
    "ds_read_b128 v[132:135], v44 offset:20480\n"
    "v_mfma_f32_16x16x32_bf16 v[172:175], v[120:123], v[128:131], v[4:7]\n"
    "global_load_dwordx4 v[8:11], v32, s[48:49] sc1\n"
    "ds_read_b128 v[136:139], v44 offset:21504\n"
    "v_mfma_f32_16x16x32_bf16 v[152:155], v[112:115], v[124:127], 0\n"
    "ds_read_b128 v[144:147], v34 offset:24576\n"
    "ds_read_b128 v[148:151], v34 offset:25600\n"
    "v_mfma_f32_16x16x32_bf16 v[156:159], v[116:119], v[124:127], 0\n"
    "ds_read_b64_tr_b16 v[48:49], v36 offset:24576\n"
    "v_mfma_f32_16x16x32_bf16 v[164:167], v[112:115], v[128:131], 0\n"
    "global_load_dwordx4 v[12:15], v32, s[48:49] offset:1024 sc1\n"
    "ds_read_b64_tr_b16 v[50:51], v36 offset:26624\n"
    "ds_read_b64_tr_b16 v[52:53], v37 offset:24576\n"
    "v_mfma_f32_16x16x32_bf16 v[168:171], v[116:119], v[128:131], 0\n"
    "ds_read_b64_tr_b16 v[54:55], v37 offset:26624\n"
    "v_rcp_f32 v24, v160\n"
    "v_mfma_f32_16x16x32_bf16 v[192:195], v[80:83], v[184:187], v[192:195]\n"
    "ds_read_b64_tr_b16 v[56:57], v38 offset:24576\n"
    "ds_read_b64_tr_b16 v[58:59], v38 offset:26624\n"
    "v_rcp_f32 v25, v172\n"
    "v_mfma_f32_16x16x32_bf16 v[200:203], v[84:87], v[184:187], v[200:203]\n"
    "global_load_dwordx4 v[16:19], v35, s[48:49] sc1\n"
    "ds_read_b64_tr_b16 v[60:61], v39 offset:24576\n"
    "v_mfma_f32_16x16x32_bf16 v[208:211], v[88:91], v[184:187], v[208:211]\n"
    "ds_read_b64_tr_b16 v[62:63], v39 offset:26624\n"
    "ds_read_b64_tr_b16 v[64:65], v40 offset:24576\n"
    "v_mul_f32 v152, v24, v152\n"
    "v_mul_f32 v153, v24, v153\n"
    "v_mfma_f32_16x16x32_bf16 v[216:219], v[92:95], v[184:187], v[216:219]\n"
    "ds_read_b64_tr_b16 v[66:67], v40 offset:26624\n"
    "v_mul_f32 v154, v24, v154\n"
    "v_mul_f32 v155, v24, v155\n"
    "v_mfma_f32_16x16x32_bf16 v[224:227], v[96:99], v[184:187], v[224:227]\n"
    "global_load_dwordx4 v[20:23], v35, s[48:49] offset:1024 sc1\n"
    "ds_read_b64_tr_b16 v[68:69], v41 offset:24576\n"
    "ds_read_b64_tr_b16 v[70:71], v41 offset:26624\n"
    "v_mul_f32 v156, v24, v156\n"
    "v_mul_f32 v157, v24, v157\n"
    "v_mfma_f32_16x16x32_bf16 v[232:235], v[100:103], v[184:187], v[232:235]\n"
    "ds_read_b64_tr_b16 v[72:73], v42 offset:24576\n"
    "v_mul_f32 v158, v24, v158\n"
    "v_mfma_f32_16x16x32_bf16 v[240:243], v[104:107], v[184:187], v[240:243]\n"
    "s_getpc_b64 s[70:71]\n"
    "2:\n"
    "v_min_u32 v26, lin_end-2b-4, v28\n"
    "s_mov_b32 m0, s47\n"
    "ds_read_b64_tr_b16 v[74:75], v42 offset:26624\n"
    "ds_read_b64_tr_b16 v[76:77], v43 offset:24576\n"
    "v_mul_f32 v159, v24, v159\n"
    "v_cvt_pk_bf16_f32 v176, v152, v153\n"
    "v_mfma_f32_16x16x32_bf16 v[248:251], v[108:111], v[184:187], v[248:251]\n"
    "global_load_lds_dword v26, s[70:71]\n"
    "ds_read_b64_tr_b16 v[78:79], v43 offset:26624\n"
    "v_cvt_pk_bf16_f32 v177, v154, v155\n"
    "v_cvt_pk_bf16_f32 v178, v156, v157\n"
    "v_mfma_f32_16x16x32_bf16 v[196:199], v[80:83], v[188:191], v[196:199]\n"
    "v_cvt_pk_bf16_f32 v179, v158, v159\n"
    "v_mfma_f32_16x16x32_bf16 v[204:207], v[84:87], v[188:191], v[204:207]\n"
    "v_mul_f32 v164, v25, v164\n"
    "v_mul_f32 v165, v25, v165\n"
    "v_mfma_f32_16x16x32_bf16 v[212:215], v[88:91], v[188:191], v[212:215]\n"
    "global_load_lds_dword v31, s[72:73]\n"
    "v_mul_f32 v166, v25, v166\n"
    "v_mul_f32 v167, v25, v167\n"
    "v_mfma_f32_16x16x32_bf16 v[220:223], v[92:95], v[188:191], v[220:223]\n"
    "v_mul_f32 v168, v25, v168\n"
    "v_mul_f32 v169, v25, v169\n"
    "v_mfma_f32_16x16x32_bf16 v[228:231], v[96:99], v[188:191], v[228:231]\n"
    "v_mul_f32 v170, v25, v170\n"
    "v_mfma_f32_16x16x32_bf16 v[236:239], v[100:103], v[188:191], v[236:239]\n"
    "v_mul_f32 v171, v25, v171\n"
    "v_cvt_pk_bf16_f32 v180, v164, v165\n"
    "s_waitcnt vmcnt(16) lgkmcnt(0)\n"
    "s_barrier\n"
    "v_mfma_f32_16x16x32_bf16 v[244:247], v[104:107], v[188:191], v[244:247]\n"
    "v_cvt_pk_bf16_f32 v181, v166, v167\n"
    "v_cvt_pk_bf16_f32 v182, v168, v169\n"
    "v_mfma_f32_16x16x32_bf16 v[252:255], v[108:111], v[188:191], v[252:255]\n"
    "v_cvt_pk_bf16_f32 v183, v170, v171\n"
    "v_mfma_f32_16x16x32_bf16 v[160:163], v[140:143], v[144:147], v[4:7]\n"
    "ds_read_b128 v[120:123], v45 offset:1792\n"
    "ds_read_b128 v[112:115], v44 offset:24576\n"
    "v_mfma_f32_16x16x32_bf16 v[172:175], v[140:143], v[148:151], v[4:7]\n"
    "ds_read_b128 v[116:119], v44 offset:25600\n"
    "v_mfma_f32_16x16x32_bf16 v[152:155], v[132:135], v[144:147], 0\n"
    "ds_read_b128 v[124:127], v34 offset:36864\n"
    "ds_read_b128 v[128:131], v34 offset:37888\n"
    "v_mfma_f32_16x16x32_bf16 v[156:159], v[136:139], v[144:147], 0\n"
    "ds_read_b64_tr_b16 v[80:81], v36 offset:36864\n"
    "v_mfma_f32_16x16x32_bf16 v[164:167], v[132:135], v[148:151], 0\n"
    "ds_read_b64_tr_b16 v[82:83], v36 offset:38912\n"
    "ds_read_b64_tr_b16 v[84:85], v37 offset:36864\n"
    "v_mfma_f32_16x16x32_bf16 v[168:171], v[136:139], v[148:151], 0\n"
    "ds_read_b64_tr_b16 v[86:87], v37 offset:38912\n"
    "v_rcp_f32 v24, v160\n"
    "v_mfma_f32_16x16x32_bf16 v[192:195], v[48:51], v[176:179], v[192:195]\n"
    "ds_read_b64_tr_b16 v[88:89], v38 offset:36864\n"
    "ds_read_b64_tr_b16 v[90:91], v38 offset:38912\n"
    "v_rcp_f32 v25, v172\n"
    "v_mfma_f32_16x16x32_bf16 v[200:203], v[52:55], v[176:179], v[200:203]\n"
    "ds_read_b64_tr_b16 v[92:93], v39 offset:36864\n"
    "v_mfma_f32_16x16x32_bf16 v[208:211], v[56:59], v[176:179], v[208:211]\n"
    "ds_read_b64_tr_b16 v[94:95], v39 offset:38912\n"
    "ds_read_b64_tr_b16 v[96:97], v40 offset:36864\n"
    "v_mul_f32 v152, v24, v152\n"
    "v_mul_f32 v153, v24, v153\n"
    "v_mfma_f32_16x16x32_bf16 v[216:219], v[60:63], v[176:179], v[216:219]\n"
    "ds_read_b64_tr_b16 v[98:99], v40 offset:38912\n"
    "v_mul_f32 v154, v24, v154\n"
    "v_mul_f32 v155, v24, v155\n"
    "v_mfma_f32_16x16x32_bf16 v[224:227], v[64:67], v[176:179], v[224:227]\n"
    "ds_read_b64_tr_b16 v[100:101], v41 offset:36864\n"
    "ds_read_b64_tr_b16 v[102:103], v41 offset:38912\n"
    "v_mul_f32 v156, v24, v156\n"
    "v_mul_f32 v157, v24, v157\n"
    "v_mfma_f32_16x16x32_bf16 v[232:235], v[68:71], v[176:179], v[232:235]\n"
    "ds_read_b64_tr_b16 v[104:105], v42 offset:36864\n"
    "v_mul_f32 v158, v24, v158\n"
    "v_mfma_f32_16x16x32_bf16 v[240:243], v[72:75], v[176:179], v[240:243]\n"
    "ds_read_b64_tr_b16 v[106:107], v42 offset:38912\n"
    "ds_read_b64_tr_b16 v[108:109], v43 offset:36864\n"
    "v_mul_f32 v159, v24, v159\n"
    "v_cvt_pk_bf16_f32 v184, v152, v153\n"
    "v_mfma_f32_16x16x32_bf16 v[248:251], v[76:79], v[176:179], v[248:251]\n"
    "ds_read_b64_tr_b16 v[110:111], v43 offset:38912\n"
    "v_cvt_pk_bf16_f32 v185, v154, v155\n"
    "v_cvt_pk_bf16_f32 v186, v156, v157\n"
    "v_mfma_f32_16x16x32_bf16 v[196:199], v[48:51], v[180:183], v[196:199]\n"
    "v_cvt_pk_bf16_f32 v187, v158, v159\n"
    "v_mfma_f32_16x16x32_bf16 v[204:207], v[52:55], v[180:183], v[204:207]\n"
    "v_mul_f32 v164, v25, v164\n"
    "v_mul_f32 v165, v25, v165\n"
    "v_mfma_f32_16x16x32_bf16 v[212:215], v[56:59], v[180:183], v[212:215]\n"
    "v_mul_f32 v166, v25, v166\n"
    "v_mul_f32 v167, v25, v167\n"
    "v_mfma_f32_16x16x32_bf16 v[220:223], v[60:63], v[180:183], v[220:223]\n"
    "v_mul_f32 v168, v25, v168\n"
    "v_mul_f32 v169, v25, v169\n"
    "v_mfma_f32_16x16x32_bf16 v[228:231], v[64:67], v[180:183], v[228:231]\n"
    "v_mul_f32 v170, v25, v170\n"
    "v_mfma_f32_16x16x32_bf16 v[236:239], v[68:71], v[180:183], v[236:239]\n"
    "v_mul_f32 v171, v25, v171\n"
    "v_cvt_pk_bf16_f32 v188, v164, v165\n"
    "s_waitcnt vmcnt(10) lgkmcnt(0)\n"
    "s_barrier\n"
    "v_mfma_f32_16x16x32_bf16 v[244:247], v[72:75], v[180:183], v[244:247]\n"
    "v_cvt_pk_bf16_f32 v189, v166, v167\n"
    "v_cvt_pk_bf16_f32 v190, v168, v169\n"
    "v_mfma_f32_16x16x32_bf16 v[252:255], v[76:79], v[180:183], v[252:255]\n"
    "v_cvt_pk_bf16_f32 v191, v170, v171\n"
    "v_mfma_f32_16x16x32_bf16 v[160:163], v[120:123], v[124:127], v[4:7]\n"
    "ds_read_b128 v[140:143], v45 offset:1920\n"
    "ds_read_b128 v[132:135], v44 offset:28672\n"
    "v_mfma_f32_16x16x32_bf16 v[172:175], v[120:123], v[128:131], v[4:7]\n"
    "ds_read_b128 v[136:139], v44 offset:29696\n"
    "v_mfma_f32_16x16x32_bf16 v[152:155], v[112:115], v[124:127], 0\n"
    "ds_read_b128 v[144:147], v34 offset:49152\n"
    "ds_read_b128 v[148:151], v34 offset:50176\n"
    "v_mfma_f32_16x16x32_bf16 v[156:159], v[116:119], v[124:127], 0\n"
    "ds_read_b64_tr_b16 v[48:49], v36 offset:49152\n"
    "v_mfma_f32_16x16x32_bf16 v[164:167], v[112:115], v[128:131], 0\n"
    "ds_read_b64_tr_b16 v[50:51], v36 offset:51200\n"
    "ds_read_b64_tr_b16 v[52:53], v37 offset:49152\n"
    "v_mfma_f32_16x16x32_bf16 v[168:171], v[116:119], v[128:131], 0\n"
    "ds_read_b64_tr_b16 v[54:55], v37 offset:51200\n"
    "v_rcp_f32 v24, v160\n"
    "v_mfma_f32_16x16x32_bf16 v[192:195], v[80:83], v[184:187], v[192:195]\n"
    "ds_read_b64_tr_b16 v[56:57], v38 offset:49152\n"
    "ds_read_b64_tr_b16 v[58:59], v38 offset:51200\n"
    "v_rcp_f32 v25, v172\n"
    "v_mfma_f32_16x16x32_bf16 v[200:203], v[84:87], v[184:187], v[200:203]\n"
    "ds_read_b64_tr_b16 v[60:61], v39 offset:49152\n"
    "v_mfma_f32_16x16x32_bf16 v[208:211], v[88:91], v[184:187], v[208:211]\n"
    "ds_read_b64_tr_b16 v[62:63], v39 offset:51200\n"
    "ds_read_b64_tr_b16 v[64:65], v40 offset:49152\n"
    "v_mul_f32 v152, v24, v152\n"
    "v_mul_f32 v153, v24, v153\n"
    "v_mfma_f32_16x16x32_bf16 v[216:219], v[92:95], v[184:187], v[216:219]\n"
    "ds_read_b64_tr_b16 v[66:67], v40 offset:51200\n"
    "v_mul_f32 v154, v24, v154\n"
    "v_mul_f32 v155, v24, v155\n"
    "v_mfma_f32_16x16x32_bf16 v[224:227], v[96:99], v[184:187], v[224:227]\n"
    "ds_read_b64_tr_b16 v[68:69], v41 offset:49152\n"
    "ds_read_b64_tr_b16 v[70:71], v41 offset:51200\n"
    "v_mul_f32 v156, v24, v156\n"
    "v_mul_f32 v157, v24, v157\n"
    "v_mfma_f32_16x16x32_bf16 v[232:235], v[100:103], v[184:187], v[232:235]\n"
    "ds_read_b64_tr_b16 v[72:73], v42 offset:49152\n"
    "v_mul_f32 v158, v24, v158\n"
    "v_mfma_f32_16x16x32_bf16 v[240:243], v[104:107], v[184:187], v[240:243]\n"
    "ds_read_b64_tr_b16 v[74:75], v42 offset:51200\n"
    "ds_read_b64_tr_b16 v[76:77], v43 offset:49152\n"
    "v_mul_f32 v159, v24, v159\n"
    "v_cvt_pk_bf16_f32 v176, v152, v153\n"
    "v_mfma_f32_16x16x32_bf16 v[248:251], v[108:111], v[184:187], v[248:251]\n"
    "ds_read_b64_tr_b16 v[78:79], v43 offset:51200\n"
    "v_cvt_pk_bf16_f32 v177, v154, v155\n"
    "v_cvt_pk_bf16_f32 v178, v156, v157\n"
    "v_mfma_f32_16x16x32_bf16 v[196:199], v[80:83], v[188:191], v[196:199]\n"
    "v_cvt_pk_bf16_f32 v179, v158, v159\n"
    "v_mfma_f32_16x16x32_bf16 v[204:207], v[84:87], v[188:191], v[204:207]\n"
    "v_mul_f32 v164, v25, v164\n"
    "v_mul_f32 v165, v25, v165\n"
    "v_mfma_f32_16x16x32_bf16 v[212:215], v[88:91], v[188:191], v[212:215]\n"
    "v_mul_f32 v166, v25, v166\n"
    "v_mul_f32 v167, v25, v167\n"
    "v_mfma_f32_16x16x32_bf16 v[220:223], v[92:95], v[188:191], v[220:223]\n"
    "v_mul_f32 v168, v25, v168\n"
    "v_mul_f32 v169, v25, v169\n"
    "v_mfma_f32_16x16x32_bf16 v[228:231], v[96:99], v[188:191], v[228:231]\n"
    "v_mul_f32 v170, v25, v170\n"
    "v_mfma_f32_16x16x32_bf16 v[236:239], v[100:103], v[188:191], v[236:239]\n"
    "v_mul_f32 v171, v25, v171\n"
    "v_cvt_pk_bf16_f32 v180, v164, v165\n"
    "s_waitcnt vmcnt(6) lgkmcnt(0)\n"
    "s_barrier\n"
    "v_mfma_f32_16x16x32_bf16 v[244:247], v[104:107], v[188:191], v[244:247]\n"
    "v_cvt_pk_bf16_f32 v181, v166, v167\n"
    "v_cvt_pk_bf16_f32 v182, v168, v169\n"
    "v_mfma_f32_16x16x32_bf16 v[252:255], v[108:111], v[188:191], v[252:255]\n"
    "v_cvt_pk_bf16_f32 v183, v170, v171\n"
    "v_mfma_f32_16x16x32_bf16 v[160:163], v[140:143], v[144:147], v[4:7]\n"
    "ds_read_b64_tr_b16 v[80:81], v36 offset:0\n"
    "ds_read_b64_tr_b16 v[82:83], v36 offset:2048\n"
    "v_mfma_f32_16x16x32_bf16 v[172:175], v[140:143], v[148:151], v[4:7]\n"
    "ds_read_b64_tr_b16 v[84:85], v37 offset:0\n"
    "v_mfma_f32_16x16x32_bf16 v[152:155], v[132:135], v[144:147], 0\n"
    "ds_read_b64_tr_b16 v[86:87], v37 offset:2048\n"
    "v_mfma_f32_16x16x32_bf16 v[156:159], v[136:139], v[144:147], 0\n"
    "ds_read_b64_tr_b16 v[88:89], v38 offset:0\n"
    "v_mfma_f32_16x16x32_bf16 v[164:167], v[132:135], v[148:151], 0\n"
    "ds_read_b64_tr_b16 v[90:91], v38 offset:2048\n"
    "v_mfma_f32_16x16x32_bf16 v[168:171], v[136:139], v[148:151], 0\n"
    "ds_read_b64_tr_b16 v[92:93], v39 offset:0\n"
    "v_rcp_f32 v24, v160\n"
    "v_mfma_f32_16x16x32_bf16 v[192:195], v[48:51], v[176:179], v[192:195]\n"
    "ds_read_b64_tr_b16 v[94:95], v39 offset:2048\n"
    "v_rcp_f32 v25, v172\n"
    "v_mfma_f32_16x16x32_bf16 v[200:203], v[52:55], v[176:179], v[200:203]\n"
    "ds_read_b64_tr_b16 v[96:97], v40 offset:0\n"
    "ds_read_b64_tr_b16 v[98:99], v40 offset:2048\n"
    "v_mfma_f32_16x16x32_bf16 v[208:211], v[56:59], v[176:179], v[208:211]\n"
    "ds_read_b64_tr_b16 v[100:101], v41 offset:0\n"
    "v_mul_f32 v152, v24, v152\n"
    "v_mul_f32 v153, v24, v153\n"
    "v_mfma_f32_16x16x32_bf16 v[216:219], v[60:63], v[176:179], v[216:219]\n"
    "ds_read_b64_tr_b16 v[102:103], v41 offset:2048\n"
    "v_mul_f32 v154, v24, v154\n"
    "v_mul_f32 v155, v24, v155\n"
    "v_mfma_f32_16x16x32_bf16 v[224:227], v[64:67], v[176:179], v[224:227]\n"
    "ds_read_b64_tr_b16 v[104:105], v42 offset:0\n"
    "v_mul_f32 v156, v24, v156\n"
    "v_mul_f32 v157, v24, v157\n"
    "v_mfma_f32_16x16x32_bf16 v[232:235], v[68:71], v[176:179], v[232:235]\n"
    "ds_read_b64_tr_b16 v[106:107], v42 offset:2048\n"
    "v_mul_f32 v158, v24, v158\n"
    "v_mfma_f32_16x16x32_bf16 v[240:243], v[72:75], v[176:179], v[240:243]\n"
    "ds_read_b64_tr_b16 v[108:109], v43 offset:0\n"
    "v_mul_f32 v159, v24, v159\n"
    "v_cvt_pk_bf16_f32 v184, v152, v153\n"
    "v_mfma_f32_16x16x32_bf16 v[248:251], v[76:79], v[176:179], v[248:251]\n"
    "ds_read_b64_tr_b16 v[110:111], v43 offset:2048\n"
    "v_cvt_pk_bf16_f32 v185, v154, v155\n"
    "v_cvt_pk_bf16_f32 v186, v156, v157\n"
    "v_mfma_f32_16x16x32_bf16 v[196:199], v[48:51], v[180:183], v[196:199]\n"
    "v_cvt_pk_bf16_f32 v187, v158, v159\n"
    "v_mfma_f32_16x16x32_bf16 v[204:207], v[52:55], v[180:183], v[204:207]\n"
    "v_mul_f32 v164, v25, v164\n"
    "v_mul_f32 v165, v25, v165\n"
    "v_mfma_f32_16x16x32_bf16 v[212:215], v[56:59], v[180:183], v[212:215]\n"
    "v_mul_f32 v166, v25, v166\n"
    "v_mul_f32 v167, v25, v167\n"
    "v_mfma_f32_16x16x32_bf16 v[220:223], v[60:63], v[180:183], v[220:223]\n"
    "v_mul_f32 v168, v25, v168\n"
    "v_mul_f32 v169, v25, v169\n"
    "v_mfma_f32_16x16x32_bf16 v[228:231], v[64:67], v[180:183], v[228:231]\n"
    "v_mul_f32 v170, v25, v170\n"
    "v_mfma_f32_16x16x32_bf16 v[236:239], v[68:71], v[180:183], v[236:239]\n"
    "v_mul_f32 v171, v25, v171\n"
    "v_cvt_pk_bf16_f32 v188, v164, v165\n"
    "s_waitcnt lgkmcnt(0)\n"
    "v_mfma_f32_16x16x32_bf16 v[244:247], v[72:75], v[180:183], v[244:247]\n"
    "v_cvt_pk_bf16_f32 v189, v166, v167\n"
    "v_cvt_pk_bf16_f32 v190, v168, v169\n"
    "v_mfma_f32_16x16x32_bf16 v[252:255], v[76:79], v[180:183], v[252:255]\n"
    "v_cvt_pk_bf16_f32 v191, v170, v171\n"
    "v_mfma_f32_16x16x32_bf16 v[192:195], v[80:83], v[184:187], v[192:195]\n"
    "v_mfma_f32_16x16x32_bf16 v[200:203], v[84:87], v[184:187], v[200:203]\n"
    "v_mfma_f32_16x16x32_bf16 v[208:211], v[88:91], v[184:187], v[208:211]\n"
    "v_mfma_f32_16x16x32_bf16 v[216:219], v[92:95], v[184:187], v[216:219]\n"
    "v_mfma_f32_16x16x32_bf16 v[224:227], v[96:99], v[184:187], v[224:227]\n"
    "v_mfma_f32_16x16x32_bf16 v[232:235], v[100:103], v[184:187], v[232:235]\n"
    "v_mfma_f32_16x16x32_bf16 v[240:243], v[104:107], v[184:187], v[240:243]\n"
    "v_mfma_f32_16x16x32_bf16 v[248:251], v[108:111], v[184:187], v[248:251]\n"
    "v_mfma_f32_16x16x32_bf16 v[196:199], v[80:83], v[188:191], v[196:199]\n"
    "v_mfma_f32_16x16x32_bf16 v[204:207], v[84:87], v[188:191], v[204:207]\n"
    "v_mfma_f32_16x16x32_bf16 v[212:215], v[88:91], v[188:191], v[212:215]\n"
    "v_mfma_f32_16x16x32_bf16 v[220:223], v[92:95], v[188:191], v[220:223]\n"
    "v_mfma_f32_16x16x32_bf16 v[228:231], v[96:99], v[188:191], v[228:231]\n"
    "v_mfma_f32_16x16x32_bf16 v[236:239], v[100:103], v[188:191], v[236:239]\n"
    "v_mfma_f32_16x16x32_bf16 v[244:247], v[104:107], v[188:191], v[244:247]\n"
    "v_mfma_f32_16x16x32_bf16 v[252:255], v[108:111], v[188:191], v[252:255]\n"
    "s_branch 40f\n"
    "22:\n"
    "s_sub_u32 s57, s57, 1\n"
    "s_cbranch_scc1 32b\n"
    "s_sleep 2\n"
    "global_load_dwordx4 v[8:11], v46, s[52:53] offset:0 sc1\n"
    "global_load_dwordx4 v[12:15], v46, s[52:53] offset:1024 sc1\n"
    "global_load_dwordx4 v[16:19], v46, s[52:53] offset:2048 sc1\n"
    "global_load_dwordx4 v[20:23], v46, s[52:53] offset:3072 sc1\n"
    "s_waitcnt vmcnt(0)\n"
    "s_branch 12b\n"
    "23:\n"
    "s_sub_u32 s57, s57, 1\n"
    "s_cbranch_scc1 33b\n"
    "s_sleep 2\n"
    "global_load_dwordx4 v[8:11], v46, s[52:53] offset:0 sc1\n"
    "global_load_dwordx4 v[12:15], v46, s[52:53] offset:1024 sc1\n"
    "global_load_dwordx4 v[16:19], v46, s[52:53] offset:2048 sc1\n"
    "global_load_dwordx4 v[20:23], v46, s[52:53] offset:3072 sc1\n"
    "s_waitcnt vmcnt(0)\n"
    "s_branch 13b\n"
    "40:\n"
    "s_waitcnt vmcnt(2)\n"
    "s_nop 7\n"
    :
      "=&{v[192:223]}"(keep), "=&{v[224:255]}"(send), "=&{v[8:23]}"(ex), "+{s[40:41]}"(pA), "+{s[42:43]}"(pB), "+{s[52:53]}"(pK)
    : "{v3}"(voffB + 3 * 15360), "{v27}"(voffB + 15360), "{v29}"(voffB + 2 * 15360), "{v28}"(tid * 128), "{v31}"(layer ? 0 : (28672 + min((bx * 256 + tid) >> 1, 28671u)) * 128), "{s[72:73]}"(A.wu), "{v32}"(voffA), "{v33}"(voffB),
      "{v34}"(rb + voffA), "{v35}"(voffA + 8192), "{v36}"(bk + (0 ^ sw) * 32), "{v37}"(bk + (1 ^ sw) * 32), "{v38}"(bk + (2 ^ sw) * 32), "{v39}"(bk + (3 ^ sw) * 32),
      "{v40}"(bs + (0 ^ sw) * 32), "{v41}"(bs + (1 ^ sw) * 32), "{v42}"(bs + (2 ^ sw) * 32), "{v43}"(bs + (3 ^ sw) * 32),
      "{v44}"(lb + L_KV + gs * 2048 + lane * 16), "{v45}"(lb + L_Z + gs * 64 + g * 16),
      "{v46}"(lane * 16 + w * 4096), "{s44}"(rb + wl * 2048), "{s45}"(rb + 4096 + wl * 4096), "{s46}"(lb + L_KV + w * 4096), "{s47}"(lb + L_SCR), "{s[48:49]}"(pX)
    : "memory", "scc", "v4", "v5", "v6", "v7", "v24", "v25", "v26", "v48", "v49", "v50", "v51", "v52", "v53", "v54", "v55", "v56", "v57", "v58", "v59", "v60", "v61", "v62", "v63", "v64", "v65", "v66", "v67", "v68", "v69", "v70", "v71", "v72", "v73", "v74", "v75", "v76", "v77", "v78", "v79", "v80", "v81", "v82", "v83", "v84", "v85", "v86", "v87", "v88", "v89", "v90", "v91", "v92", "v93", "v94", "v95", "v96", "v97", "v98", "v99", "v100", "v101", "v102", "v103", "v104", "v105", "v106", "v107", "v108", "v109", "v110", "v111", "v112", "v113", "v114", "v115", "v116", "v117", "v118", "v119", "v120", "v121", "v122", "v123", "v124", "v125", "v126", "v127", "v128", "v129", "v130", "v131", "v132", "v133", "v134", "v135", "v136", "v137", "v138", "v139", "v140", "v141", "v142", "v143", "v144", "v145", "v146", "v147", "v148", "v149", "v150", "v151", "v152", "v153", "v154", "v155", "v156", "v157", "v158", "v159", "v160", "v161", "v162", "v163", "v164", "v165", "v166", "v167", "v168", "v169", "v170", "v171", "v172", "v173", "v174", "v175", "v176", "v177", "v178", "v179", "v180", "v181", "v182", "v183", "v184", "v185", "v186", "v187", "v188", "v189", "v190", "v191", "s57", "s70", "s71", "vcc");
  {
  LANE_IDS
#pragma unroll
  for (int f = 0; f < 8; ++f) *(LAS f32x4*)(lds + L_KV + (w * 8 + f) * 1024 + lane * 16) = (f32x4){send[4 * f], send[4 * f + 1], send[4 * f + 2], send[4 * f + 3]};
  wg_barrier();
  f32x4 got[8];
#pragma unroll
  for (int f = 0; f < 8; ++f) got[f] = *(const LAS f32x4*)(lds + L_KV + ((w ^ 2) * 8 + f) * 1024 + lane * 16);
  epi_res<2>([&](int m, int n, int j) { return keep[(n * 2 + m) * 4 + j] + got[n * 2 + m][j]; }, [&](int kf, int m, int i) { return ex[(kf * 2 + m) * 4 + i]; },
             ws, nullptr, nullptr, 2 * layer + 1, 4 * pm + 2 * wl, 2 * pn + gs, lane);
  PUBLISH(stage_gu, NB_3, if (tid < 4) set_word(ws, flag(layer, FL_A + (pm >> 2) * 4 + tid, (pm & 3) * 8 + pn)))
  }
}

DI void phase_gu(const Args& A, LAS char* lds, int layer) {
  int w = A.w; unsigned bx = blockIdx.x; char* ws = A.ws;
  asm volatile("" : "+s"(w), "+s"(bx), "+s"(ws));
  LANE_IDS
  const unsigned lb = (unsigned)(size_t)lds;
  const int xcd = bx & 7, bi = bx >> 3, pm = 4 * (xcd & 1) + (bi & 3), pn = 8 * (xcd >> 1) + (bi >> 2);
  const int wm = w >> 1, wn = w & 1;
  const char* pA = ws + WS_XT + (2 * layer + 1) * XB + (2 * pm + wm) * 32 * 8192;
  const char* pB = (const char*)(wm ? A.wu : A.wg) + pn * 224 + 4 * 229376;
  const char* pS = ws + WS_ST + (2 * layer + 1) * SB + pm * 256 * 8;
  const char* pC = ws + WS_CS + 16 * 3 * D * 4 + pn * 448;
  const unsigned voffA = lane * 16 + wn * 4096;
  unsigned voffB[4];
#pragma unroll
  for (int j = 0; j < 4; ++j) voffB[j] = gu_voffB(lane, wn, j);
  const unsigned dsA = lb + wm * 8192 + lane * 16;
  const unsigned rowo = lb + G3_B + (4 * g + q) * 224;
  const unsigned dsB = rowo + p * 8 + wn * 128, dsBm = rowo + (p & 1) * 8 + (p >> 1) * 7184 + (6 + wn) * 16;
  const unsigned voffC = (tid < 112 ? tid : min(tid, 223) - 112 + F) * 4;
  const unsigned m0A = lb + w * 4096, m0B = lb + G3_B + wm * 7184 + wn * 3072;
  char* pE = ws + WS_XC + (layer * 256 + bx) * 16384;
  char* pR = ws + ((pn & 15) ? WS_R2 + (4 * S + bx * 256) * 4 : WS_R2 + ((layer * 2 + (pn >> 4)) * S + pm * 256) * 4);
  const unsigned tc = lb + L_TAB + 1024, tcw = tc + w * 448 + g * 16;
  unsigned cw0 = tcw, cw1 = tc + (w ^ 2) * 448 + g * 16;
  if (layer) {
    const int part = tid >= 112, col = tid - part * 112, cn = col >= 56, gi = (col - cn * 64) >> 4;
    const bool mix = col >= 48 && col < 64;
    cw0 = tc + (cn * 7 + (mix ? 6 : 2 * gi + part)) * 64 + (mix ? part * 8 + (col & 7) : col & 15) * 4;
    cw1 = tc + ((2 + cn) * 7 + (mix ? 6 : 2 * (2 - gi) + (part ^ (gi == 1)))) * 64 + (mix ? part * 8 + (col & 7) : col & 15) * 4;
    if (tid >= 224) cw0 = cw1 = lb + L_SCR;
  }
  const unsigned ht = WS_HT + layer * HB + (2 * pm + wm) * 112 * 8192;
  const int G0 = 7 * pn + 4 * wn, Gm = 7 * pn + 3;
  u32x16 ia = {voffA, voffB[0], voffB[1], voffB[2], voffB[3], dsA, dsB + wm * 64, dsBm, tid * 8u, voffC, dsB + 32 + wm * 7184, dsB + 32 + (wm ^ 1) * 7184,
               dsB + (wm ^ 1) * 64, tid * 128u, WD_OFF, tid * 16u};
  u32x8 ib; u32x4 ic; u32x2 id = {tcw, lb + L_TAB + (128 * wm + r) * 4};
#pragma unroll
  for (int i = 0; i < 3; ++i) { const int G = G0 + (wm ? 2 - i : i); ic[i] = ht + (G >> 1) * 8192 + (G & 1) * 512 + lane * 8; }
  ic[3] = ht + (Gm >> 1) * 8192 + (Gm & 1) * 512 + (lane >> 5) * 1024 + (32 * wn + (lane & 31)) * 8;
  ib[1] = lb + L_TAB + tid * 4; ib[2] = cw0; ib[3] = cw1; ib[6] = tid * 4;
  f32x16 c[14], cm; f32x32 ce;
  await(w, ws, flag(layer, FL_A + pm * 4 + (pn & 3), lane & 31));
  if (layer == 0) {
    asm volatile(
    "s_nop 4\n"
    "s_add_u32 m0, s44, 0\n"
    "s_nop 0\n"
    "global_load_lds_dwordx4 v2, s[40:41] offset:0 sc1\n"
    "global_load_lds_dwordx4 v2, s[40:41] offset:1024 sc1\n"
    "global_load_lds_dwordx4 v2, s[40:41] offset:2048 sc1\n"
    "global_load_lds_dwordx4 v2, s[40:41] offset:3072 sc1\n"
    "s_add_u32 s40, s40, 8192\n"
    "s_addc_u32 s41, s41, 0\n"
    "s_add_u32 m0, s44, 16384\n"
    "s_nop 0\n"
    "global_load_lds_dwordx4 v2, s[40:41] offset:0 sc1\n"
    "global_load_lds_dwordx4 v2, s[40:41] offset:1024 sc1\n"
    "global_load_lds_dwordx4 v2, s[40:41] offset:2048 sc1\n"
    "global_load_lds_dwordx4 v2, s[40:41] offset:3072 sc1\n"
    "s_add_u32 s40, s40, 8192\n"
    "s_addc_u32 s41, s41, 0\n"
    "s_add_u32 m0, s44, 32768\n"
    "s_nop 0\n"
    "global_load_lds_dwordx4 v2, s[40:41] offset:0 sc1\n"
    "global_load_lds_dwordx4 v2, s[40:41] offset:1024 sc1\n"
    "global_load_lds_dwordx4 v2, s[40:41] offset:2048 sc1\n"
    "global_load_lds_dwordx4 v2, s[40:41] offset:3072 sc1\n"
    "s_add_u32 s40, s40, 8192\n"
    "s_addc_u32 s41, s41, 0\n"
    "v_mov_b32 v18, 0x3f803f80\n"
    "v_accvgpr_write_b32 a120, v18\n"
    "v_accvgpr_write_b32 a121, v18\n"
    "v_accvgpr_write_b32 a122, v18\n"
    "v_accvgpr_write_b32 a123, v18\n"
    "v_accvgpr_write_b32 a124, -1\n"
    "v_accvgpr_write_b32 a125, -1\n"
    "v_accvgpr_write_b32 a126, -1\n"
    "v_accvgpr_write_b32 a127, -1\n"
    "s_waitcnt vmcnt(8)\n"
    "s_barrier\n"
    "ds_read_b64_tr_b16 a[32:33], v8 offset:0\n"
    "ds_read_b64_tr_b16 a[34:35], v8 offset:3584\n"
    "ds_read_b128 a[0:3], v7 offset:0\n"
    "ds_read_b64_tr_b16 a[36:37], v8 offset:7184\n"
    "ds_read_b64_tr_b16 a[38:39], v8 offset:10768\n"
    "ds_read_b128 a[4:7], v7 offset:1024\n"
    "ds_read_b64_tr_b16 a[40:41], v12 offset:0\n"
    "ds_read_b64_tr_b16 a[42:43], v12 offset:3584\n"
    "ds_read_b128 a[8:11], v7 offset:2048\n"
    "ds_read_b64_tr_b16 a[44:45], v13 offset:0\n"
    "ds_read_b64_tr_b16 a[46:47], v13 offset:3584\n"
    "ds_read_b128 a[12:15], v7 offset:3072\n"
    "ds_read_b64_tr_b16 a[48:49], v14 offset:0\n"
    "ds_read_b64_tr_b16 a[50:51], v14 offset:3584\n"
    "ds_read_b128 a[16:19], v7 offset:4096\n"
    "ds_read_b64_tr_b16 a[52:53], v14 offset:7184\n"
    "ds_read_b64_tr_b16 a[54:55], v14 offset:10768\n"
    "ds_read_b128 a[20:23], v7 offset:5120\n"
    "ds_read_b64_tr_b16 a[56:57], v9 offset:0\n"
    "ds_read_b64_tr_b16 a[58:59], v9 offset:3584\n"
    "ds_read_b128 a[24:27], v7 offset:6144\n"
    "ds_read_b128 a[28:31], v7 offset:7168\n"
    "s_add_u32 m0, s44, 49152\n"
    "s_nop 0\n"
    "global_load_lds_dwordx4 v2, s[40:41] offset:0 sc1\n"
    "global_load_lds_dwordx4 v2, s[40:41] offset:1024 sc1\n"
    "global_load_lds_dwordx4 v2, s[40:41] offset:2048 sc1\n"
    "global_load_lds_dwordx4 v2, s[40:41] offset:3072 sc1\n"
    "s_add_u32 s40, s40, 8192\n"
    "s_addc_u32 s41, s41, 0\n"
    "s_waitcnt vmcnt(8) lgkmcnt(0)\n"
    "s_barrier\n"
    "v_mfma_f32_16x16x32_bf16 v[32:35], a[32:35], a[0:3], 0\n"
    "s_add_u32 m0, s44, 0\n"
    "ds_read_b64_tr_b16 a[92:93], v8 offset:14464\n"
    "v_mfma_f32_16x16x32_bf16 v[36:39], a[36:39], a[0:3], 0\n"
    "global_load_lds_dwordx4 v2, s[40:41] offset:0 sc1\n"
    "v_mfma_f32_16x16x32_bf16 v[40:43], a[40:43], a[0:3], 0\n"
    "ds_read_b64_tr_b16 a[94:95], v8 offset:18048\n"
    "v_mfma_f32_16x16x32_bf16 v[44:47], a[44:47], a[0:3], 0\n"
    "v_mfma_f32_16x16x32_bf16 v[48:51], a[48:51], a[0:3], 0\n"
    "ds_read_b128 a[60:63], v7 offset:16384\n"
    "v_mfma_f32_16x16x32_bf16 v[52:55], a[52:55], a[0:3], 0\n"
    "v_mfma_f32_16x16x32_bf16 v[56:59], a[56:59], a[0:3], 0\n"
    "global_load_lds_dwordx4 v2, s[40:41] offset:1024 sc1\n"
    "ds_read_b64_tr_b16 a[96:97], v8 offset:21648\n"
    "v_mfma_f32_16x16x32_bf16 v[60:63], a[32:35], a[4:7], 0\n"
    "v_mfma_f32_16x16x32_bf16 v[64:67], a[36:39], a[4:7], 0\n"
    "ds_read_b64_tr_b16 a[98:99], v8 offset:25232\n"
    "v_mfma_f32_16x16x32_bf16 v[68:71], a[40:43], a[4:7], 0\n"
    "v_mfma_f32_16x16x32_bf16 v[72:75], a[44:47], a[4:7], 0\n"
    "ds_read_b128 a[64:67], v7 offset:17408\n"
    "v_mfma_f32_16x16x32_bf16 v[76:79], a[48:51], a[4:7], 0\n"
    "global_load_lds_dwordx4 v2, s[40:41] offset:2048 sc1\n"
    "v_mfma_f32_16x16x32_bf16 v[80:83], a[52:55], a[4:7], 0\n"
    "v_mfma_f32_16x16x32_bf16 v[84:87], a[56:59], a[4:7], 0\n"
    "ds_read_b64_tr_b16 a[100:101], v12 offset:14464\n"
    "v_mfma_f32_16x16x32_bf16 a[160:163], a[32:35], a[120:123], 0\n"
    "v_mfma_f32_16x16x32_bf16 v[88:91], a[32:35], a[8:11], 0\n"
    "ds_read_b64_tr_b16 a[102:103], v12 offset:18048\n"
    "v_mfma_f32_16x16x32_bf16 v[92:95], a[36:39], a[8:11], 0\n"
    "v_mfma_f32_16x16x32_bf16 v[96:99], a[40:43], a[8:11], 0\n"
    "global_load_lds_dwordx4 v2, s[40:41] offset:3072 sc1\n"
    "s_add_u32 s40, s40, 8192\n"
    "s_addc_u32 s41, s41, 0\n"
    "ds_read_b128 a[68:71], v7 offset:18432\n"
    "v_mfma_f32_16x16x32_bf16 v[100:103], a[44:47], a[8:11], 0\n"
    "v_mfma_f32_16x16x32_bf16 v[104:107], a[48:51], a[8:11], 0\n"
    "ds_read_b64_tr_b16 a[104:105], v13 offset:14464\n"
    "v_mfma_f32_16x16x32_bf16 v[108:111], a[52:55], a[8:11], 0\n"
    "v_mfma_f32_16x16x32_bf16 v[112:115], a[56:59], a[8:11], 0\n"
    "s_add_u32 m0, s45, 0\n"
    "ds_read_b64_tr_b16 a[106:107], v13 offset:18048\n"
    "v_mfma_f32_16x16x32_bf16 v[116:119], a[32:35], a[12:15], 0\n"
    "global_load_lds_dwordx4 v3, s[42:43] offset:0\n"
    "v_mfma_f32_16x16x32_bf16 v[120:123], a[36:39], a[12:15], 0\n"
    "v_mfma_f32_16x16x32_bf16 v[124:127], a[40:43], a[12:15], 0\n"
    "ds_read_b128 a[72:75], v7 offset:19456\n"
    "v_mfma_f32_16x16x32_bf16 v[128:131], a[44:47], a[12:15], 0\n"
    "v_mfma_f32_16x16x32_bf16 v[132:135], a[48:51], a[12:15], 0\n"
    "ds_read_b64_tr_b16 a[108:109], v14 offset:14464\n"
    "v_mfma_f32_16x16x32_bf16 v[136:139], a[52:55], a[12:15], 0\n"
    "v_mfma_f32_16x16x32_bf16 v[140:143], a[56:59], a[12:15], 0\n"
    "global_load_lds_dwordx4 v4, s[42:43] offset:1024\n"
    "ds_read_b64_tr_b16 a[110:111], v14 offset:18048\n"
    "v_mfma_f32_16x16x32_bf16 a[164:167], a[36:39], a[120:123], 0\n"
    "v_mfma_f32_16x16x32_bf16 v[144:147], a[32:35], a[16:19], 0\n"
    "ds_read_b128 a[76:79], v7 offset:20480\n"
    "v_mfma_f32_16x16x32_bf16 v[148:151], a[36:39], a[16:19], 0\n"
    "v_mfma_f32_16x16x32_bf16 v[152:155], a[40:43], a[16:19], 0\n"
    "ds_read_b64_tr_b16 a[112:113], v14 offset:21648\n"
    "v_mfma_f32_16x16x32_bf16 v[156:159], a[44:47], a[16:19], 0\n"
    "global_load_lds_dwordx4 v5, s[42:43] offset:2048\n"
    "v_mfma_f32_16x16x32_bf16 v[160:163], a[48:51], a[16:19], 0\n"
    "ds_read_b64_tr_b16 a[114:115], v14 offset:25232\n"
    "v_mfma_f32_16x16x32_bf16 v[164:167], a[52:55], a[16:19], 0\n"
    "v_mfma_f32_16x16x32_bf16 v[168:171], a[56:59], a[16:19], 0\n"
    "v_mfma_f32_16x16x32_bf16 v[172:175], a[32:35], a[20:23], 0\n"
    "ds_read_b128 a[80:83], v7 offset:21504\n"
    "v_mfma_f32_16x16x32_bf16 v[176:179], a[36:39], a[20:23], 0\n"
    "v_mfma_f32_16x16x32_bf16 v[180:183], a[40:43], a[20:23], 0\n"
    "global_load_lds_dwordx4 v6, s[42:43] offset:3072\n"
    "s_add_u32 s42, s42, 229376\n"
    "s_addc_u32 s43, s43, 0\n"
    "ds_read_b64_tr_b16 a[116:117], v9 offset:14464\n"
    "v_mfma_f32_16x16x32_bf16 v[184:187], a[44:47], a[20:23], 0\n"
    "v_mfma_f32_16x16x32_bf16 v[188:191], a[48:51], a[20:23], 0\n"
    "ds_read_b64_tr_b16 a[118:119], v9 offset:18048\n"
    "v_mfma_f32_16x16x32_bf16 v[192:195], a[52:55], a[20:23], 0\n"
    "v_mfma_f32_16x16x32_bf16 v[196:199], a[56:59], a[20:23], 0\n"
    "ds_read_b128 a[84:87], v7 offset:22528\n"
    "v_mfma_f32_16x16x32_bf16 a[168:171], a[40:43], a[120:123], 0\n"
    "global_store_dwordx4 v17, a[124:127], s[74:75] sc1\n"
    "s_add_u32 s74, s74, 4096\n"
    "s_addc_u32 s75, s75, 0\n"
    "v_mfma_f32_16x16x32_bf16 v[200:203], a[32:35], a[24:27], 0\n"
    "ds_read_b128 a[88:91], v7 offset:23552\n"
    "v_mfma_f32_16x16x32_bf16 v[204:207], a[36:39], a[24:27], 0\n"
    "v_mfma_f32_16x16x32_bf16 v[208:211], a[40:43], a[24:27], 0\n"
    "v_mfma_f32_16x16x32_bf16 v[212:215], a[44:47], a[24:27], 0\n"
    "v_mfma_f32_16x16x32_bf16 v[216:219], a[48:51], a[24:27], 0\n"
    "v_mfma_f32_16x16x32_bf16 v[220:223], a[52:55], a[24:27], 0\n"
    "v_mfma_f32_16x16x32_bf16 v[224:227], a[56:59], a[24:27], 0\n"
    "v_mfma_f32_16x16x32_bf16 v[228:231], a[32:35], a[28:31], 0\n"
    "v_mfma_f32_16x16x32_bf16 v[232:235], a[36:39], a[28:31], 0\n"
    "v_mfma_f32_16x16x32_bf16 v[236:239], a[40:43], a[28:31], 0\n"
    "v_mfma_f32_16x16x32_bf16 v[240:243], a[44:47], a[28:31], 0\n"
    "v_mfma_f32_16x16x32_bf16 v[244:247], a[48:51], a[28:31], 0\n"
    "s_waitcnt vmcnt(13) lgkmcnt(0)\n"
    "s_barrier\n"
    "v_mfma_f32_16x16x32_bf16 v[248:251], a[52:55], a[28:31], 0\n"
    "v_mfma_f32_16x16x32_bf16 v[252:255], a[56:59], a[28:31], 0\n"
    "v_mfma_f32_16x16x32_bf16 a[172:175], a[56:59], a[120:123], 0\n"
    "v_mfma_f32_16x16x32_bf16 v[32:35], a[92:95], a[60:63], v[32:35]\n"
    "s_add_u32 m0, s44, 16384\n"
    "ds_read_b64_tr_b16 a[32:33], v8 offset:28928\n"
    "v_mfma_f32_16x16x32_bf16 v[36:39], a[96:99], a[60:63], v[36:39]\n"
    "global_load_lds_dwordx4 v2, s[40:41] offset:0 sc1\n"
    "v_mfma_f32_16x16x32_bf16 v[40:43], a[100:103], a[60:63], v[40:43]\n"
    "ds_read_b64_tr_b16 a[34:35], v8 offset:32512\n"
    "v_mfma_f32_16x16x32_bf16 v[44:47], a[104:107], a[60:63], v[44:47]\n"
    "v_mfma_f32_16x16x32_bf16 v[48:51], a[108:111], a[60:63], v[48:51]\n"
    "ds_read_b128 a[0:3], v7 offset:32768\n"
    "v_mfma_f32_16x16x32_bf16 v[52:55], a[112:115], a[60:63], v[52:55]\n"
    "v_mfma_f32_16x16x32_bf16 v[56:59], a[116:119], a[60:63], v[56:59]\n"
    "global_load_lds_dwordx4 v2, s[40:41] offset:1024 sc1\n"
    "ds_read_b64_tr_b16 a[36:37], v8 offset:36112\n"
    "v_mfma_f32_16x16x32_bf16 v[60:63], a[92:95], a[64:67], v[60:63]\n"
    "v_mfma_f32_16x16x32_bf16 v[64:67], a[96:99], a[64:67], v[64:67]\n"
    "ds_read_b64_tr_b16 a[38:39], v8 offset:39696\n"
    "v_mfma_f32_16x16x32_bf16 v[68:71], a[100:103], a[64:67], v[68:71]\n"
    "v_mfma_f32_16x16x32_bf16 v[72:75], a[104:107], a[64:67], v[72:75]\n"
    "ds_read_b128 a[4:7], v7 offset:33792\n"
    "v_mfma_f32_16x16x32_bf16 v[76:79], a[108:111], a[64:67], v[76:79]\n"
    "global_load_lds_dwordx4 v2, s[40:41] offset:2048 sc1\n"
    "v_mfma_f32_16x16x32_bf16 v[80:83], a[112:115], a[64:67], v[80:83]\n"
    "v_mfma_f32_16x16x32_bf16 v[84:87], a[116:119], a[64:67], v[84:87]\n"
    "ds_read_b64_tr_b16 a[40:41], v12 offset:28928\n"
    "v_mfma_f32_16x16x32_bf16 a[160:163], a[92:95], a[120:123], a[160:163]\n"
    "v_mfma_f32_16x16x32_bf16 v[88:91], a[92:95], a[68:71], v[88:91]\n"
    "ds_read_b64_tr_b16 a[42:43], v12 offset:32512\n"
    "v_mfma_f32_16x16x32_bf16 v[92:95], a[96:99], a[68:71], v[92:95]\n"
    "v_mfma_f32_16x16x32_bf16 v[96:99], a[100:103], a[68:71], v[96:99]\n"
    "global_load_lds_dwordx4 v2, s[40:41] offset:3072 sc1\n"
    "s_add_u32 s40, s40, 8192\n"
    "s_addc_u32 s41, s41, 0\n"
    "ds_read_b128 a[8:11], v7 offset:34816\n"
    "v_mfma_f32_16x16x32_bf16 v[100:103], a[104:107], a[68:71], v[100:103]\n"
    "v_mfma_f32_16x16x32_bf16 v[104:107], a[108:111], a[68:71], v[104:107]\n"
    "ds_read_b64_tr_b16 a[44:45], v13 offset:28928\n"
    "v_mfma_f32_16x16x32_bf16 v[108:111], a[112:115], a[68:71], v[108:111]\n"
    "v_mfma_f32_16x16x32_bf16 v[112:115], a[116:119], a[68:71], v[112:115]\n"
    "s_add_u32 m0, s45, 14464\n"
    "ds_read_b64_tr_b16 a[46:47], v13 offset:32512\n"
    "v_mfma_f32_16x16x32_bf16 v[116:119], a[92:95], a[72:75], v[116:119]\n"
    "global_load_lds_dwordx4 v3, s[42:43] offset:0\n"
    "v_mfma_f32_16x16x32_bf16 v[120:123], a[96:99], a[72:75], v[120:123]\n"
    "v_mfma_f32_16x16x32_bf16 v[124:127], a[100:103], a[72:75], v[124:127]\n"
    "ds_read_b128 a[12:15], v7 offset:35840\n"
    "v_mfma_f32_16x16x32_bf16 v[128:131], a[104:107], a[72:75], v[128:131]\n"
    "v_mfma_f32_16x16x32_bf16 v[132:135], a[108:111], a[72:75], v[132:135]\n"
    "ds_read_b64_tr_b16 a[48:49], v14 offset:28928\n"
    "v_mfma_f32_16x16x32_bf16 v[136:139], a[112:115], a[72:75], v[136:139]\n"
    "v_mfma_f32_16x16x32_bf16 v[140:143], a[116:119], a[72:75], v[140:143]\n"
    "global_load_lds_dwordx4 v4, s[42:43] offset:1024\n"
    "ds_read_b64_tr_b16 a[50:51], v14 offset:32512\n"
    "v_mfma_f32_16x16x32_bf16 a[164:167], a[96:99], a[120:123], a[164:167]\n"
    "v_mfma_f32_16x16x32_bf16 v[144:147], a[92:95], a[76:79], v[144:147]\n"
    "ds_read_b128 a[16:19], v7 offset:36864\n"
    "v_mfma_f32_16x16x32_bf16 v[148:151], a[96:99], a[76:79], v[148:151]\n"
    "v_mfma_f32_16x16x32_bf16 v[152:155], a[100:103], a[76:79], v[152:155]\n"
    "ds_read_b64_tr_b16 a[52:53], v14 offset:36112\n"
    "v_mfma_f32_16x16x32_bf16 v[156:159], a[104:107], a[76:79], v[156:159]\n"
    "global_load_lds_dwordx4 v5, s[42:43] offset:2048\n"
    "v_mfma_f32_16x16x32_bf16 v[160:163], a[108:111], a[76:79], v[160:163]\n"
    "ds_read_b64_tr_b16 a[54:55], v14 offset:39696\n"
    "v_mfma_f32_16x16x32_bf16 v[164:167], a[112:115], a[76:79], v[164:167]\n"
    "v_mfma_f32_16x16x32_bf16 v[168:171], a[116:119], a[76:79], v[168:171]\n"
    "v_mfma_f32_16x16x32_bf16 v[172:175], a[92:95], a[80:83], v[172:175]\n"
    "ds_read_b128 a[20:23], v7 offset:37888\n"
    "v_mfma_f32_16x16x32_bf16 v[176:179], a[96:99], a[80:83], v[176:179]\n"
    "v_mfma_f32_16x16x32_bf16 v[180:183], a[100:103], a[80:83], v[180:183]\n"
    "global_load_lds_dwordx4 v6, s[42:43] offset:3072\n"
    "s_add_u32 s42, s42, 229376\n"
    "s_addc_u32 s43, s43, 0\n"
    "ds_read_b64_tr_b16 a[56:57], v9 offset:28928\n"
    "v_mfma_f32_16x16x32_bf16 v[184:187], a[104:107], a[80:83], v[184:187]\n"
    "v_mfma_f32_16x16x32_bf16 v[188:191], a[108:111], a[80:83], v[188:191]\n"
    "ds_read_b64_tr_b16 a[58:59], v9 offset:32512\n"
    "v_mfma_f32_16x16x32_bf16 v[192:195], a[112:115], a[80:83], v[192:195]\n"
    "v_mfma_f32_16x16x32_bf16 v[196:199], a[116:119], a[80:83], v[196:199]\n"
    "ds_read_b128 a[24:27], v7 offset:38912\n"
    "v_mfma_f32_16x16x32_bf16 a[168:171], a[100:103], a[120:123], a[168:171]\n"
    "global_store_dwordx4 v17, a[124:127], s[74:75] sc1\n"
    "s_add_u32 s74, s74, 4096\n"
    "s_addc_u32 s75, s75, 0\n"
    "v_mfma_f32_16x16x32_bf16 v[200:203], a[92:95], a[84:87], v[200:203]\n"
    "ds_read_b128 a[28:31], v7 offset:39936\n"
    "v_mfma_f32_16x16x32_bf16 v[204:207], a[96:99], a[84:87], v[204:207]\n"
    "v_mfma_f32_16x16x32_bf16 v[208:211], a[100:103], a[84:87], v[208:211]\n"
    "v_mfma_f32_16x16x32_bf16 v[212:215], a[104:107], a[84:87], v[212:215]\n"
    "v_mfma_f32_16x16x32_bf16 v[216:219], a[108:111], a[84:87], v[216:219]\n"
    "v_mfma_f32_16x16x32_bf16 v[220:223], a[112:115], a[84:87], v[220:223]\n"
    "v_mfma_f32_16x16x32_bf16 v[224:227], a[116:119], a[84:87], v[224:227]\n"
    "v_mfma_f32_16x16x32_bf16 v[228:231], a[92:95], a[88:91], v[228:231]\n"
    "v_mfma_f32_16x16x32_bf16 v[232:235], a[96:99], a[88:91], v[232:235]\n"
    "v_mfma_f32_16x16x32_bf16 v[236:239], a[100:103], a[88:91], v[236:239]\n"
    "v_mfma_f32_16x16x32_bf16 v[240:243], a[104:107], a[88:91], v[240:243]\n"
    "v_mfma_f32_16x16x32_bf16 v[244:247], a[108:111], a[88:91], v[244:247]\n"
    "s_waitcnt vmcnt(18) lgkmcnt(0)\n"
    "s_barrier\n"
    "v_mfma_f32_16x16x32_bf16 v[248:251], a[112:115], a[88:91], v[248:251]\n"
    "v_mfma_f32_16x16x32_bf16 v[252:255], a[116:119], a[88:91], v[252:255]\n"
    "v_mfma_f32_16x16x32_bf16 a[172:175], a[116:119], a[120:123], a[172:175]\n"
    "s_mov_b32 s46, 6\n"
    "1:\n"
    "v_mfma_f32_16x16x32_bf16 v[32:35], a[32:35], a[0:3], v[32:35]\n"
    "s_add_u32 m0, s44, 32768\n"
    "ds_read_b64_tr_b16 a[92:93], v8 offset:43392\n"
    "v_mfma_f32_16x16x32_bf16 v[36:39], a[36:39], a[0:3], v[36:39]\n"
    "global_load_lds_dwordx4 v2, s[40:41] offset:0 sc1\n"
    "v_mfma_f32_16x16x32_bf16 v[40:43], a[40:43], a[0:3], v[40:43]\n"
    "ds_read_b64_tr_b16 a[94:95], v8 offset:46976\n"
    "v_mfma_f32_16x16x32_bf16 v[44:47], a[44:47], a[0:3], v[44:47]\n"
    "v_mfma_f32_16x16x32_bf16 v[48:51], a[48:51], a[0:3], v[48:51]\n"
    "ds_read_b128 a[60:63], v7 offset:49152\n"
    "v_mfma_f32_16x16x32_bf16 v[52:55], a[52:55], a[0:3], v[52:55]\n"
    "v_mfma_f32_16x16x32_bf16 v[56:59], a[56:59], a[0:3], v[56:59]\n"
    "ds_read_b64_tr_b16 a[96:97], v8 offset:50576\n"
    "v_mfma_f32_16x16x32_bf16 v[60:63], a[32:35], a[4:7], v[60:63]\n"
    "global_load_lds_dwordx4 v2, s[40:41] offset:1024 sc1\n"
    "v_mfma_f32_16x16x32_bf16 v[64:67], a[36:39], a[4:7], v[64:67]\n"
    "ds_read_b64_tr_b16 a[98:99], v8 offset:54160\n"
    "v_mfma_f32_16x16x32_bf16 v[68:71], a[40:43], a[4:7], v[68:71]\n"
    "v_mfma_f32_16x16x32_bf16 v[72:75], a[44:47], a[4:7], v[72:75]\n"
    "ds_read_b128 a[64:67], v7 offset:50176\n"
    "v_mfma_f32_16x16x32_bf16 v[76:79], a[48:51], a[4:7], v[76:79]\n"
    "v_mfma_f32_16x16x32_bf16 v[80:83], a[52:55], a[4:7], v[80:83]\n"
    "v_mfma_f32_16x16x32_bf16 v[84:87], a[56:59], a[4:7], v[84:87]\n"
    "global_load_lds_dwordx4 v2, s[40:41] offset:2048 sc1\n"
    "ds_read_b64_tr_b16 a[100:101], v12 offset:43392\n"
    "v_mfma_f32_16x16x32_bf16 a[160:163], a[32:35], a[120:123], a[160:163]\n"
    "v_mfma_f32_16x16x32_bf16 v[88:91], a[32:35], a[8:11], v[88:91]\n"
    "ds_read_b64_tr_b16 a[102:103], v12 offset:46976\n"
    "v_mfma_f32_16x16x32_bf16 v[92:95], a[36:39], a[8:11], v[92:95]\n"
    "v_mfma_f32_16x16x32_bf16 v[96:99], a[40:43], a[8:11], v[96:99]\n"
    "ds_read_b128 a[68:71], v7 offset:51200\n"
    "v_mfma_f32_16x16x32_bf16 v[100:103], a[44:47], a[8:11], v[100:103]\n"
    "v_mfma_f32_16x16x32_bf16 v[104:107], a[48:51], a[8:11], v[104:107]\n"
    "global_load_lds_dwordx4 v2, s[40:41] offset:3072 sc1\n"
    "s_add_u32 s40, s40, 8192\n"
    "s_addc_u32 s41, s41, 0\n"
    "ds_read_b64_tr_b16 a[104:105], v13 offset:43392\n"
    "v_mfma_f32_16x16x32_bf16 v[108:111], a[52:55], a[8:11], v[108:111]\n"
    "v_mfma_f32_16x16x32_bf16 v[112:115], a[56:59], a[8:11], v[112:115]\n"
    "ds_read_b64_tr_b16 a[106:107], v13 offset:46976\n"
    "v_mfma_f32_16x16x32_bf16 v[116:119], a[32:35], a[12:15], v[116:119]\n"
    "v_mfma_f32_16x16x32_bf16 v[120:123], a[36:39], a[12:15], v[120:123]\n"
    "v_mfma_f32_16x16x32_bf16 v[124:127], a[40:43], a[12:15], v[124:127]\n"
    "s_add_u32 m0, s45, 28928\n"
    "ds_read_b128 a[72:75], v7 offset:52224\n"
    "v_mfma_f32_16x16x32_bf16 v[128:131], a[44:47], a[12:15], v[128:131]\n"
    "global_load_lds_dwordx4 v3, s[42:43] offset:0\n"
    "v_mfma_f32_16x16x32_bf16 v[132:135], a[48:51], a[12:15], v[132:135]\n"
    "ds_read_b64_tr_b16 a[108:109], v14 offset:43392\n"
    "v_mfma_f32_16x16x32_bf16 v[136:139], a[52:55], a[12:15], v[136:139]\n"
    "v_mfma_f32_16x16x32_bf16 v[140:143], a[56:59], a[12:15], v[140:143]\n"
    "ds_read_b64_tr_b16 a[110:111], v14 offset:46976\n"
    "v_mfma_f32_16x16x32_bf16 a[164:167], a[36:39], a[120:123], a[164:167]\n"
    "v_mfma_f32_16x16x32_bf16 v[144:147], a[32:35], a[16:19], v[144:147]\n"
    "ds_read_b128 a[76:79], v7 offset:53248\n"
    "v_mfma_f32_16x16x32_bf16 v[148:151], a[36:39], a[16:19], v[148:151]\n"
    "global_load_lds_dwordx4 v4, s[42:43] offset:1024\n"
    "v_mfma_f32_16x16x32_bf16 v[152:155], a[40:43], a[16:19], v[152:155]\n"
    "ds_read_b64_tr_b16 a[112:113], v14 offset:50576\n"
    "v_mfma_f32_16x16x32_bf16 v[156:159], a[44:47], a[16:19], v[156:159]\n"
    "v_mfma_f32_16x16x32_bf16 v[160:163], a[48:51], a[16:19], v[160:163]\n"
    "ds_read_b64_tr_b16 a[114:115], v14 offset:54160\n"
    "v_mfma_f32_16x16x32_bf16 v[164:167], a[52:55], a[16:19], v[164:167]\n"
    "v_mfma_f32_16x16x32_bf16 v[168:171], a[56:59], a[16:19], v[168:171]\n"
    "v_mfma_f32_16x16x32_bf16 v[172:175], a[32:35], a[20:23], v[172:175]\n"
    "global_load_lds_dwordx4 v5, s[42:43] offset:2048\n"
    "ds_read_b128 a[80:83], v7 offset:54272\n"
    "v_mfma_f32_16x16x32_bf16 v[176:179], a[36:39], a[20:23], v[176:179]\n"
    "v_mfma_f32_16x16x32_bf16 v[180:183], a[40:43], a[20:23], v[180:183]\n"
    "ds_read_b64_tr_b16 a[116:117], v9 offset:43392\n"
    "v_mfma_f32_16x16x32_bf16 v[184:187], a[44:47], a[20:23], v[184:187]\n"
    "v_mfma_f32_16x16x32_bf16 v[188:191], a[48:51], a[20:23], v[188:191]\n"
    "ds_read_b64_tr_b16 a[118:119], v9 offset:46976\n"
    "v_mfma_f32_16x16x32_bf16 v[192:195], a[52:55], a[20:23], v[192:195]\n"
    "v_mfma_f32_16x16x32_bf16 v[196:199], a[56:59], a[20:23], v[196:199]\n"
    "global_load_lds_dwordx4 v6, s[42:43] offset:3072\n"
    "s_add_u32 s42, s42, 229376\n"
    "s_addc_u32 s43, s43, 0\n"
    "ds_read_b128 a[84:87], v7 offset:55296\n"
    "v_mfma_f32_16x16x32_bf16 a[168:171], a[40:43], a[120:123], a[168:171]\n"
    "v_mfma_f32_16x16x32_bf16 v[200:203], a[32:35], a[24:27], v[200:203]\n"
    "ds_read_b128 a[88:91], v7 offset:56320\n"
    "v_mfma_f32_16x16x32_bf16 v[204:207], a[36:39], a[24:27], v[204:207]\n"
    "v_mfma_f32_16x16x32_bf16 v[208:211], a[40:43], a[24:27], v[208:211]\n"
    "v_mfma_f32_16x16x32_bf16 v[212:215], a[44:47], a[24:27], v[212:215]\n"
    "v_mfma_f32_16x16x32_bf16 v[216:219], a[48:51], a[24:27], v[216:219]\n"
    "v_mfma_f32_16x16x32_bf16 v[220:223], a[52:55], a[24:27], v[220:223]\n"
    "v_mfma_f32_16x16x32_bf16 v[224:227], a[56:59], a[24:27], v[224:227]\n"
    "v_mfma_f32_16x16x32_bf16 v[228:231], a[32:35], a[28:31], v[228:231]\n"
    "v_mfma_f32_16x16x32_bf16 v[232:235], a[36:39], a[28:31], v[232:235]\n"
    "v_mfma_f32_16x16x32_bf16 v[236:239], a[40:43], a[28:31], v[236:239]\n"
    "v_mfma_f32_16x16x32_bf16 v[240:243], a[44:47], a[28:31], v[240:243]\n"
    "v_mfma_f32_16x16x32_bf16 v[244:247], a[48:51], a[28:31], v[244:247]\n"
    "s_waitcnt vmcnt(16) lgkmcnt(0)\n"
    "s_barrier\n"
    "v_mfma_f32_16x16x32_bf16 v[248:251], a[52:55], a[28:31], v[248:251]\n"
    "v_mfma_f32_16x16x32_bf16 v[252:255], a[56:59], a[28:31], v[252:255]\n"
    "v_mfma_f32_16x16x32_bf16 a[172:175], a[56:59], a[120:123], a[172:175]\n"
    "v_mfma_f32_16x16x32_bf16 v[32:35], a[92:95], a[60:63], v[32:35]\n"
    "s_add_u32 m0, s44, 49152\n"
    "ds_read_b64_tr_b16 a[32:33], v8 offset:0\n"
    "v_mfma_f32_16x16x32_bf16 v[36:39], a[96:99], a[60:63], v[36:39]\n"
    "global_load_lds_dwordx4 v2, s[40:41] offset:0 sc1\n"
    "v_mfma_f32_16x16x32_bf16 v[40:43], a[100:103], a[60:63], v[40:43]\n"
    "ds_read_b64_tr_b16 a[34:35], v8 offset:3584\n"
    "v_mfma_f32_16x16x32_bf16 v[44:47], a[104:107], a[60:63], v[44:47]\n"
    "v_mfma_f32_16x16x32_bf16 v[48:51], a[108:111], a[60:63], v[48:51]\n"
    "ds_read_b128 a[0:3], v7 offset:0\n"
    "v_mfma_f32_16x16x32_bf16 v[52:55], a[112:115], a[60:63], v[52:55]\n"
    "v_mfma_f32_16x16x32_bf16 v[56:59], a[116:119], a[60:63], v[56:59]\n"
    "ds_read_b64_tr_b16 a[36:37], v8 offset:7184\n"
    "v_mfma_f32_16x16x32_bf16 v[60:63], a[92:95], a[64:67], v[60:63]\n"
    "global_load_lds_dwordx4 v2, s[40:41] offset:1024 sc1\n"
    "v_mfma_f32_16x16x32_bf16 v[64:67], a[96:99], a[64:67], v[64:67]\n"
    "ds_read_b64_tr_b16 a[38:39], v8 offset:10768\n"
    "v_mfma_f32_16x16x32_bf16 v[68:71], a[100:103], a[64:67], v[68:71]\n"
    "v_mfma_f32_16x16x32_bf16 v[72:75], a[104:107], a[64:67], v[72:75]\n"
    "ds_read_b128 a[4:7], v7 offset:1024\n"
    "v_mfma_f32_16x16x32_bf16 v[76:79], a[108:111], a[64:67], v[76:79]\n"
    "v_mfma_f32_16x16x32_bf16 v[80:83], a[112:115], a[64:67], v[80:83]\n"
    "v_mfma_f32_16x16x32_bf16 v[84:87], a[116:119], a[64:67], v[84:87]\n"
    "global_load_lds_dwordx4 v2, s[40:41] offset:2048 sc1\n"
    "ds_read_b64_tr_b16 a[40:41], v12 offset:0\n"
    "v_mfma_f32_16x16x32_bf16 a[160:163], a[92:95], a[120:123], a[160:163]\n"
    "v_mfma_f32_16x16x32_bf16 v[88:91], a[92:95], a[68:71], v[88:91]\n"
    "ds_read_b64_tr_b16 a[42:43], v12 offset:3584\n"
    "v_mfma_f32_16x16x32_bf16 v[92:95], a[96:99], a[68:71], v[92:95]\n"
    "v_mfma_f32_16x16x32_bf16 v[96:99], a[100:103], a[68:71], v[96:99]\n"
    "ds_read_b128 a[8:11], v7 offset:2048\n"
    "v_mfma_f32_16x16x32_bf16 v[100:103], a[104:107], a[68:71], v[100:103]\n"
    "v_mfma_f32_16x16x32_bf16 v[104:107], a[108:111], a[68:71], v[104:107]\n"
    "global_load_lds_dwordx4 v2, s[40:41] offset:3072 sc1\n"
    "s_add_u32 s40, s40, 8192\n"
    "s_addc_u32 s41, s41, 0\n"
    "ds_read_b64_tr_b16 a[44:45], v13 offset:0\n"
    "v_mfma_f32_16x16x32_bf16 v[108:111], a[112:115], a[68:71], v[108:111]\n"
    "v_mfma_f32_16x16x32_bf16 v[112:115], a[116:119], a[68:71], v[112:115]\n"
    "ds_read_b64_tr_b16 a[46:47], v13 offset:3584\n"
    "v_mfma_f32_16x16x32_bf16 v[116:119], a[92:95], a[72:75], v[116:119]\n"
    "v_mfma_f32_16x16x32_bf16 v[120:123], a[96:99], a[72:75], v[120:123]\n"
    "v_mfma_f32_16x16x32_bf16 v[124:127], a[100:103], a[72:75], v[124:127]\n"
    "s_add_u32 m0, s45, 43392\n"
    "ds_read_b128 a[12:15], v7 offset:3072\n"
    "v_mfma_f32_16x16x32_bf16 v[128:131], a[104:107], a[72:75], v[128:131]\n"
    "global_load_lds_dwordx4 v3, s[42:43] offset:0\n"
    "v_mfma_f32_16x16x32_bf16 v[132:135], a[108:111], a[72:75], v[132:135]\n"
    "ds_read_b64_tr_b16 a[48:49], v14 offset:0\n"
    "v_mfma_f32_16x16x32_bf16 v[136:139], a[112:115], a[72:75], v[136:139]\n"
    "v_mfma_f32_16x16x32_bf16 v[140:143], a[116:119], a[72:75], v[140:143]\n"
    "ds_read_b64_tr_b16 a[50:51], v14 offset:3584\n"
    "v_mfma_f32_16x16x32_bf16 a[164:167], a[96:99], a[120:123], a[164:167]\n"
    "v_mfma_f32_16x16x32_bf16 v[144:147], a[92:95], a[76:79], v[144:147]\n"
    "ds_read_b128 a[16:19], v7 offset:4096\n"
    "v_mfma_f32_16x16x32_bf16 v[148:151], a[96:99], a[76:79], v[148:151]\n"
    "global_load_lds_dwordx4 v4, s[42:43] offset:1024\n"
    "v_mfma_f32_16x16x32_bf16 v[152:155], a[100:103], a[76:79], v[152:155]\n"
    "ds_read_b64_tr_b16 a[52:53], v14 offset:7184\n"
    "v_mfma_f32_16x16x32_bf16 v[156:159], a[104:107], a[76:79], v[156:159]\n"
    "v_mfma_f32_16x16x32_bf16 v[160:163], a[108:111], a[76:79], v[160:163]\n"
    "ds_read_b64_tr_b16 a[54:55], v14 offset:10768\n"
    "v_mfma_f32_16x16x32_bf16 v[164:167], a[112:115], a[76:79], v[164:167]\n"
    "v_mfma_f32_16x16x32_bf16 v[168:171], a[116:119], a[76:79], v[168:171]\n"
    "v_mfma_f32_16x16x32_bf16 v[172:175], a[92:95], a[80:83], v[172:175]\n"
    "global_load_lds_dwordx4 v5, s[42:43] offset:2048\n"
    "ds_read_b128 a[20:23], v7 offset:5120\n"
    "v_mfma_f32_16x16x32_bf16 v[176:179], a[96:99], a[80:83], v[176:179]\n"
    "v_mfma_f32_16x16x32_bf16 v[180:183], a[100:103], a[80:83], v[180:183]\n"
    "ds_read_b64_tr_b16 a[56:57], v9 offset:0\n"
    "v_mfma_f32_16x16x32_bf16 v[184:187], a[104:107], a[80:83], v[184:187]\n"
    "v_mfma_f32_16x16x32_bf16 v[188:191], a[108:111], a[80:83], v[188:191]\n"
    "ds_read_b64_tr_b16 a[58:59], v9 offset:3584\n"
    "v_mfma_f32_16x16x32_bf16 v[192:195], a[112:115], a[80:83], v[192:195]\n"
    "v_mfma_f32_16x16x32_bf16 v[196:199], a[116:119], a[80:83], v[196:199]\n"
    "global_load_lds_dwordx4 v6, s[42:43] offset:3072\n"
    "s_add_u32 s42, s42, 229376\n"
    "s_addc_u32 s43, s43, 0\n"
    "ds_read_b128 a[24:27], v7 offset:6144\n"
    "v_mfma_f32_16x16x32_bf16 a[168:171], a[100:103], a[120:123], a[168:171]\n"
    "v_mfma_f32_16x16x32_bf16 v[200:203], a[92:95], a[84:87], v[200:203]\n"
    "ds_read_b128 a[28:31], v7 offset:7168\n"
    "v_mfma_f32_16x16x32_bf16 v[204:207], a[96:99], a[84:87], v[204:207]\n"
    "v_mfma_f32_16x16x32_bf16 v[208:211], a[100:103], a[84:87], v[208:211]\n"
    "v_mfma_f32_16x16x32_bf16 v[212:215], a[104:107], a[84:87], v[212:215]\n"
    "v_mfma_f32_16x16x32_bf16 v[216:219], a[108:111], a[84:87], v[216:219]\n"
    "v_mfma_f32_16x16x32_bf16 v[220:223], a[112:115], a[84:87], v[220:223]\n"
    "v_mfma_f32_16x16x32_bf16 v[224:227], a[116:119], a[84:87], v[224:227]\n"
    "v_mfma_f32_16x16x32_bf16 v[228:231], a[92:95], a[88:91], v[228:231]\n"
    "v_mfma_f32_16x16x32_bf16 v[232:235], a[96:99], a[88:91], v[232:235]\n"
    "v_mfma_f32_16x16x32_bf16 v[236:239], a[100:103], a[88:91], v[236:239]\n"
    "v_mfma_f32_16x16x32_bf16 v[240:243], a[104:107], a[88:91], v[240:243]\n"
    "v_mfma_f32_16x16x32_bf16 v[244:247], a[108:111], a[88:91], v[244:247]\n"
    "s_waitcnt vmcnt(16) lgkmcnt(0)\n"
    "s_barrier\n"
    "v_mfma_f32_16x16x32_bf16 v[248:251], a[112:115], a[88:91], v[248:251]\n"
    "v_mfma_f32_16x16x32_bf16 v[252:255], a[116:119], a[88:91], v[252:255]\n"
    "v_mfma_f32_16x16x32_bf16 a[172:175], a[116:119], a[120:123], a[172:175]\n"
    "v_mfma_f32_16x16x32_bf16 v[32:35], a[32:35], a[0:3], v[32:35]\n"
    "s_add_u32 m0, s44, 0\n"
    "ds_read_b64_tr_b16 a[92:93], v8 offset:14464\n"
    "v_mfma_f32_16x16x32_bf16 v[36:39], a[36:39], a[0:3], v[36:39]\n"
    "global_load_lds_dwordx4 v2, s[40:41] offset:0 sc1\n"
    "v_mfma_f32_16x16x32_bf16 v[40:43], a[40:43], a[0:3], v[40:43]\n"
    "ds_read_b64_tr_b16 a[94:95], v8 offset:18048\n"
    "v_mfma_f32_16x16x32_bf16 v[44:47], a[44:47], a[0:3], v[44:47]\n"
    "v_mfma_f32_16x16x32_bf16 v[48:51], a[48:51], a[0:3], v[48:51]\n"
    "ds_read_b128 a[60:63], v7 offset:16384\n"
    "v_mfma_f32_16x16x32_bf16 v[52:55], a[52:55], a[0:3], v[52:55]\n"
    "v_mfma_f32_16x16x32_bf16 v[56:59], a[56:59], a[0:3], v[56:59]\n"
    "ds_read_b64_tr_b16 a[96:97], v8 offset:21648\n"
    "v_mfma_f32_16x16x32_bf16 v[60:63], a[32:35], a[4:7], v[60:63]\n"
    "global_load_lds_dwordx4 v2, s[40:41] offset:1024 sc1\n"
    "v_mfma_f32_16x16x32_bf16 v[64:67], a[36:39], a[4:7], v[64:67]\n"
    "ds_read_b64_tr_b16 a[98:99], v8 offset:25232\n"
    "v_mfma_f32_16x16x32_bf16 v[68:71], a[40:43], a[4:7], v[68:71]\n"
    "v_mfma_f32_16x16x32_bf16 v[72:75], a[44:47], a[4:7], v[72:75]\n"
    "ds_read_b128 a[64:67], v7 offset:17408\n"
    "v_mfma_f32_16x16x32_bf16 v[76:79], a[48:51], a[4:7], v[76:79]\n"
    "v_mfma_f32_16x16x32_bf16 v[80:83], a[52:55], a[4:7], v[80:83]\n"
    "v_mfma_f32_16x16x32_bf16 v[84:87], a[56:59], a[4:7], v[84:87]\n"
    "global_load_lds_dwordx4 v2, s[40:41] offset:2048 sc1\n"
    "ds_read_b64_tr_b16 a[100:101], v12 offset:14464\n"
    "v_mfma_f32_16x16x32_bf16 a[160:163], a[32:35], a[120:123], a[160:163]\n"
    "v_mfma_f32_16x16x32_bf16 v[88:91], a[32:35], a[8:11], v[88:91]\n"
    "ds_read_b64_tr_b16 a[102:103], v12 offset:18048\n"
    "v_mfma_f32_16x16x32_bf16 v[92:95], a[36:39], a[8:11], v[92:95]\n"
    "v_mfma_f32_16x16x32_bf16 v[96:99], a[40:43], a[8:11], v[96:99]\n"
    "ds_read_b128 a[68:71], v7 offset:18432\n"
    "v_mfma_f32_16x16x32_bf16 v[100:103], a[44:47], a[8:11], v[100:103]\n"
    "v_mfma_f32_16x16x32_bf16 v[104:107], a[48:51], a[8:11], v[104:107]\n"
    "global_load_lds_dwordx4 v2, s[40:41] offset:3072 sc1\n"
    "s_add_u32 s40, s40, 8192\n"
    "s_addc_u32 s41, s41, 0\n"
    "ds_read_b64_tr_b16 a[104:105], v13 offset:14464\n"
    "v_mfma_f32_16x16x32_bf16 v[108:111], a[52:55], a[8:11], v[108:111]\n"
    "v_mfma_f32_16x16x32_bf16 v[112:115], a[56:59], a[8:11], v[112:115]\n"
    "ds_read_b64_tr_b16 a[106:107], v13 offset:18048\n"
    "v_mfma_f32_16x16x32_bf16 v[116:119], a[32:35], a[12:15], v[116:119]\n"
    "v_mfma_f32_16x16x32_bf16 v[120:123], a[36:39], a[12:15], v[120:123]\n"
    "v_mfma_f32_16x16x32_bf16 v[124:127], a[40:43], a[12:15], v[124:127]\n"
    "s_add_u32 m0, s45, 0\n"
    "ds_read_b128 a[72:75], v7 offset:19456\n"
    "v_mfma_f32_16x16x32_bf16 v[128:131], a[44:47], a[12:15], v[128:131]\n"
    "global_load_lds_dwordx4 v3, s[42:43] offset:0\n"
    "v_mfma_f32_16x16x32_bf16 v[132:135], a[48:51], a[12:15], v[132:135]\n"
    "ds_read_b64_tr_b16 a[108:109], v14 offset:14464\n"
    "v_mfma_f32_16x16x32_bf16 v[136:139], a[52:55], a[12:15], v[136:139]\n"
    "v_mfma_f32_16x16x32_bf16 v[140:143], a[56:59], a[12:15], v[140:143]\n"
    "ds_read_b64_tr_b16 a[110:111], v14 offset:18048\n"
    "v_mfma_f32_16x16x32_bf16 a[164:167], a[36:39], a[120:123], a[164:167]\n"
    "v_mfma_f32_16x16x32_bf16 v[144:147], a[32:35], a[16:19], v[144:147]\n"
    "ds_read_b128 a[76:79], v7 offset:20480\n"
    "v_mfma_f32_16x16x32_bf16 v[148:151], a[36:39], a[16:19], v[148:151]\n"
    "global_load_lds_dwordx4 v4, s[42:43] offset:1024\n"
    "v_mfma_f32_16x16x32_bf16 v[152:155], a[40:43], a[16:19], v[152:155]\n"
    "ds_read_b64_tr_b16 a[112:113], v14 offset:21648\n"
    "v_mfma_f32_16x16x32_bf16 v[156:159], a[44:47], a[16:19], v[156:159]\n"
    "v_mfma_f32_16x16x32_bf16 v[160:163], a[48:51], a[16:19], v[160:163]\n"
    "ds_read_b64_tr_b16 a[114:115], v14 offset:25232\n"
    "v_mfma_f32_16x16x32_bf16 v[164:167], a[52:55], a[16:19], v[164:167]\n"
    "v_mfma_f32_16x16x32_bf16 v[168:171], a[56:59], a[16:19], v[168:171]\n"
    "v_mfma_f32_16x16x32_bf16 v[172:175], a[32:35], a[20:23], v[172:175]\n"
    "global_load_lds_dwordx4 v5, s[42:43] offset:2048\n"
    "ds_read_b128 a[80:83], v7 offset:21504\n"
    "v_mfma_f32_16x16x32_bf16 v[176:179], a[36:39], a[20:23], v[176:179]\n"
    "v_mfma_f32_16x16x32_bf16 v[180:183], a[40:43], a[20:23], v[180:183]\n"
    "ds_read_b64_tr_b16 a[116:117], v9 offset:14464\n"
    "v_mfma_f32_16x16x32_bf16 v[184:187], a[44:47], a[20:23], v[184:187]\n"
    "v_mfma_f32_16x16x32_bf16 v[188:191], a[48:51], a[20:23], v[188:191]\n"
    "ds_read_b64_tr_b16 a[118:119], v9 offset:18048\n"
    "v_mfma_f32_16x16x32_bf16 v[192:195], a[52:55], a[20:23], v[192:195]\n"
    "v_mfma_f32_16x16x32_bf16 v[196:199], a[56:59], a[20:23], v[196:199]\n"
    "global_load_lds_dwordx4 v6, s[42:43] offset:3072\n"
    "s_add_u32 s42, s42, 229376\n"
    "s_addc_u32 s43, s43, 0\n"
    "ds_read_b128 a[84:87], v7 offset:22528\n"
    "v_mfma_f32_16x16x32_bf16 a[168:171], a[40:43], a[120:123], a[168:171]\n"
    "v_mfma_f32_16x16x32_bf16 v[200:203], a[32:35], a[24:27], v[200:203]\n"
    "ds_read_b128 a[88:91], v7 offset:23552\n"
    "v_mfma_f32_16x16x32_bf16 v[204:207], a[36:39], a[24:27], v[204:207]\n"
    "v_mfma_f32_16x16x32_bf16 v[208:211], a[40:43], a[24:27], v[208:211]\n"
    "v_mfma_f32_16x16x32_bf16 v[212:215], a[44:47], a[24:27], v[212:215]\n"
    "v_mfma_f32_16x16x32_bf16 v[216:219], a[48:51], a[24:27], v[216:219]\n"
    "v_mfma_f32_16x16x32_bf16 v[220:223], a[52:55], a[24:27], v[220:223]\n"
    "v_mfma_f32_16x16x32_bf16 v[224:227], a[56:59], a[24:27], v[224:227]\n"
    "v_mfma_f32_16x16x32_bf16 v[228:231], a[32:35], a[28:31], v[228:231]\n"
    "v_mfma_f32_16x16x32_bf16 v[232:235], a[36:39], a[28:31], v[232:235]\n"
    "v_mfma_f32_16x16x32_bf16 v[236:239], a[40:43], a[28:31], v[236:239]\n"
    "v_mfma_f32_16x16x32_bf16 v[240:243], a[44:47], a[28:31], v[240:243]\n"
    "v_mfma_f32_16x16x32_bf16 v[244:247], a[48:51], a[28:31], v[244:247]\n"
    "s_waitcnt vmcnt(16) lgkmcnt(0)\n"
    "s_barrier\n"
    "v_mfma_f32_16x16x32_bf16 v[248:251], a[52:55], a[28:31], v[248:251]\n"
    "v_mfma_f32_16x16x32_bf16 v[252:255], a[56:59], a[28:31], v[252:255]\n"
    "v_mfma_f32_16x16x32_bf16 a[172:175], a[56:59], a[120:123], a[172:175]\n"
    "v_mfma_f32_16x16x32_bf16 v[32:35], a[92:95], a[60:63], v[32:35]\n"
    "s_add_u32 m0, s44, 16384\n"
    "ds_read_b64_tr_b16 a[32:33], v8 offset:28928\n"
    "v_mfma_f32_16x16x32_bf16 v[36:39], a[96:99], a[60:63], v[36:39]\n"
    "global_load_lds_dwordx4 v2, s[40:41] offset:0 sc1\n"
    "v_mfma_f32_16x16x32_bf16 v[40:43], a[100:103], a[60:63], v[40:43]\n"
    "ds_read_b64_tr_b16 a[34:35], v8 offset:32512\n"
    "v_mfma_f32_16x16x32_bf16 v[44:47], a[104:107], a[60:63], v[44:47]\n"
    "v_mfma_f32_16x16x32_bf16 v[48:51], a[108:111], a[60:63], v[48:51]\n"
    "ds_read_b128 a[0:3], v7 offset:32768\n"
    "v_mfma_f32_16x16x32_bf16 v[52:55], a[112:115], a[60:63], v[52:55]\n"
    "v_mfma_f32_16x16x32_bf16 v[56:59], a[116:119], a[60:63], v[56:59]\n"
    "ds_read_b64_tr_b16 a[36:37], v8 offset:36112\n"
    "v_mfma_f32_16x16x32_bf16 v[60:63], a[92:95], a[64:67], v[60:63]\n"
    "global_load_lds_dwordx4 v2, s[40:41] offset:1024 sc1\n"
    "v_mfma_f32_16x16x32_bf16 v[64:67], a[96:99], a[64:67], v[64:67]\n"
    "ds_read_b64_tr_b16 a[38:39], v8 offset:39696\n"
    "v_mfma_f32_16x16x32_bf16 v[68:71], a[100:103], a[64:67], v[68:71]\n"
    "v_mfma_f32_16x16x32_bf16 v[72:75], a[104:107], a[64:67], v[72:75]\n"
    "ds_read_b128 a[4:7], v7 offset:33792\n"
    "v_mfma_f32_16x16x32_bf16 v[76:79], a[108:111], a[64:67], v[76:79]\n"
    "v_mfma_f32_16x16x32_bf16 v[80:83], a[112:115], a[64:67], v[80:83]\n"
    "v_mfma_f32_16x16x32_bf16 v[84:87], a[116:119], a[64:67], v[84:87]\n"
    "global_load_lds_dwordx4 v2, s[40:41] offset:2048 sc1\n"
    "ds_read_b64_tr_b16 a[40:41], v12 offset:28928\n"
    "v_mfma_f32_16x16x32_bf16 a[160:163], a[92:95], a[120:123], a[160:163]\n"
    "v_mfma_f32_16x16x32_bf16 v[88:91], a[92:95], a[68:71], v[88:91]\n"
    "ds_read_b64_tr_b16 a[42:43], v12 offset:32512\n"
    "v_mfma_f32_16x16x32_bf16 v[92:95], a[96:99], a[68:71], v[92:95]\n"
    "v_mfma_f32_16x16x32_bf16 v[96:99], a[100:103], a[68:71], v[96:99]\n"
    "ds_read_b128 a[8:11], v7 offset:34816\n"
    "v_mfma_f32_16x16x32_bf16 v[100:103], a[104:107], a[68:71], v[100:103]\n"
    "v_mfma_f32_16x16x32_bf16 v[104:107], a[108:111], a[68:71], v[104:107]\n"
    "global_load_lds_dwordx4 v2, s[40:41] offset:3072 sc1\n"
    "s_add_u32 s40, s40, 8192\n"
    "s_addc_u32 s41, s41, 0\n"
    "ds_read_b64_tr_b16 a[44:45], v13 offset:28928\n"
    "v_mfma_f32_16x16x32_bf16 v[108:111], a[112:115], a[68:71], v[108:111]\n"
    "v_mfma_f32_16x16x32_bf16 v[112:115], a[116:119], a[68:71], v[112:115]\n"
    "ds_read_b64_tr_b16 a[46:47], v13 offset:32512\n"
    "v_mfma_f32_16x16x32_bf16 v[116:119], a[92:95], a[72:75], v[116:119]\n"
    "v_mfma_f32_16x16x32_bf16 v[120:123], a[96:99], a[72:75], v[120:123]\n"
    "v_mfma_f32_16x16x32_bf16 v[124:127], a[100:103], a[72:75], v[124:127]\n"
    "s_add_u32 m0, s45, 14464\n"
    "ds_read_b128 a[12:15], v7 offset:35840\n"
    "v_mfma_f32_16x16x32_bf16 v[128:131], a[104:107], a[72:75], v[128:131]\n"
    "global_load_lds_dwordx4 v3, s[42:43] offset:0\n"
    "v_mfma_f32_16x16x32_bf16 v[132:135], a[108:111], a[72:75], v[132:135]\n"
    "ds_read_b64_tr_b16 a[48:49], v14 offset:28928\n"
    "v_mfma_f32_16x16x32_bf16 v[136:139], a[112:115], a[72:75], v[136:139]\n"
    "v_mfma_f32_16x16x32_bf16 v[140:143], a[116:119], a[72:75], v[140:143]\n"
    "ds_read_b64_tr_b16 a[50:51], v14 offset:32512\n"
    "v_mfma_f32_16x16x32_bf16 a[164:167], a[96:99], a[120:123], a[164:167]\n"
    "v_mfma_f32_16x16x32_bf16 v[144:147], a[92:95], a[76:79], v[144:147]\n"
    "ds_read_b128 a[16:19], v7 offset:36864\n"
    "v_mfma_f32_16x16x32_bf16 v[148:151], a[96:99], a[76:79], v[148:151]\n"
    "global_load_lds_dwordx4 v4, s[42:43] offset:1024\n"
    "v_mfma_f32_16x16x32_bf16 v[152:155], a[100:103], a[76:79], v[152:155]\n"
    "ds_read_b64_tr_b16 a[52:53], v14 offset:36112\n"
    "v_mfma_f32_16x16x32_bf16 v[156:159], a[104:107], a[76:79], v[156:159]\n"
    "v_mfma_f32_16x16x32_bf16 v[160:163], a[108:111], a[76:79], v[160:163]\n"
    "ds_read_b64_tr_b16 a[54:55], v14 offset:39696\n"
    "v_mfma_f32_16x16x32_bf16 v[164:167], a[112:115], a[76:79], v[164:167]\n"
    "v_mfma_f32_16x16x32_bf16 v[168:171], a[116:119], a[76:79], v[168:171]\n"
    "v_mfma_f32_16x16x32_bf16 v[172:175], a[92:95], a[80:83], v[172:175]\n"
    "global_load_lds_dwordx4 v5, s[42:43] offset:2048\n"
    "ds_read_b128 a[20:23], v7 offset:37888\n"
    "v_mfma_f32_16x16x32_bf16 v[176:179], a[96:99], a[80:83], v[176:179]\n"
    "v_mfma_f32_16x16x32_bf16 v[180:183], a[100:103], a[80:83], v[180:183]\n"
    "ds_read_b64_tr_b16 a[56:57], v9 offset:28928\n"
    "v_mfma_f32_16x16x32_bf16 v[184:187], a[104:107], a[80:83], v[184:187]\n"
    "v_mfma_f32_16x16x32_bf16 v[188:191], a[108:111], a[80:83], v[188:191]\n"
    "ds_read_b64_tr_b16 a[58:59], v9 offset:32512\n"
    "v_mfma_f32_16x16x32_bf16 v[192:195], a[112:115], a[80:83], v[192:195]\n"
    "v_mfma_f32_16x16x32_bf16 v[196:199], a[116:119], a[80:83], v[196:199]\n"
    "global_load_lds_dwordx4 v6, s[42:43] offset:3072\n"
    "s_add_u32 s42, s42, 229376\n"
    "s_addc_u32 s43, s43, 0\n"
    "ds_read_b128 a[24:27], v7 offset:38912\n"
    "v_mfma_f32_16x16x32_bf16 a[168:171], a[100:103], a[120:123], a[168:171]\n"
    "v_mfma_f32_16x16x32_bf16 v[200:203], a[92:95], a[84:87], v[200:203]\n"
    "ds_read_b128 a[28:31], v7 offset:39936\n"
    "v_mfma_f32_16x16x32_bf16 v[204:207], a[96:99], a[84:87], v[204:207]\n"
    "v_mfma_f32_16x16x32_bf16 v[208:211], a[100:103], a[84:87], v[208:211]\n"
    "v_mfma_f32_16x16x32_bf16 v[212:215], a[104:107], a[84:87], v[212:215]\n"
    "v_mfma_f32_16x16x32_bf16 v[216:219], a[108:111], a[84:87], v[216:219]\n"
    "v_mfma_f32_16x16x32_bf16 v[220:223], a[112:115], a[84:87], v[220:223]\n"
    "v_mfma_f32_16x16x32_bf16 v[224:227], a[116:119], a[84:87], v[224:227]\n"
    "v_mfma_f32_16x16x32_bf16 v[228:231], a[92:95], a[88:91], v[228:231]\n"
    "v_mfma_f32_16x16x32_bf16 v[232:235], a[96:99], a[88:91], v[232:235]\n"
    "v_mfma_f32_16x16x32_bf16 v[236:239], a[100:103], a[88:91], v[236:239]\n"
    "v_mfma_f32_16x16x32_bf16 v[240:243], a[104:107], a[88:91], v[240:243]\n"
    "v_mfma_f32_16x16x32_bf16 v[244:247], a[108:111], a[88:91], v[244:247]\n"
    "s_waitcnt vmcnt(16) lgkmcnt(0)\n"
    "s_barrier\n"
    "v_mfma_f32_16x16x32_bf16 v[248:251], a[112:115], a[88:91], v[248:251]\n"
    "v_mfma_f32_16x16x32_bf16 v[252:255], a[116:119], a[88:91], v[252:255]\n"
    "v_mfma_f32_16x16x32_bf16 a[172:175], a[116:119], a[120:123], a[172:175]\n"
    "s_sub_u32 s46, s46, 1\n"
    "s_cmp_lg_u32 s46, 0\n"
    "s_cbranch_scc1 1b\n"
    "v_mfma_f32_16x16x32_bf16 v[32:35], a[32:35], a[0:3], v[32:35]\n"
    "ds_read_b64_tr_b16 a[92:93], v8 offset:43392\n"
    "v_mfma_f32_16x16x32_bf16 v[36:39], a[36:39], a[0:3], v[36:39]\n"
    "global_store_dwordx4 v17, a[124:127], s[74:75] sc1\n"
    "s_add_u32 s74, s74, 4096\n"
    "s_addc_u32 s75, s75, 0\n"
    "v_mfma_f32_16x16x32_bf16 v[40:43], a[40:43], a[0:3], v[40:43]\n"
    "s_add_u32 m0, s44, 32768\n"
    "ds_read_b64_tr_b16 a[94:95], v8 offset:46976\n"
    "v_mfma_f32_16x16x32_bf16 v[44:47], a[44:47], a[0:3], v[44:47]\n"
    "global_load_lds_dwordx4 v2, s[40:41] offset:0 sc1\n"
    "v_mfma_f32_16x16x32_bf16 v[48:51], a[48:51], a[0:3], v[48:51]\n"
    "ds_read_b128 a[60:63], v7 offset:49152\n"
    "v_mfma_f32_16x16x32_bf16 v[52:55], a[52:55], a[0:3], v[52:55]\n"
    "v_mfma_f32_16x16x32_bf16 v[56:59], a[56:59], a[0:3], v[56:59]\n"
    "global_load_lds_dwordx4 v2, s[40:41] offset:1024 sc1\n"
    "ds_read_b64_tr_b16 a[96:97], v8 offset:50576\n"
    "v_mfma_f32_16x16x32_bf16 v[60:63], a[32:35], a[4:7], v[60:63]\n"
    "v_mfma_f32_16x16x32_bf16 v[64:67], a[36:39], a[4:7], v[64:67]\n"
    "ds_read_b64_tr_b16 a[98:99], v8 offset:54160\n"
    "v_mfma_f32_16x16x32_bf16 v[68:71], a[40:43], a[4:7], v[68:71]\n"
    "global_load_lds_dwordx4 v2, s[40:41] offset:2048 sc1\n"
    "v_mfma_f32_16x16x32_bf16 v[72:75], a[44:47], a[4:7], v[72:75]\n"
    "ds_read_b128 a[64:67], v7 offset:50176\n"
    "v_mfma_f32_16x16x32_bf16 v[76:79], a[48:51], a[4:7], v[76:79]\n"
    "v_mfma_f32_16x16x32_bf16 v[80:83], a[52:55], a[4:7], v[80:83]\n"
    "global_load_lds_dwordx4 v2, s[40:41] offset:3072 sc1\n"
    "s_add_u32 s40, s40, 8192\n"
    "s_addc_u32 s41, s41, 0\n"
    "v_mfma_f32_16x16x32_bf16 v[84:87], a[56:59], a[4:7], v[84:87]\n"
    "ds_read_b64_tr_b16 a[100:101], v12 offset:43392\n"
    "v_mfma_f32_16x16x32_bf16 a[160:163], a[32:35], a[120:123], a[160:163]\n"
    "s_add_u32 m0, s45, 28928\n"
    "v_mfma_f32_16x16x32_bf16 v[88:91], a[32:35], a[8:11], v[88:91]\n"
    "global_load_lds_dwordx4 v3, s[42:43] offset:0\n"
    "ds_read_b64_tr_b16 a[102:103], v12 offset:46976\n"
    "v_mfma_f32_16x16x32_bf16 v[92:95], a[36:39], a[8:11], v[92:95]\n"
    "v_mfma_f32_16x16x32_bf16 v[96:99], a[40:43], a[8:11], v[96:99]\n"
    "ds_read_b128 a[68:71], v7 offset:51200\n"
    "v_mfma_f32_16x16x32_bf16 v[100:103], a[44:47], a[8:11], v[100:103]\n"
    "global_load_lds_dwordx4 v4, s[42:43] offset:1024\n"
    "v_mfma_f32_16x16x32_bf16 v[104:107], a[48:51], a[8:11], v[104:107]\n"
    "ds_read_b64_tr_b16 a[104:105], v13 offset:43392\n"
    "v_mfma_f32_16x16x32_bf16 v[108:111], a[52:55], a[8:11], v[108:111]\n"
    "v_mfma_f32_16x16x32_bf16 v[112:115], a[56:59], a[8:11], v[112:115]\n"
    "global_load_lds_dwordx4 v5, s[42:43] offset:2048\n"
    "ds_read_b64_tr_b16 a[106:107], v13 offset:46976\n"
    "v_mfma_f32_16x16x32_bf16 v[116:119], a[32:35], a[12:15], v[116:119]\n"
    "v_mfma_f32_16x16x32_bf16 v[120:123], a[36:39], a[12:15], v[120:123]\n"
    "v_mfma_f32_16x16x32_bf16 v[124:127], a[40:43], a[12:15], v[124:127]\n"
    "global_load_lds_dwordx4 v6, s[42:43] offset:3072\n"
    "s_add_u32 s42, s42, 229376\n"
    "s_addc_u32 s43, s43, 0\n"
    "ds_read_b128 a[72:75], v7 offset:52224\n"
    "v_mfma_f32_16x16x32_bf16 v[128:131], a[44:47], a[12:15], v[128:131]\n"
    "v_mfma_f32_16x16x32_bf16 v[132:135], a[48:51], a[12:15], v[132:135]\n"
    "global_load_dwordx2 a[128:129], v10, s[48:49] sc1\n"
    "s_add_u32 s48, s48, 16384\n"
    "s_addc_u32 s49, s49, 0\n"
    "ds_read_b64_tr_b16 a[108:109], v14 offset:43392\n"
    "v_mfma_f32_16x16x32_bf16 v[136:139], a[52:55], a[12:15], v[136:139]\n"
    "v_mfma_f32_16x16x32_bf16 v[140:143], a[56:59], a[12:15], v[140:143]\n"
    "ds_read_b64_tr_b16 a[110:111], v14 offset:46976\n"
    "v_mfma_f32_16x16x32_bf16 a[164:167], a[36:39], a[120:123], a[164:167]\n"
    "global_load_dwordx2 a[130:131], v10, s[48:49] sc1\n"
    "s_add_u32 s48, s48, 16384\n"
    "s_addc_u32 s49, s49, 0\n"
    "v_mfma_f32_16x16x32_bf16 v[144:147], a[32:35], a[16:19], v[144:147]\n"
    "ds_read_b128 a[76:79], v7 offset:53248\n"
    "v_mfma_f32_16x16x32_bf16 v[148:151], a[36:39], a[16:19], v[148:151]\n"
    "v_mfma_f32_16x16x32_bf16 v[152:155], a[40:43], a[16:19], v[152:155]\n"
    "global_load_dwordx2 a[132:133], v10, s[48:49] sc1\n"
    "s_add_u32 s48, s48, 16384\n"
    "s_addc_u32 s49, s49, 0\n"
    "ds_read_b64_tr_b16 a[112:113], v14 offset:50576\n"
    "v_mfma_f32_16x16x32_bf16 v[156:159], a[44:47], a[16:19], v[156:159]\n"
    "v_mfma_f32_16x16x32_bf16 v[160:163], a[48:51], a[16:19], v[160:163]\n"
    "ds_read_b64_tr_b16 a[114:115], v14 offset:54160\n"
    "v_mfma_f32_16x16x32_bf16 v[164:167], a[52:55], a[16:19], v[164:167]\n"
    "global_load_dwordx2 a[134:135], v10, s[48:49] sc1\n"
    "s_add_u32 s48, s48, 16384\n"
    "s_addc_u32 s49, s49, 0\n"
    "v_mfma_f32_16x16x32_bf16 v[168:171], a[56:59], a[16:19], v[168:171]\n"
    "v_mfma_f32_16x16x32_bf16 v[172:175], a[32:35], a[20:23], v[172:175]\n"
    "ds_read_b128 a[80:83], v7 offset:54272\n"
    "v_mfma_f32_16x16x32_bf16 v[176:179], a[36:39], a[20:23], v[176:179]\n"
    "global_load_dwordx2 a[136:137], v10, s[48:49] sc1\n"
    "s_add_u32 s48, s48, 16384\n"
    "s_addc_u32 s49, s49, 0\n"
    "v_mfma_f32_16x16x32_bf16 v[180:183], a[40:43], a[20:23], v[180:183]\n"
    "ds_read_b64_tr_b16 a[116:117], v9 offset:43392\n"
    "v_mfma_f32_16x16x32_bf16 v[184:187], a[44:47], a[20:23], v[184:187]\n"
    "v_mfma_f32_16x16x32_bf16 v[188:191], a[48:51], a[20:23], v[188:191]\n"
    "global_load_dwordx2 a[138:139], v10, s[48:49] sc1\n"
    "s_add_u32 s48, s48, 16384\n"
    "s_addc_u32 s49, s49, 0\n"
    "ds_read_b64_tr_b16 a[118:119], v9 offset:46976\n"
    "v_mfma_f32_16x16x32_bf16 v[192:195], a[52:55], a[20:23], v[192:195]\n"
    "v_mfma_f32_16x16x32_bf16 v[196:199], a[56:59], a[20:23], v[196:199]\n"
    "ds_read_b128 a[84:87], v7 offset:55296\n"
    "v_mfma_f32_16x16x32_bf16 a[168:171], a[40:43], a[120:123], a[168:171]\n"
    "global_load_dwordx2 a[140:141], v10, s[48:49] sc1\n"
    "s_add_u32 s48, s48, 16384\n"
    "s_addc_u32 s49, s49, 0\n"
    "v_mfma_f32_16x16x32_bf16 v[200:203], a[32:35], a[24:27], v[200:203]\n"
    "ds_read_b128 a[88:91], v7 offset:56320\n"
    "v_mfma_f32_16x16x32_bf16 v[204:207], a[36:39], a[24:27], v[204:207]\n"
    "v_mfma_f32_16x16x32_bf16 v[208:211], a[40:43], a[24:27], v[208:211]\n"
    "global_load_dwordx2 a[142:143], v10, s[48:49] sc1\n"
    "s_add_u32 s48, s48, 16384\n"
    "s_addc_u32 s49, s49, 0\n"
    "v_mfma_f32_16x16x32_bf16 v[212:215], a[44:47], a[24:27], v[212:215]\n"
    "v_mfma_f32_16x16x32_bf16 v[216:219], a[48:51], a[24:27], v[216:219]\n"
    "v_mfma_f32_16x16x32_bf16 v[220:223], a[52:55], a[24:27], v[220:223]\n"
    "v_mfma_f32_16x16x32_bf16 v[224:227], a[56:59], a[24:27], v[224:227]\n"
    "v_mfma_f32_16x16x32_bf16 v[228:231], a[32:35], a[28:31], v[228:231]\n"
    "v_mfma_f32_16x16x32_bf16 v[232:235], a[36:39], a[28:31], v[232:235]\n"
    "v_mfma_f32_16x16x32_bf16 v[236:239], a[40:43], a[28:31], v[236:239]\n"
    "v_mfma_f32_16x16x32_bf16 v[240:243], a[44:47], a[28:31], v[240:243]\n"
    "v_mfma_f32_16x16x32_bf16 v[244:247], a[48:51], a[28:31], v[244:247]\n"
    "s_waitcnt vmcnt(25) lgkmcnt(0)\n"
    "s_barrier\n"
    "v_mfma_f32_16x16x32_bf16 v[248:251], a[52:55], a[28:31], v[248:251]\n"
    "v_mfma_f32_16x16x32_bf16 v[252:255], a[56:59], a[28:31], v[252:255]\n"
    "v_mfma_f32_16x16x32_bf16 a[172:175], a[56:59], a[120:123], a[172:175]\n"
    "v_mfma_f32_16x16x32_bf16 v[32:35], a[92:95], a[60:63], v[32:35]\n"
    "ds_read_b64_tr_b16 a[32:33], v8 offset:0\n"
    "v_mfma_f32_16x16x32_bf16 v[36:39], a[96:99], a[60:63], v[36:39]\n"
    "global_store_dwordx4 v17, a[124:127], s[74:75] sc1\n"
    "s_add_u32 s74, s74, 4096\n"
    "s_addc_u32 s75, s75, 0\n"
    "v_mfma_f32_16x16x32_bf16 v[40:43], a[100:103], a[60:63], v[40:43]\n"
    "s_add_u32 m0, s44, 49152\n"
    "ds_read_b64_tr_b16 a[34:35], v8 offset:3584\n"
    "v_mfma_f32_16x16x32_bf16 v[44:47], a[104:107], a[60:63], v[44:47]\n"
    "global_load_lds_dwordx4 v2, s[40:41] offset:0 sc1\n"
    "v_mfma_f32_16x16x32_bf16 v[48:51], a[108:111], a[60:63], v[48:51]\n"
    "ds_read_b128 a[0:3], v7 offset:0\n"
    "v_mfma_f32_16x16x32_bf16 v[52:55], a[112:115], a[60:63], v[52:55]\n"
    "v_mfma_f32_16x16x32_bf16 v[56:59], a[116:119], a[60:63], v[56:59]\n"
    "global_load_lds_dwordx4 v2, s[40:41] offset:1024 sc1\n"
    "ds_read_b64_tr_b16 a[36:37], v8 offset:7184\n"
    "v_mfma_f32_16x16x32_bf16 v[60:63], a[92:95], a[64:67], v[60:63]\n"
    "v_mfma_f32_16x16x32_bf16 v[64:67], a[96:99], a[64:67], v[64:67]\n"
    "ds_read_b64_tr_b16 a[38:39], v8 offset:10768\n"
    "v_mfma_f32_16x16x32_bf16 v[68:71], a[100:103], a[64:67], v[68:71]\n"
    "global_load_lds_dwordx4 v2, s[40:41] offset:2048 sc1\n"
    "v_mfma_f32_16x16x32_bf16 v[72:75], a[104:107], a[64:67], v[72:75]\n"
    "ds_read_b128 a[4:7], v7 offset:1024\n"
    "v_mfma_f32_16x16x32_bf16 v[76:79], a[108:111], a[64:67], v[76:79]\n"
    "v_mfma_f32_16x16x32_bf16 v[80:83], a[112:115], a[64:67], v[80:83]\n"
    "global_load_lds_dwordx4 v2, s[40:41] offset:3072 sc1\n"
    "s_add_u32 s40, s40, 8192\n"
    "s_addc_u32 s41, s41, 0\n"
    "v_mfma_f32_16x16x32_bf16 v[84:87], a[116:119], a[64:67], v[84:87]\n"
    "ds_read_b64_tr_b16 a[40:41], v12 offset:0\n"
    "v_mfma_f32_16x16x32_bf16 a[160:163], a[92:95], a[120:123], a[160:163]\n"
    "s_add_u32 m0, s45, 43392\n"
    "v_mfma_f32_16x16x32_bf16 v[88:91], a[92:95], a[68:71], v[88:91]\n"
    "global_load_lds_dwordx4 v3, s[42:43] offset:0\n"
    "ds_read_b64_tr_b16 a[42:43], v12 offset:3584\n"
    "v_mfma_f32_16x16x32_bf16 v[92:95], a[96:99], a[68:71], v[92:95]\n"
    "v_mfma_f32_16x16x32_bf16 v[96:99], a[100:103], a[68:71], v[96:99]\n"
    "ds_read_b128 a[8:11], v7 offset:2048\n"
    "v_mfma_f32_16x16x32_bf16 v[100:103], a[104:107], a[68:71], v[100:103]\n"
    "global_load_lds_dwordx4 v4, s[42:43] offset:1024\n"
    "v_mfma_f32_16x16x32_bf16 v[104:107], a[108:111], a[68:71], v[104:107]\n"
    "ds_read_b64_tr_b16 a[44:45], v13 offset:0\n"
    "v_mfma_f32_16x16x32_bf16 v[108:111], a[112:115], a[68:71], v[108:111]\n"
    "v_mfma_f32_16x16x32_bf16 v[112:115], a[116:119], a[68:71], v[112:115]\n"
    "global_load_lds_dwordx4 v5, s[42:43] offset:2048\n"
    "ds_read_b64_tr_b16 a[46:47], v13 offset:3584\n"
    "v_mfma_f32_16x16x32_bf16 v[116:119], a[92:95], a[72:75], v[116:119]\n"
    "v_mfma_f32_16x16x32_bf16 v[120:123], a[96:99], a[72:75], v[120:123]\n"
    "v_mfma_f32_16x16x32_bf16 v[124:127], a[100:103], a[72:75], v[124:127]\n"
    "global_load_lds_dwordx4 v6, s[42:43] offset:3072\n"
    "s_add_u32 s42, s42, 229376\n"
    "s_addc_u32 s43, s43, 0\n"
    "ds_read_b128 a[12:15], v7 offset:3072\n"
    "v_mfma_f32_16x16x32_bf16 v[128:131], a[104:107], a[72:75], v[128:131]\n"
    "v_mfma_f32_16x16x32_bf16 v[132:135], a[108:111], a[72:75], v[132:135]\n"
    "global_load_dwordx2 a[144:145], v10, s[48:49] sc1\n"
    "s_add_u32 s48, s48, 16384\n"
    "s_addc_u32 s49, s49, 0\n"
    "ds_read_b64_tr_b16 a[48:49], v14 offset:0\n"
    "v_mfma_f32_16x16x32_bf16 v[136:139], a[112:115], a[72:75], v[136:139]\n"
    "v_mfma_f32_16x16x32_bf16 v[140:143], a[116:119], a[72:75], v[140:143]\n"
    "ds_read_b64_tr_b16 a[50:51], v14 offset:3584\n"
    "v_mfma_f32_16x16x32_bf16 a[164:167], a[96:99], a[120:123], a[164:167]\n"
    "global_load_dwordx2 a[146:147], v10, s[48:49] sc1\n"
    "s_add_u32 s48, s48, 16384\n"
    "s_addc_u32 s49, s49, 0\n"
    "v_mfma_f32_16x16x32_bf16 v[144:147], a[92:95], a[76:79], v[144:147]\n"
    "ds_read_b128 a[16:19], v7 offset:4096\n"
    "v_mfma_f32_16x16x32_bf16 v[148:151], a[96:99], a[76:79], v[148:151]\n"
    "v_mfma_f32_16x16x32_bf16 v[152:155], a[100:103], a[76:79], v[152:155]\n"
    "global_load_dwordx2 a[148:149], v10, s[48:49] sc1\n"
    "s_add_u32 s48, s48, 16384\n"
    "s_addc_u32 s49, s49, 0\n"
    "ds_read_b64_tr_b16 a[52:53], v14 offset:7184\n"
    "v_mfma_f32_16x16x32_bf16 v[156:159], a[104:107], a[76:79], v[156:159]\n"
    "v_mfma_f32_16x16x32_bf16 v[160:163], a[108:111], a[76:79], v[160:163]\n"
    "ds_read_b64_tr_b16 a[54:55], v14 offset:10768\n"
    "v_mfma_f32_16x16x32_bf16 v[164:167], a[112:115], a[76:79], v[164:167]\n"
    "global_load_dwordx2 a[150:151], v10, s[48:49] sc1\n"
    "s_add_u32 s48, s48, 16384\n"
    "s_addc_u32 s49, s49, 0\n"
    "v_mfma_f32_16x16x32_bf16 v[168:171], a[116:119], a[76:79], v[168:171]\n"
    "v_mfma_f32_16x16x32_bf16 v[172:175], a[92:95], a[80:83], v[172:175]\n"
    "ds_read_b128 a[20:23], v7 offset:5120\n"
    "v_mfma_f32_16x16x32_bf16 v[176:179], a[96:99], a[80:83], v[176:179]\n"
    "global_load_dwordx2 a[152:153], v10, s[48:49] sc1\n"
    "s_add_u32 s48, s48, 16384\n"
    "s_addc_u32 s49, s49, 0\n"
    "v_mfma_f32_16x16x32_bf16 v[180:183], a[100:103], a[80:83], v[180:183]\n"
    "ds_read_b64_tr_b16 a[56:57], v9 offset:0\n"
    "v_mfma_f32_16x16x32_bf16 v[184:187], a[104:107], a[80:83], v[184:187]\n"
    "v_mfma_f32_16x16x32_bf16 v[188:191], a[108:111], a[80:83], v[188:191]\n"
    "global_load_dwordx2 a[154:155], v10, s[48:49] sc1\n"
    "s_add_u32 s48, s48, 16384\n"
    "s_addc_u32 s49, s49, 0\n"
    "ds_read_b64_tr_b16 a[58:59], v9 offset:3584\n"
    "v_mfma_f32_16x16x32_bf16 v[192:195], a[112:115], a[80:83], v[192:195]\n"
    "v_mfma_f32_16x16x32_bf16 v[196:199], a[116:119], a[80:83], v[196:199]\n"
    "ds_read_b128 a[24:27], v7 offset:6144\n"
    "v_mfma_f32_16x16x32_bf16 a[168:171], a[100:103], a[120:123], a[168:171]\n"
    "global_load_dwordx2 a[156:157], v10, s[48:49] sc1\n"
    "s_add_u32 s48, s48, 16384\n"
    "s_addc_u32 s49, s49, 0\n"
    "v_mfma_f32_16x16x32_bf16 v[200:203], a[92:95], a[84:87], v[200:203]\n"
    "ds_read_b128 a[28:31], v7 offset:7168\n"
    "v_mfma_f32_16x16x32_bf16 v[204:207], a[96:99], a[84:87], v[204:207]\n"
    "v_mfma_f32_16x16x32_bf16 v[208:211], a[100:103], a[84:87], v[208:211]\n"
    "global_load_dwordx2 a[158:159], v10, s[48:49] sc1\n"
    "v_mfma_f32_16x16x32_bf16 v[212:215], a[104:107], a[84:87], v[212:215]\n"
    "v_mfma_f32_16x16x32_bf16 v[216:219], a[108:111], a[84:87], v[216:219]\n"
    "v_mfma_f32_16x16x32_bf16 v[220:223], a[112:115], a[84:87], v[220:223]\n"
    "v_mfma_f32_16x16x32_bf16 v[224:227], a[116:119], a[84:87], v[224:227]\n"
    "v_mfma_f32_16x16x32_bf16 v[228:231], a[92:95], a[88:91], v[228:231]\n"
    "v_mfma_f32_16x16x32_bf16 v[232:235], a[96:99], a[88:91], v[232:235]\n"
    "v_mfma_f32_16x16x32_bf16 v[236:239], a[100:103], a[88:91], v[236:239]\n"
    "v_mfma_f32_16x16x32_bf16 v[240:243], a[104:107], a[88:91], v[240:243]\n"
    "v_mfma_f32_16x16x32_bf16 v[244:247], a[108:111], a[88:91], v[244:247]\n"
    "s_waitcnt vmcnt(34) lgkmcnt(0)\n"
    "s_barrier\n"
    "v_mfma_f32_16x16x32_bf16 v[248:251], a[112:115], a[88:91], v[248:251]\n"
    "v_mfma_f32_16x16x32_bf16 v[252:255], a[116:119], a[88:91], v[252:255]\n"
    "v_mfma_f32_16x16x32_bf16 a[172:175], a[116:119], a[120:123], a[172:175]\n"
    "v_mfma_f32_16x16x32_bf16 v[32:35], a[32:35], a[0:3], v[32:35]\n"
    "s_getpc_b64 s[70:71]\n"
    "2:\n"
    "v_min_u32 v18, lin_end-2b-4, v15\n"
    "s_mov_b32 m0, s47\n"
    "ds_read_b64_tr_b16 a[92:93], v8 offset:14464\n"
    "v_mfma_f32_16x16x32_bf16 v[36:39], a[36:39], a[0:3], v[36:39]\n"
    "global_load_lds_dword v18, s[70:71]\n"
    "v_mfma_f32_16x16x32_bf16 v[40:43], a[40:43], a[0:3], v[40:43]\n"
    "ds_read_b64_tr_b16 a[94:95], v8 offset:18048\n"
    "v_mfma_f32_16x16x32_bf16 v[44:47], a[44:47], a[0:3], v[44:47]\n"
    "v_mfma_f32_16x16x32_bf16 v[48:51], a[48:51], a[0:3], v[48:51]\n"
    "ds_read_b128 a[60:63], v7 offset:16384\n"
    "v_mfma_f32_16x16x32_bf16 v[52:55], a[52:55], a[0:3], v[52:55]\n"
    "v_mfma_f32_16x16x32_bf16 v[56:59], a[56:59], a[0:3], v[56:59]\n"
    "ds_read_b64_tr_b16 a[96:97], v8 offset:21648\n"
    "v_mfma_f32_16x16x32_bf16 v[60:63], a[32:35], a[4:7], v[60:63]\n"
    "v_mfma_f32_16x16x32_bf16 v[64:67], a[36:39], a[4:7], v[64:67]\n"
    "ds_read_b64_tr_b16 a[98:99], v8 offset:25232\n"
    "v_mfma_f32_16x16x32_bf16 v[68:71], a[40:43], a[4:7], v[68:71]\n"
    "v_mfma_f32_16x16x32_bf16 v[72:75], a[44:47], a[4:7], v[72:75]\n"
    "ds_read_b128 a[64:67], v7 offset:17408\n"
    "v_mfma_f32_16x16x32_bf16 v[76:79], a[48:51], a[4:7], v[76:79]\n"
    "v_mfma_f32_16x16x32_bf16 v[80:83], a[52:55], a[4:7], v[80:83]\n"
    "v_mfma_f32_16x16x32_bf16 v[84:87], a[56:59], a[4:7], v[84:87]\n"
    "ds_read_b64_tr_b16 a[100:101], v12 offset:14464\n"
    "v_mfma_f32_16x16x32_bf16 a[160:163], a[32:35], a[120:123], a[160:163]\n"
    "v_mfma_f32_16x16x32_bf16 v[88:91], a[32:35], a[8:11], v[88:91]\n"
    "ds_read_b64_tr_b16 a[102:103], v12 offset:18048\n"
    "v_mfma_f32_16x16x32_bf16 v[92:95], a[36:39], a[8:11], v[92:95]\n"
    "v_mfma_f32_16x16x32_bf16 v[96:99], a[40:43], a[8:11], v[96:99]\n"
    "ds_read_b128 a[68:71], v7 offset:18432\n"
    "v_mfma_f32_16x16x32_bf16 v[100:103], a[44:47], a[8:11], v[100:103]\n"
    "v_mfma_f32_16x16x32_bf16 v[104:107], a[48:51], a[8:11], v[104:107]\n"
    "ds_read_b64_tr_b16 a[104:105], v13 offset:14464\n"
    "v_mfma_f32_16x16x32_bf16 v[108:111], a[52:55], a[8:11], v[108:111]\n"
    "v_mfma_f32_16x16x32_bf16 v[112:115], a[56:59], a[8:11], v[112:115]\n"
    "ds_read_b64_tr_b16 a[106:107], v13 offset:18048\n"
    "v_mfma_f32_16x16x32_bf16 v[116:119], a[32:35], a[12:15], v[116:119]\n"
    "v_mfma_f32_16x16x32_bf16 v[120:123], a[36:39], a[12:15], v[120:123]\n"
    "v_mfma_f32_16x16x32_bf16 v[124:127], a[40:43], a[12:15], v[124:127]\n"
    "ds_read_b128 a[72:75], v7 offset:19456\n"
    "v_mfma_f32_16x16x32_bf16 v[128:131], a[44:47], a[12:15], v[128:131]\n"
    "global_load_lds_dword v16, s[72:73]\n"
    "v_mfma_f32_16x16x32_bf16 v[132:135], a[48:51], a[12:15], v[132:135]\n"
    "ds_read_b64_tr_b16 a[108:109], v14 offset:14464\n"
    "v_mfma_f32_16x16x32_bf16 v[136:139], a[52:55], a[12:15], v[136:139]\n"
    "v_mfma_f32_16x16x32_bf16 v[140:143], a[56:59], a[12:15], v[140:143]\n"
    "ds_read_b64_tr_b16 a[110:111], v14 offset:18048\n"
    "v_mfma_f32_16x16x32_bf16 a[164:167], a[36:39], a[120:123], a[164:167]\n"
    "v_mfma_f32_16x16x32_bf16 v[144:147], a[32:35], a[16:19], v[144:147]\n"
    "ds_read_b128 a[76:79], v7 offset:20480\n"
    "v_mfma_f32_16x16x32_bf16 v[148:151], a[36:39], a[16:19], v[148:151]\n"
    "v_mfma_f32_16x16x32_bf16 v[152:155], a[40:43], a[16:19], v[152:155]\n"
    "ds_read_b64_tr_b16 a[112:113], v14 offset:21648\n"
    "v_mfma_f32_16x16x32_bf16 v[156:159], a[44:47], a[16:19], v[156:159]\n"
    "v_mfma_f32_16x16x32_bf16 v[160:163], a[48:51], a[16:19], v[160:163]\n"
    "ds_read_b64_tr_b16 a[114:115], v14 offset:25232\n"
    "v_mfma_f32_16x16x32_bf16 v[164:167], a[52:55], a[16:19], v[164:167]\n"
    "v_mfma_f32_16x16x32_bf16 v[168:171], a[56:59], a[16:19], v[168:171]\n"
    "v_mfma_f32_16x16x32_bf16 v[172:175], a[32:35], a[20:23], v[172:175]\n"
    "ds_read_b128 a[80:83], v7 offset:21504\n"
    "v_mfma_f32_16x16x32_bf16 v[176:179], a[36:39], a[20:23], v[176:179]\n"
    "v_mfma_f32_16x16x32_bf16 v[180:183], a[40:43], a[20:23], v[180:183]\n"
    "ds_read_b64_tr_b16 a[116:117], v9 offset:14464\n"
    "v_mfma_f32_16x16x32_bf16 v[184:187], a[44:47], a[20:23], v[184:187]\n"
    "v_mfma_f32_16x16x32_bf16 v[188:191], a[48:51], a[20:23], v[188:191]\n"
    "ds_read_b64_tr_b16 a[118:119], v9 offset:18048\n"
    "v_mfma_f32_16x16x32_bf16 v[192:195], a[52:55], a[20:23], v[192:195]\n"
    "v_mfma_f32_16x16x32_bf16 v[196:199], a[56:59], a[20:23], v[196:199]\n"
    "ds_read_b128 a[84:87], v7 offset:22528\n"
    "v_mfma_f32_16x16x32_bf16 a[168:171], a[40:43], a[120:123], a[168:171]\n"
    "v_mfma_f32_16x16x32_bf16 v[200:203], a[32:35], a[24:27], v[200:203]\n"
    "ds_read_b128 a[88:91], v7 offset:23552\n"
    "v_mfma_f32_16x16x32_bf16 v[204:207], a[36:39], a[24:27], v[204:207]\n"
    "v_mfma_f32_16x16x32_bf16 v[208:211], a[40:43], a[24:27], v[208:211]\n"
    "v_mfma_f32_16x16x32_bf16 v[212:215], a[44:47], a[24:27], v[212:215]\n"
    "v_mfma_f32_16x16x32_bf16 v[216:219], a[48:51], a[24:27], v[216:219]\n"
    "v_mfma_f32_16x16x32_bf16 v[220:223], a[52:55], a[24:27], v[220:223]\n"
    "v_mfma_f32_16x16x32_bf16 v[224:227], a[56:59], a[24:27], v[224:227]\n"
    "v_mfma_f32_16x16x32_bf16 v[228:231], a[32:35], a[28:31], v[228:231]\n"
    "v_mfma_f32_16x16x32_bf16 v[232:235], a[36:39], a[28:31], v[232:235]\n"
    "v_mfma_f32_16x16x32_bf16 v[236:239], a[40:43], a[28:31], v[236:239]\n"
    "v_mfma_f32_16x16x32_bf16 v[240:243], a[44:47], a[28:31], v[240:243]\n"
    "v_mfma_f32_16x16x32_bf16 v[244:247], a[48:51], a[28:31], v[244:247]\n"
    "s_waitcnt vmcnt(27) lgkmcnt(0)\n"
    "s_barrier\n"
    "v_mfma_f32_16x16x32_bf16 v[248:251], a[52:55], a[28:31], v[248:251]\n"
    "v_mfma_f32_16x16x32_bf16 v[252:255], a[56:59], a[28:31], v[252:255]\n"
    "v_mfma_f32_16x16x32_bf16 a[172:175], a[56:59], a[120:123], a[172:175]\n"
    "v_mfma_f32_16x16x32_bf16 v[32:35], a[92:95], a[60:63], v[32:35]\n"
    "ds_read_b128 a[0:3], v7 offset:32768\n"
    "s_waitcnt vmcnt(2)\n"
    "v_mfma_f32_16x16x32_bf16 v[36:39], a[96:99], a[60:63], v[36:39]\n"
    "ds_read_b64_tr_b16 a[32:33], v8 offset:28928\n"
    "v_accvgpr_read_b32 v25, a128\n"
    "v_accvgpr_read_b32 v10, a129\n"
    "v_mfma_f32_16x16x32_bf16 v[40:43], a[100:103], a[60:63], v[40:43]\n"
    "ds_read_b64_tr_b16 a[34:35], v8 offset:32512\n"
    "v_mov_b32 v15, 0\n"
    "v_mov_b32 v16, 0\n"
    "v_mfma_f32_16x16x32_bf16 v[44:47], a[104:107], a[60:63], v[44:47]\n"
    "ds_read_b64_tr_b16 a[36:37], v8 offset:36112\n"
    "v_accvgpr_read_b32 v18, a130\n"
    "v_accvgpr_read_b32 v11, a131\n"
    "v_mfma_f32_16x16x32_bf16 v[48:51], a[108:111], a[60:63], v[48:51]\n"
    "ds_read_b64_tr_b16 a[38:39], v8 offset:39696\n"
    "v_sub_f32 v18, v18, v25\n"
    "v_add_f32 v10, v10, v11\n"
    "v_mfma_f32_16x16x32_bf16 v[52:55], a[112:115], a[60:63], v[52:55]\n"
    "ds_read_b64_tr_b16 a[40:41], v12 offset:28928\n"
    "v_add_f32 v15, v15, v18\n"
    "v_fmac_f32 v16, v18, v18\n"
    "v_mfma_f32_16x16x32_bf16 v[56:59], a[116:119], a[60:63], v[56:59]\n"
    "ds_read_b64_tr_b16 a[42:43], v12 offset:32512\n"
    "v_accvgpr_read_b32 v18, a132\n"
    "v_accvgpr_read_b32 v11, a133\n"
    "v_mfma_f32_16x16x32_bf16 a[160:163], a[92:95], a[120:123], a[160:163]\n"
    "ds_read_b64_tr_b16 a[44:45], v13 offset:28928\n"
    "v_sub_f32 v18, v18, v25\n"
    "v_add_f32 v10, v10, v11\n"
    "v_mfma_f32_16x16x32_bf16 v[60:63], a[92:95], a[64:67], v[60:63]\n"
    "ds_read_b64_tr_b16 a[46:47], v13 offset:32512\n"
    "v_add_f32 v15, v15, v18\n"
    "v_fmac_f32 v16, v18, v18\n"
    "v_mfma_f32_16x16x32_bf16 v[64:67], a[96:99], a[64:67], v[64:67]\n"
    "v_accvgpr_read_b32 v18, a134\n"
    "v_mfma_f32_16x16x32_bf16 v[68:71], a[100:103], a[64:67], v[68:71]\n"
    "ds_read_b64_tr_b16 a[48:49], v14 offset:28928\n"
    "v_accvgpr_read_b32 v11, a135\n"
    "v_sub_f32 v18, v18, v25\n"
    "v_mfma_f32_16x16x32_bf16 v[72:75], a[104:107], a[64:67], v[72:75]\n"
    "ds_read_b64_tr_b16 a[50:51], v14 offset:32512\n"
    "v_add_f32 v10, v10, v11\n"
    "v_add_f32 v15, v15, v18\n"
    "v_mfma_f32_16x16x32_bf16 v[76:79], a[108:111], a[64:67], v[76:79]\n"
    "ds_read_b64_tr_b16 a[52:53], v14 offset:36112\n"
    "v_fmac_f32 v16, v18, v18\n"
    "v_accvgpr_read_b32 v18, a136\n"
    "v_mfma_f32_16x16x32_bf16 v[80:83], a[112:115], a[64:67], v[80:83]\n"
    "ds_read_b64_tr_b16 a[54:55], v14 offset:39696\n"
    "v_accvgpr_read_b32 v11, a137\n"
    "v_sub_f32 v18, v18, v25\n"
    "v_mfma_f32_16x16x32_bf16 v[84:87], a[116:119], a[64:67], v[84:87]\n"
    "ds_read_b64_tr_b16 a[56:57], v9 offset:28928\n"
    "v_add_f32 v10, v10, v11\n"
    "v_add_f32 v15, v15, v18\n"
    "v_mfma_f32_16x16x32_bf16 a[164:167], a[96:99], a[120:123], a[164:167]\n"
    "ds_read_b64_tr_b16 a[58:59], v9 offset:32512\n"
    "v_fmac_f32 v16, v18, v18\n"
    "v_accvgpr_read_b32 v18, a138\n"
    "v_mfma_f32_16x16x32_bf16 v[88:91], a[92:95], a[68:71], v[88:91]\n"
    "ds_read_b128 a[4:7], v7 offset:33792\n"
    "v_accvgpr_read_b32 v11, a139\n"
    "v_sub_f32 v18, v18, v25\n"
    "v_mfma_f32_16x16x32_bf16 v[92:95], a[96:99], a[68:71], v[92:95]\n"
    "ds_read_b128 a[8:11], v7 offset:34816\n"
    "v_add_f32 v10, v10, v11\n"
    "v_add_f32 v15, v15, v18\n"
    "v_mfma_f32_16x16x32_bf16 v[96:99], a[100:103], a[68:71], v[96:99]\n"
    "ds_read_b128 a[12:15], v7 offset:35840\n"
    "v_fmac_f32 v16, v18, v18\n"
    "v_mfma_f32_16x16x32_bf16 v[100:103], a[104:107], a[68:71], v[100:103]\n"
    "v_accvgpr_read_b32 v18, a140\n"
    "v_accvgpr_read_b32 v11, a141\n"
    "v_mfma_f32_16x16x32_bf16 v[104:107], a[108:111], a[68:71], v[104:107]\n"
    "v_sub_f32 v18, v18, v25\n"
    "v_add_f32 v10, v10, v11\n"
    "v_mfma_f32_16x16x32_bf16 v[108:111], a[112:115], a[68:71], v[108:111]\n"
    "v_add_f32 v15, v15, v18\n"
    "v_fmac_f32 v16, v18, v18\n"
    "v_mfma_f32_16x16x32_bf16 v[112:115], a[116:119], a[68:71], v[112:115]\n"
    "v_accvgpr_read_b32 v18, a142\n"
    "v_accvgpr_read_b32 v11, a143\n"
    "v_mfma_f32_16x16x32_bf16 a[168:171], a[100:103], a[120:123], a[168:171]\n"
    "v_sub_f32 v18, v18, v25\n"
    "v_add_f32 v10, v10, v11\n"
    "v_mfma_f32_16x16x32_bf16 v[116:119], a[92:95], a[72:75], v[116:119]\n"
    "v_add_f32 v15, v15, v18\n"
    "v_fmac_f32 v16, v18, v18\n"
    "v_mfma_f32_16x16x32_bf16 v[120:123], a[96:99], a[72:75], v[120:123]\n"
    "v_accvgpr_read_b32 v18, a144\n"
    "v_accvgpr_read_b32 v11, a145\n"
    "v_mfma_f32_16x16x32_bf16 v[124:127], a[100:103], a[72:75], v[124:127]\n"
    "v_sub_f32 v18, v18, v25\n"
    "v_add_f32 v10, v10, v11\n"
    "s_waitcnt vmcnt(10)\n"
    "s_barrier\n"
    "v_mfma_f32_16x16x32_bf16 v[128:131], a[104:107], a[72:75], v[128:131]\n"
    "v_add_f32 v15, v15, v18\n"
    "v_mfma_f32_16x16x32_bf16 v[132:135], a[108:111], a[72:75], v[132:135]\n"
    "v_mfma_f32_16x16x32_bf16 v[136:139], a[112:115], a[72:75], v[136:139]\n"
    "v_mfma_f32_16x16x32_bf16 v[140:143], a[116:119], a[72:75], v[140:143]\n"
    "v_mfma_f32_16x16x32_bf16 a[172:175], a[116:119], a[120:123], a[172:175]\n"
    "s_waitcnt lgkmcnt(15)\n"
    "v_mfma_f32_16x16x32_bf16 v[32:35], a[32:35], a[0:3], v[32:35]\n"
    "ds_read_b128 a[60:63], v7 offset:49152\n"
    "v_fmac_f32 v16, v18, v18\n"
    "v_accvgpr_read_b32 v18, a146\n"
    "s_waitcnt lgkmcnt(14)\n"
    "v_mfma_f32_16x16x32_bf16 v[36:39], a[36:39], a[0:3], v[36:39]\n"
    "ds_read_b64_tr_b16 a[92:93], v8 offset:43392\n"
    "v_accvgpr_read_b32 v11, a147\n"
    "v_sub_f32 v18, v18, v25\n"
    "s_waitcnt lgkmcnt(13)\n"
    "v_mfma_f32_16x16x32_bf16 v[40:43], a[40:43], a[0:3], v[40:43]\n"
    "ds_read_b64_tr_b16 a[94:95], v8 offset:46976\n"
    "v_add_f32 v10, v10, v11\n"
    "v_add_f32 v15, v15, v18\n"
    "s_waitcnt lgkmcnt(12)\n"
    "v_mfma_f32_16x16x32_bf16 v[44:47], a[44:47], a[0:3], v[44:47]\n"
    "ds_read_b64_tr_b16 a[96:97], v8 offset:50576\n"
    "v_fmac_f32 v16, v18, v18\n"
    "v_accvgpr_read_b32 v18, a148\n"
    "s_waitcnt lgkmcnt(11)\n"
    "v_mfma_f32_16x16x32_bf16 v[48:51], a[48:51], a[0:3], v[48:51]\n"
    "ds_read_b64_tr_b16 a[98:99], v8 offset:54160\n"
    "v_accvgpr_read_b32 v11, a149\n"
    "v_sub_f32 v18, v18, v25\n"
    "s_waitcnt lgkmcnt(10)\n"
    "v_mfma_f32_16x16x32_bf16 v[52:55], a[52:55], a[0:3], v[52:55]\n"
    "ds_read_b64_tr_b16 a[100:101], v12 offset:43392\n"
    "v_add_f32 v10, v10, v11\n"
    "s_waitcnt lgkmcnt(9)\n"
    "v_mfma_f32_16x16x32_bf16 v[56:59], a[56:59], a[0:3], v[56:59]\n"
    "ds_read_b64_tr_b16 a[102:103], v12 offset:46976\n"
    "v_add_f32 v15, v15, v18\n"
    "v_fmac_f32 v16, v18, v18\n"
    "v_mfma_f32_16x16x32_bf16 a[160:163], a[32:35], a[120:123], a[160:163]\n"
    "ds_read_b64_tr_b16 a[104:105], v13 offset:43392\n"
    "v_accvgpr_read_b32 v18, a150\n"
    "v_accvgpr_read_b32 v11, a151\n"
    "s_waitcnt lgkmcnt(10)\n"
    "v_mfma_f32_16x16x32_bf16 v[60:63], a[32:35], a[4:7], v[60:63]\n"
    "ds_read_b64_tr_b16 a[106:107], v13 offset:46976\n"
    "v_sub_f32 v18, v18, v25\n"
    "v_add_f32 v10, v10, v11\n"
    "v_mfma_f32_16x16x32_bf16 v[64:67], a[36:39], a[4:7], v[64:67]\n"
    "v_add_f32 v15, v15, v18\n"
    "v_fmac_f32 v16, v18, v18\n"
    "v_mfma_f32_16x16x32_bf16 v[68:71], a[40:43], a[4:7], v[68:71]\n"
    "ds_read_b64_tr_b16 a[108:109], v14 offset:43392\n"
    "v_accvgpr_read_b32 v18, a152\n"
    "v_accvgpr_read_b32 v11, a153\n"
    "v_mfma_f32_16x16x32_bf16 v[72:75], a[44:47], a[4:7], v[72:75]\n"
    "ds_read_b64_tr_b16 a[110:111], v14 offset:46976\n"
    "v_sub_f32 v18, v18, v25\n"
    "v_mfma_f32_16x16x32_bf16 v[76:79], a[48:51], a[4:7], v[76:79]\n"
    "ds_read_b64_tr_b16 a[112:113], v14 offset:50576\n"
    "v_add_f32 v10, v10, v11\n"
    "v_add_f32 v15, v15, v18\n"
    "v_mfma_f32_16x16x32_bf16 v[80:83], a[52:55], a[4:7], v[80:83]\n"
    "ds_read_b64_tr_b16 a[114:115], v14 offset:54160\n"
    "v_fmac_f32 v16, v18, v18\n"
    "v_accvgpr_read_b32 v18, a154\n"
    "v_mfma_f32_16x16x32_bf16 v[84:87], a[56:59], a[4:7], v[84:87]\n"
    "ds_read_b64_tr_b16 a[116:117], v9 offset:43392\n"
    "v_accvgpr_read_b32 v11, a155\n"
    "v_sub_f32 v18, v18, v25\n"
    "v_mfma_f32_16x16x32_bf16 a[164:167], a[36:39], a[120:123], a[164:167]\n"
    "ds_read_b64_tr_b16 a[118:119], v9 offset:46976\n"
    "v_add_f32 v10, v10, v11\n"
    "v_add_f32 v15, v15, v18\n"
    "s_waitcnt lgkmcnt(15)\n"
    "v_mfma_f32_16x16x32_bf16 v[88:91], a[32:35], a[8:11], v[88:91]\n"
    "ds_read_b128 a[64:67], v7 offset:50176\n"
    "v_fmac_f32 v16, v18, v18\n"
    "v_mfma_f32_16x16x32_bf16 v[92:95], a[36:39], a[8:11], v[92:95]\n"
    "ds_read_b128 a[68:71], v7 offset:51200\n"
    "v_accvgpr_read_b32 v18, a156\n"
    "v_accvgpr_read_b32 v11, a157\n"
    "v_mfma_f32_16x16x32_bf16 v[96:99], a[40:43], a[8:11], v[96:99]\n"
    "ds_read_b128 a[72:75], v7 offset:52224\n"
    "v_sub_f32 v18, v18, v25\n"
    "v_add_f32 v10, v10, v11\n"
    "v_mfma_f32_16x16x32_bf16 v[100:103], a[44:47], a[8:11], v[100:103]\n"
    "v_add_f32 v15, v15, v18\n"
    "v_fmac_f32 v16, v18, v18\n"
    "v_mfma_f32_16x16x32_bf16 v[104:107], a[48:51], a[8:11], v[104:107]\n"
    "v_accvgpr_read_b32 v18, a158\n"
    "v_accvgpr_read_b32 v11, a159\n"
    "v_mfma_f32_16x16x32_bf16 v[108:111], a[52:55], a[8:11], v[108:111]\n"
    "v_sub_f32 v18, v18, v25\n"
    "v_add_f32 v10, v10, v11\n"
    "v_mfma_f32_16x16x32_bf16 v[112:115], a[56:59], a[8:11], v[112:115]\n"
    "v_add_f32 v15, v15, v18\n"
    "v_mfma_f32_16x16x32_bf16 a[168:171], a[40:43], a[120:123], a[168:171]\n"
    "v_fmac_f32 v16, v18, v18\n"
    "v_mul_f32 v15, 0x3d800000, v15\n"
    "v_mfma_f32_16x16x32_bf16 v[116:119], a[32:35], a[12:15], v[116:119]\n"
    "v_add_f32 v25, v25, v15\n"
    "v_mul_f32 v15, v15, v15\n"
    "v_mfma_f32_16x16x32_bf16 v[120:123], a[36:39], a[12:15], v[120:123]\n"
    "v_fmac_f32 v16, 0xc1800000, v15\n"
    "v_fmac_f32 v10, 0x42800000, v16\n"
    "v_mfma_f32_16x16x32_bf16 v[124:127], a[40:43], a[12:15], v[124:127]\n"
    "v_mov_b32 v11, 0x3727c5ac\n"
    "v_fmac_f32 v11, 0x3a800000, v10\n"
    "v_mfma_f32_16x16x32_bf16 v[128:131], a[44:47], a[12:15], v[128:131]\n"
    "v_rcp_f32 v11, v11\n"
    "v_mfma_f32_16x16x32_bf16 v[132:135], a[48:51], a[12:15], v[132:135]\n"
    "v_mfma_f32_16x16x32_bf16 v[136:139], a[52:55], a[12:15], v[136:139]\n"
    "v_mfma_f32_16x16x32_bf16 v[140:143], a[56:59], a[12:15], v[140:143]\n"
    "v_mfma_f32_16x16x32_bf16 a[172:175], a[56:59], a[120:123], a[172:175]\n"
    "s_waitcnt lgkmcnt(15)\n"
    "v_mfma_f32_16x16x32_bf16 a[160:163], a[92:95], a[120:123], a[160:163]\n"
    "ds_write_b32 v19, v25\n"
    "s_waitcnt lgkmcnt(14)\n"
    "v_mfma_f32_16x16x32_bf16 a[164:167], a[96:99], a[120:123], a[164:167]\n"
    "global_store_dword v24, v11, s[76:77] sc1\n"
    "s_waitcnt lgkmcnt(12)\n"
    "v_mfma_f32_16x16x32_bf16 a[168:171], a[100:103], a[120:123], a[168:171]\n"
    "ds_read_b128 a[16:19], v7 offset:20480\n"
    "s_waitcnt lgkmcnt(5)\n"
    "v_mfma_f32_16x16x32_bf16 a[172:175], a[116:119], a[120:123], a[172:175]\n"
    "ds_read_b64_tr_b16 a[32:33], v8 offset:14464\n"
    "v_mfma_f32_16x16x32_bf16 v[32:35], a[92:95], a[60:63], v[32:35]\n"
    "ds_read_b64_tr_b16 a[34:35], v8 offset:18048\n"
    "v_mfma_f32_16x16x32_bf16 v[36:39], a[96:99], a[60:63], v[36:39]\n"
    "ds_read_b64_tr_b16 a[36:37], v8 offset:21648\n"
    "v_mfma_f32_16x16x32_bf16 v[40:43], a[100:103], a[60:63], v[40:43]\n"
    "ds_read_b64_tr_b16 a[38:39], v8 offset:25232\n"
    "v_mfma_f32_16x16x32_bf16 v[44:47], a[104:107], a[60:63], v[44:47]\n"
    "ds_read_b64_tr_b16 a[40:41], v12 offset:14464\n"
    "v_mfma_f32_16x16x32_bf16 v[48:51], a[108:111], a[60:63], v[48:51]\n"
    "ds_read_b64_tr_b16 a[42:43], v12 offset:18048\n"
    "v_mfma_f32_16x16x32_bf16 v[52:55], a[112:115], a[60:63], v[52:55]\n"
    "ds_read_b64_tr_b16 a[44:45], v13 offset:14464\n"
    "v_mfma_f32_16x16x32_bf16 v[56:59], a[116:119], a[60:63], v[56:59]\n"
    "ds_read_b64_tr_b16 a[46:47], v13 offset:18048\n"
    "s_waitcnt lgkmcnt(12)\n"
    "v_mfma_f32_16x16x32_bf16 v[60:63], a[92:95], a[64:67], v[60:63]\n"
    "v_mfma_f32_16x16x32_bf16 v[64:67], a[96:99], a[64:67], v[64:67]\n"
    "ds_write_b128 v20, a[160:163] offset:0\n"
    "v_mfma_f32_16x16x32_bf16 v[68:71], a[100:103], a[64:67], v[68:71]\n"
    "ds_write_b128 v21, a[160:163] offset:256\n"
    "v_mfma_f32_16x16x32_bf16 v[72:75], a[104:107], a[64:67], v[72:75]\n"
    "ds_write_b128 v20, a[164:167] offset:64\n"
    "ds_read_b64_tr_b16 a[48:49], v14 offset:14464\n"
    "v_mfma_f32_16x16x32_bf16 v[76:79], a[108:111], a[64:67], v[76:79]\n"
    "ds_read_b64_tr_b16 a[50:51], v14 offset:18048\n"
    "v_mfma_f32_16x16x32_bf16 v[80:83], a[112:115], a[64:67], v[80:83]\n"
    "ds_write_b128 v21, a[164:167] offset:320\n"
    "ds_read_b64_tr_b16 a[52:53], v14 offset:21648\n"
    "v_mfma_f32_16x16x32_bf16 v[84:87], a[116:119], a[64:67], v[84:87]\n"
    "ds_write_b128 v20, a[168:171] offset:128\n"
    "ds_read_b64_tr_b16 a[54:55], v14 offset:25232\n"
    "s_waitcnt lgkmcnt(15)\n"
    "v_mfma_f32_16x16x32_bf16 v[88:91], a[92:95], a[68:71], v[88:91]\n"
    "ds_read_b64_tr_b16 a[56:57], v9 offset:14464\n"
    "v_mfma_f32_16x16x32_bf16 v[92:95], a[96:99], a[68:71], v[92:95]\n"
    "ds_write_b128 v21, a[168:171] offset:192\n"
    "ds_read_b64_tr_b16 a[58:59], v9 offset:18048\n"
    "v_mfma_f32_16x16x32_bf16 v[96:99], a[100:103], a[68:71], v[96:99]\n"
    "ds_write_b128 v20, a[172:175] offset:384\n"
    "ds_read_b128 a[20:23], v7 offset:21504\n"
    "v_mfma_f32_16x16x32_bf16 v[100:103], a[104:107], a[68:71], v[100:103]\n"
    "ds_read_b128 a[24:27], v7 offset:22528\n"
    "v_mfma_f32_16x16x32_bf16 v[104:107], a[108:111], a[68:71], v[104:107]\n"
    "ds_read_b128 a[28:31], v7 offset:23552\n"
    "v_mfma_f32_16x16x32_bf16 v[108:111], a[112:115], a[68:71], v[108:111]\n"
    "v_mfma_f32_16x16x32_bf16 v[112:115], a[116:119], a[68:71], v[112:115]\n"
    "v_mfma_f32_16x16x32_bf16 v[116:119], a[92:95], a[72:75], v[116:119]\n"
    "v_mfma_f32_16x16x32_bf16 v[120:123], a[96:99], a[72:75], v[120:123]\n"
    "v_mfma_f32_16x16x32_bf16 v[124:127], a[100:103], a[72:75], v[124:127]\n"
    "v_mfma_f32_16x16x32_bf16 v[128:131], a[104:107], a[72:75], v[128:131]\n"
    "v_mfma_f32_16x16x32_bf16 v[132:135], a[108:111], a[72:75], v[132:135]\n"
    "v_mfma_f32_16x16x32_bf16 v[136:139], a[112:115], a[72:75], v[136:139]\n"
    "v_mfma_f32_16x16x32_bf16 v[140:143], a[116:119], a[72:75], v[140:143]\n"
    "s_waitcnt lgkmcnt(0)\n"
    "s_barrier\n"
    "v_mfma_f32_16x16x32_bf16 v[144:147], a[32:35], a[16:19], v[144:147]\n"
    "ds_read2_b32 v[20:21], v31 offset1:16\n"
    "ds_read2_b32 v[22:23], v31 offset0:32 offset1:48\n"
    "ds_read_b128 a[176:179], v30 offset:0\n"
    "v_mfma_f32_16x16x32_bf16 v[148:151], a[36:39], a[16:19], v[148:151]\n"
    "ds_read_b128 a[180:183], v30 offset:64\n"
    "ds_read_b128 a[184:187], v30 offset:128\n"
    "ds_read_b128 a[188:191], v30 offset:192\n"
    "v_mfma_f32_16x16x32_bf16 v[152:155], a[40:43], a[16:19], v[152:155]\n"
    "ds_read_b128 a[192:195], v30 offset:256\n"
    "ds_read_b128 a[196:199], v30 offset:320\n"
    "ds_read_b128 a[200:203], v30 offset:384\n"
    "v_mfma_f32_16x16x32_bf16 v[156:159], a[44:47], a[16:19], v[156:159]\n"
    "ds_read_b128 a[76:79], v7 offset:36864\n"
    "ds_read_b64_tr_b16 a[92:93], v8 offset:28928\n"
    "v_mfma_f32_16x16x32_bf16 v[160:163], a[48:51], a[16:19], v[160:163]\n"
    "ds_read_b64_tr_b16 a[94:95], v8 offset:32512\n"
    "v_mfma_f32_16x16x32_bf16 v[164:167], a[52:55], a[16:19], v[164:167]\n"
    "ds_read_b64_tr_b16 a[96:97], v8 offset:36112\n"
    "v_mfma_f32_16x16x32_bf16 v[168:171], a[56:59], a[16:19], v[168:171]\n"
    "ds_read_b64_tr_b16 a[98:99], v8 offset:39696\n"
    "s_waitcnt lgkmcnt(11)\n"
    "v_accvgpr_read_b32 v10, a176\n"
    "v_accvgpr_read_b32 v11, a177\n"
    "v_accvgpr_read_b32 v15, a178\n"
    "v_accvgpr_read_b32 v16, a179\n"
    "v_mfma_f32_16x16x32_bf16 v[172:175], a[32:35], a[20:23], v[172:175]\n"
    "ds_read_b64_tr_b16 a[100:101], v12 offset:28928\n"
    "s_waitcnt lgkmcnt(11)\n"
    "v_accvgpr_read_b32 v17, a180\n"
    "v_accvgpr_read_b32 v18, a181\n"
    "v_accvgpr_read_b32 v24, a182\n"
    "v_mfma_f32_16x16x32_bf16 v[176:179], a[36:39], a[20:23], v[176:179]\n"
    "ds_read_b64_tr_b16 a[102:103], v12 offset:32512\n"
    "v_accvgpr_read_b32 v25, a183\n"
    "v_fma_f32 v32, -v20, v10, v32\n"
    "v_fma_f32 v33, -v20, v11, v33\n"
    "v_mfma_f32_16x16x32_bf16 v[180:183], a[40:43], a[20:23], v[180:183]\n"
    "ds_read_b64_tr_b16 a[104:105], v13 offset:28928\n"
    "v_fma_f32 v34, -v20, v15, v34\n"
    "v_fma_f32 v35, -v20, v16, v35\n"
    "v_fma_f32 v36, -v20, v17, v36\n"
    "v_mfma_f32_16x16x32_bf16 v[184:187], a[44:47], a[20:23], v[184:187]\n"
    "ds_read_b64_tr_b16 a[106:107], v13 offset:32512\n"
    "v_fma_f32 v37, -v20, v18, v37\n"
    "v_fma_f32 v38, -v20, v24, v38\n"
    "v_fma_f32 v39, -v20, v25, v39\n"
    "v_mfma_f32_16x16x32_bf16 v[188:191], a[48:51], a[20:23], v[188:191]\n"
    "ds_read_b64_tr_b16 a[108:109], v14 offset:28928\n"
    "v_mul_f32 v32, v32, v36\n"
    "v_mul_f32 v33, v33, v37\n"
    "v_mul_f32 v34, v34, v38\n"
    "v_mul_f32 v35, v35, v39\n"
    "v_mfma_f32_16x16x32_bf16 v[192:195], a[52:55], a[20:23], v[192:195]\n"
    "ds_read_b64_tr_b16 a[110:111], v14 offset:32512\n"
    "v_cvt_pk_bf16_f32 v32, v32, v33\n"
    "v_cvt_pk_bf16_f32 v33, v34, v35\n"
    "global_store_dwordx2 v26, v[32:33], s[78:79] offset:0 sc1\n"
    "v_mfma_f32_16x16x32_bf16 v[196:199], a[56:59], a[20:23], v[196:199]\n"
    "ds_read_b64_tr_b16 a[112:113], v14 offset:36112\n"
    "v_fma_f32 v60, -v21, v10, v60\n"
    "v_fma_f32 v61, -v21, v11, v61\n"
    "v_fma_f32 v62, -v21, v15, v62\n"
    "v_mfma_f32_16x16x32_bf16 v[200:203], a[32:35], a[24:27], v[200:203]\n"
    "ds_read_b64_tr_b16 a[114:115], v14 offset:39696\n"
    "v_fma_f32 v63, -v21, v16, v63\n"
    "v_fma_f32 v64, -v21, v17, v64\n"
    "v_fma_f32 v65, -v21, v18, v65\n"
    "v_mfma_f32_16x16x32_bf16 v[204:207], a[36:39], a[24:27], v[204:207]\n"
    "ds_read_b64_tr_b16 a[116:117], v9 offset:28928\n"
    "v_fma_f32 v66, -v21, v24, v66\n"
    "v_fma_f32 v67, -v21, v25, v67\n"
    "v_mul_f32 v60, v60, v64\n"
    "v_mfma_f32_16x16x32_bf16 v[208:211], a[40:43], a[24:27], v[208:211]\n"
    "ds_read_b64_tr_b16 a[118:119], v9 offset:32512\n"
    "v_mul_f32 v61, v61, v65\n"
    "v_mul_f32 v62, v62, v66\n"
    "v_mul_f32 v63, v63, v67\n"
    "v_mfma_f32_16x16x32_bf16 v[212:215], a[44:47], a[24:27], v[212:215]\n"
    "ds_read_b128 a[80:83], v7 offset:37888\n"
    "v_cvt_pk_bf16_f32 v60, v60, v61\n"
    "v_cvt_pk_bf16_f32 v61, v62, v63\n"
    "global_store_dwordx2 v26, v[60:61], s[78:79] offset:1024 sc1\n"
    "v_fma_f32 v88, -v22, v10, v88\n"
    "v_mfma_f32_16x16x32_bf16 v[216:219], a[48:51], a[24:27], v[216:219]\n"
    "ds_read_b128 a[84:87], v7 offset:38912\n"
    "v_fma_f32 v89, -v22, v11, v89\n"
    "v_fma_f32 v90, -v22, v15, v90\n"
    "v_fma_f32 v91, -v22, v16, v91\n"
    "v_mfma_f32_16x16x32_bf16 v[220:223], a[52:55], a[24:27], v[220:223]\n"
    "ds_read_b128 a[88:91], v7 offset:39936\n"
    "v_fma_f32 v92, -v22, v17, v92\n"
    "v_fma_f32 v93, -v22, v18, v93\n"
    "v_fma_f32 v94, -v22, v24, v94\n"
    "v_mfma_f32_16x16x32_bf16 v[224:227], a[56:59], a[24:27], v[224:227]\n"
    "v_fma_f32 v95, -v22, v25, v95\n"
    "v_mul_f32 v88, v88, v92\n"
    "v_mul_f32 v89, v89, v93\n"
    "v_mfma_f32_16x16x32_bf16 v[228:231], a[32:35], a[28:31], v[228:231]\n"
    "v_mul_f32 v90, v90, v94\n"
    "v_mul_f32 v91, v91, v95\n"
    "v_cvt_pk_bf16_f32 v88, v88, v89\n"
    "v_mfma_f32_16x16x32_bf16 v[232:235], a[36:39], a[28:31], v[232:235]\n"
    "v_cvt_pk_bf16_f32 v89, v90, v91\n"
    "global_store_dwordx2 v26, v[88:89], s[78:79] offset:2048 sc1\n"
    "v_fma_f32 v116, -v23, v10, v116\n"
    "v_fma_f32 v117, -v23, v11, v117\n"
    "v_mfma_f32_16x16x32_bf16 v[236:239], a[40:43], a[28:31], v[236:239]\n"
    "v_fma_f32 v118, -v23, v15, v118\n"
    "v_fma_f32 v119, -v23, v16, v119\n"
    "v_fma_f32 v120, -v23, v17, v120\n"
    "v_mfma_f32_16x16x32_bf16 v[240:243], a[44:47], a[28:31], v[240:243]\n"
    "v_fma_f32 v121, -v23, v18, v121\n"
    "v_fma_f32 v122, -v23, v24, v122\n"
    "v_fma_f32 v123, -v23, v25, v123\n"
    "v_mfma_f32_16x16x32_bf16 v[244:247], a[48:51], a[28:31], v[244:247]\n"
    "v_mul_f32 v116, v116, v120\n"
    "v_mul_f32 v117, v117, v121\n"
    "v_mul_f32 v118, v118, v122\n"
    "v_mfma_f32_16x16x32_bf16 v[248:251], a[52:55], a[28:31], v[248:251]\n"
    "v_mul_f32 v119, v119, v123\n"
    "v_cvt_pk_bf16_f32 v116, v116, v117\n"
    "v_cvt_pk_bf16_f32 v117, v118, v119\n"
    "v_mfma_f32_16x16x32_bf16 v[252:255], a[56:59], a[28:31], v[252:255]\n"
    "global_store_dwordx2 v26, v[116:117], s[78:79] offset:3072 sc1\n"
    "s_waitcnt lgkmcnt(15)\n"
    "v_accvgpr_read_b32 v10, a184\n"
    "v_accvgpr_read_b32 v11, a185\n"
    "v_mfma_f32_16x16x32_bf16 v[144:147], a[92:95], a[76:79], v[144:147]\n"
    "ds_read_b128 a[16:19], v7 offset:53248\n"
    "v_accvgpr_read_b32 v15, a186\n"
    "v_accvgpr_read_b32 v16, a187\n"
    "v_accvgpr_read_b32 v17, a188\n"
    "v_accvgpr_read_b32 v18, a189\n"
    "s_waitcnt lgkmcnt(14)\n"
    "v_mfma_f32_16x16x32_bf16 v[148:151], a[96:99], a[76:79], v[148:151]\n"
    "ds_read_b64_tr_b16 a[32:33], v8 offset:43392\n"
    "v_accvgpr_read_b32 v24, a190\n"
    "v_accvgpr_read_b32 v25, a191\n"
    "v_fma_f32 v40, -v20, v10, v40\n"
    "s_waitcnt lgkmcnt(13)\n"
    "v_mfma_f32_16x16x32_bf16 v[152:155], a[100:103], a[76:79], v[152:155]\n"
    "ds_read_b64_tr_b16 a[34:35], v8 offset:46976\n"
    "v_fma_f32 v41, -v20, v11, v41\n"
    "v_fma_f32 v42, -v20, v15, v42\n"
    "v_fma_f32 v43, -v20, v16, v43\n"
    "s_waitcnt lgkmcnt(12)\n"
    "v_mfma_f32_16x16x32_bf16 v[156:159], a[104:107], a[76:79], v[156:159]\n"
    "ds_read_b64_tr_b16 a[36:37], v8 offset:50576\n"
    "v_fma_f32 v44, -v20, v17, v44\n"
    "v_fma_f32 v45, -v20, v18, v45\n"
    "v_fma_f32 v46, -v20, v24, v46\n"
    "s_waitcnt lgkmcnt(11)\n"
    "v_mfma_f32_16x16x32_bf16 v[160:163], a[108:111], a[76:79], v[160:163]\n"
    "ds_read_b64_tr_b16 a[38:39], v8 offset:54160\n"
    "v_fma_f32 v47, -v20, v25, v47\n"
    "v_mul_f32 v40, v40, v44\n"
    "v_mul_f32 v41, v41, v45\n"
    "s_waitcnt lgkmcnt(10)\n"
    "v_mfma_f32_16x16x32_bf16 v[164:167], a[112:115], a[76:79], v[164:167]\n"
    "ds_read_b64_tr_b16 a[40:41], v12 offset:43392\n"
    "v_mul_f32 v42, v42, v46\n"
    "v_mul_f32 v43, v43, v47\n"
    "v_cvt_pk_bf16_f32 v40, v40, v41\n"
    "s_waitcnt lgkmcnt(9)\n"
    "v_mfma_f32_16x16x32_bf16 v[168:171], a[116:119], a[76:79], v[168:171]\n"
    "ds_read_b64_tr_b16 a[42:43], v12 offset:46976\n"
    "v_cvt_pk_bf16_f32 v41, v42, v43\n"
    "global_store_dwordx2 v27, v[40:41], s[78:79] offset:0 sc1\n"
    "v_fma_f32 v68, -v21, v10, v68\n"
    "s_waitcnt lgkmcnt(9)\n"
    "v_mfma_f32_16x16x32_bf16 v[172:175], a[92:95], a[80:83], v[172:175]\n"
    "ds_read_b64_tr_b16 a[44:45], v13 offset:43392\n"
    "v_fma_f32 v69, -v21, v11, v69\n"
    "v_fma_f32 v70, -v21, v15, v70\n"
    "v_fma_f32 v71, -v21, v16, v71\n"
    "v_fma_f32 v72, -v21, v17, v72\n"
    "v_mfma_f32_16x16x32_bf16 v[176:179], a[96:99], a[80:83], v[176:179]\n"
    "ds_read_b64_tr_b16 a[46:47], v13 offset:46976\n"
    "v_fma_f32 v73, -v21, v18, v73\n"
    "v_fma_f32 v74, -v21, v24, v74\n"
    "v_fma_f32 v75, -v21, v25, v75\n"
    "v_mfma_f32_16x16x32_bf16 v[180:183], a[100:103], a[80:83], v[180:183]\n"
    "v_mul_f32 v68, v68, v72\n"
    "v_mul_f32 v69, v69, v73\n"
    "v_mul_f32 v70, v70, v74\n"
    "v_mfma_f32_16x16x32_bf16 v[184:187], a[104:107], a[80:83], v[184:187]\n"
    "ds_read_b64_tr_b16 a[48:49], v14 offset:43392\n"
    "v_mul_f32 v71, v71, v75\n"
    "v_cvt_pk_bf16_f32 v68, v68, v69\n"
    "v_cvt_pk_bf16_f32 v69, v70, v71\n"
    "v_mfma_f32_16x16x32_bf16 v[188:191], a[108:111], a[80:83], v[188:191]\n"
    "ds_read_b64_tr_b16 a[50:51], v14 offset:46976\n"
    "global_store_dwordx2 v27, v[68:69], s[78:79] offset:1024 sc1\n"
    "v_fma_f32 v96, -v22, v10, v96\n"
    "v_fma_f32 v97, -v22, v11, v97\n"
    "v_mfma_f32_16x16x32_bf16 v[192:195], a[112:115], a[80:83], v[192:195]\n"
    "ds_read_b64_tr_b16 a[52:53], v14 offset:50576\n"
    "v_fma_f32 v98, -v22, v15, v98\n"
    "v_fma_f32 v99, -v22, v16, v99\n"
    "v_fma_f32 v100, -v22, v17, v100\n"
    "v_mfma_f32_16x16x32_bf16 v[196:199], a[116:119], a[80:83], v[196:199]\n"
    "ds_read_b64_tr_b16 a[54:55], v14 offset:54160\n"
    "v_fma_f32 v101, -v22, v18, v101\n"
    "v_fma_f32 v102, -v22, v24, v102\n"
    "v_fma_f32 v103, -v22, v25, v103\n"
    "s_waitcnt lgkmcnt(14)\n"
    "v_mfma_f32_16x16x32_bf16 v[200:203], a[92:95], a[84:87], v[200:203]\n"
    "ds_read_b64_tr_b16 a[56:57], v9 offset:43392\n"
    "v_mul_f32 v96, v96, v100\n"
    "v_mul_f32 v97, v97, v101\n"
    "v_mul_f32 v98, v98, v102\n"
    "v_mul_f32 v99, v99, v103\n"
    "v_mfma_f32_16x16x32_bf16 v[204:207], a[96:99], a[84:87], v[204:207]\n"
    "ds_read_b64_tr_b16 a[58:59], v9 offset:46976\n"
    "v_cvt_pk_bf16_f32 v96, v96, v97\n"
    "v_cvt_pk_bf16_f32 v97, v98, v99\n"
    "global_store_dwordx2 v27, v[96:97], s[78:79] offset:2048 sc1\n"
    "v_mfma_f32_16x16x32_bf16 v[208:211], a[100:103], a[84:87], v[208:211]\n"
    "ds_read_b128 a[20:23], v7 offset:54272\n"
    "v_fma_f32 v124, -v23, v10, v124\n"
    "v_fma_f32 v125, -v23, v11, v125\n"
    "v_fma_f32 v126, -v23, v15, v126\n"
    "v_mfma_f32_16x16x32_bf16 v[212:215], a[104:107], a[84:87], v[212:215]\n"
    "ds_read_b128 a[24:27], v7 offset:55296\n"
    "v_fma_f32 v127, -v23, v16, v127\n"
    "v_fma_f32 v128, -v23, v17, v128\n"
    "v_fma_f32 v129, -v23, v18, v129\n"
    "v_mfma_f32_16x16x32_bf16 v[216:219], a[108:111], a[84:87], v[216:219]\n"
    "ds_read_b128 a[28:31], v7 offset:56320\n"
    "v_fma_f32 v130, -v23, v24, v130\n"
    "v_fma_f32 v131, -v23, v25, v131\n"
    "v_mul_f32 v124, v124, v128\n"
    "v_mfma_f32_16x16x32_bf16 v[220:223], a[112:115], a[84:87], v[220:223]\n"
    "v_mul_f32 v125, v125, v129\n"
    "v_mul_f32 v126, v126, v130\n"
    "v_mul_f32 v127, v127, v131\n"
    "v_mfma_f32_16x16x32_bf16 v[224:227], a[116:119], a[84:87], v[224:227]\n"
    "v_cvt_pk_bf16_f32 v124, v124, v125\n"
    "v_cvt_pk_bf16_f32 v125, v126, v127\n"
    "global_store_dwordx2 v27, v[124:125], s[78:79] offset:3072 sc1\n"
    "s_waitcnt lgkmcnt(15)\n"
    "v_mfma_f32_16x16x32_bf16 v[228:231], a[92:95], a[88:91], v[228:231]\n"
    "v_accvgpr_read_b32 v10, a192\n"
    "v_accvgpr_read_b32 v11, a193\n"
    "v_accvgpr_read_b32 v15, a194\n"
    "v_accvgpr_read_b32 v16, a195\n"
    "v_mfma_f32_16x16x32_bf16 v[232:235], a[96:99], a[88:91], v[232:235]\n"
    "v_accvgpr_read_b32 v17, a196\n"
    "v_accvgpr_read_b32 v18, a197\n"
    "v_accvgpr_read_b32 v24, a198\n"
    "v_mfma_f32_16x16x32_bf16 v[236:239], a[100:103], a[88:91], v[236:239]\n"
    "v_accvgpr_read_b32 v25, a199\n"
    "v_fma_f32 v48, -v20, v10, v48\n"
    "v_fma_f32 v49, -v20, v11, v49\n"
    "v_mfma_f32_16x16x32_bf16 v[240:243], a[104:107], a[88:91], v[240:243]\n"
    "v_fma_f32 v50, -v20, v15, v50\n"
    "v_fma_f32 v51, -v20, v16, v51\n"
    "v_fma_f32 v52, -v20, v17, v52\n"
    "v_mfma_f32_16x16x32_bf16 v[244:247], a[108:111], a[88:91], v[244:247]\n"
    "v_fma_f32 v53, -v20, v18, v53\n"
    "v_fma_f32 v54, -v20, v24, v54\n"
    "v_fma_f32 v55, -v20, v25, v55\n"
    "v_mfma_f32_16x16x32_bf16 v[248:251], a[112:115], a[88:91], v[248:251]\n"
    "v_mul_f32 v48, v48, v52\n"
    "v_mul_f32 v49, v49, v53\n"
    "v_mul_f32 v50, v50, v54\n"
    "v_mfma_f32_16x16x32_bf16 v[252:255], a[116:119], a[88:91], v[252:255]\n"
    "v_mul_f32 v51, v51, v55\n"
    "v_cvt_pk_bf16_f32 v48, v48, v49\n"
    "v_cvt_pk_bf16_f32 v49, v50, v51\n"
    "v_mfma_f32_16x16x32_bf16 v[144:147], a[32:35], a[16:19], v[144:147]\n"
    "global_store_dwordx2 v28, v[48:49], s[78:79] offset:0 sc1\n"
    "v_fma_f32 v76, -v21, v10, v76\n"
    "v_fma_f32 v77, -v21, v11, v77\n"
    "v_fma_f32 v78, -v21, v15, v78\n"
    "s_waitcnt lgkmcnt(13)\n"
    "v_mfma_f32_16x16x32_bf16 v[148:151], a[36:39], a[16:19], v[148:151]\n"
    "v_fma_f32 v79, -v21, v16, v79\n"
    "v_fma_f32 v80, -v21, v17, v80\n"
    "v_fma_f32 v81, -v21, v18, v81\n"
    "s_waitcnt lgkmcnt(11)\n"
    "v_mfma_f32_16x16x32_bf16 v[152:155], a[40:43], a[16:19], v[152:155]\n"
    "v_fma_f32 v82, -v21, v24, v82\n"
    "v_fma_f32 v83, -v21, v25, v83\n"
    "v_mul_f32 v76, v76, v80\n"
    "v_mul_f32 v77, v77, v81\n"
    "s_waitcnt lgkmcnt(9)\n"
    "v_mfma_f32_16x16x32_bf16 v[156:159], a[44:47], a[16:19], v[156:159]\n"
    "v_mul_f32 v78, v78, v82\n"
    "v_mul_f32 v79, v79, v83\n"
    "v_cvt_pk_bf16_f32 v76, v76, v77\n"
    "s_waitcnt lgkmcnt(7)\n"
    "v_mfma_f32_16x16x32_bf16 v[160:163], a[48:51], a[16:19], v[160:163]\n"
    "v_cvt_pk_bf16_f32 v77, v78, v79\n"
    "global_store_dwordx2 v28, v[76:77], s[78:79] offset:1024 sc1\n"
    "v_fma_f32 v104, -v22, v10, v104\n"
    "s_waitcnt lgkmcnt(5)\n"
    "v_mfma_f32_16x16x32_bf16 v[164:167], a[52:55], a[16:19], v[164:167]\n"
    "v_fma_f32 v105, -v22, v11, v105\n"
    "v_fma_f32 v106, -v22, v15, v106\n"
    "v_fma_f32 v107, -v22, v16, v107\n"
    "v_fma_f32 v108, -v22, v17, v108\n"
    "s_waitcnt lgkmcnt(3)\n"
    "v_mfma_f32_16x16x32_bf16 v[168:171], a[56:59], a[16:19], v[168:171]\n"
    "v_fma_f32 v109, -v22, v18, v109\n"
    "v_fma_f32 v110, -v22, v24, v110\n"
    "v_fma_f32 v111, -v22, v25, v111\n"
    "s_waitcnt lgkmcnt(2)\n"
    "v_mfma_f32_16x16x32_bf16 v[172:175], a[32:35], a[20:23], v[172:175]\n"
    "v_mul_f32 v104, v104, v108\n"
    "v_mul_f32 v105, v105, v109\n"
    "v_mul_f32 v106, v106, v110\n"
    "v_mul_f32 v107, v107, v111\n"
    "v_mfma_f32_16x16x32_bf16 v[176:179], a[36:39], a[20:23], v[176:179]\n"
    "v_cvt_pk_bf16_f32 v104, v104, v105\n"
    "v_cvt_pk_bf16_f32 v105, v106, v107\n"
    "global_store_dwordx2 v28, v[104:105], s[78:79] offset:2048 sc1\n"
    "v_mfma_f32_16x16x32_bf16 v[180:183], a[40:43], a[20:23], v[180:183]\n"
    "v_fma_f32 v132, -v23, v10, v132\n"
    "v_fma_f32 v133, -v23, v11, v133\n"
    "v_fma_f32 v134, -v23, v15, v134\n"
    "v_mfma_f32_16x16x32_bf16 v[184:187], a[44:47], a[20:23], v[184:187]\n"
    "v_fma_f32 v135, -v23, v16, v135\n"
    "v_fma_f32 v136, -v23, v17, v136\n"
    "v_fma_f32 v137, -v23, v18, v137\n"
    "v_fma_f32 v138, -v23, v24, v138\n"
    "v_mfma_f32_16x16x32_bf16 v[188:191], a[48:51], a[20:23], v[188:191]\n"
    "v_fma_f32 v139, -v23, v25, v139\n"
    "v_mul_f32 v132, v132, v136\n"
    "v_mul_f32 v133, v133, v137\n"
    "v_mfma_f32_16x16x32_bf16 v[192:195], a[52:55], a[20:23], v[192:195]\n"
    "v_mul_f32 v134, v134, v138\n"
    "v_mul_f32 v135, v135, v139\n"
    "v_cvt_pk_bf16_f32 v132, v132, v133\n"
    "v_mfma_f32_16x16x32_bf16 v[196:199], a[56:59], a[20:23], v[196:199]\n"
    "v_cvt_pk_bf16_f32 v133, v134, v135\n"
    "global_store_dwordx2 v28, v[132:133], s[78:79] offset:3072 sc1\n"
    "v_accvgpr_read_b32 v10, a200\n"
    "v_accvgpr_read_b32 v11, a201\n"
    "s_waitcnt lgkmcnt(1)\n"
    "v_mfma_f32_16x16x32_bf16 v[200:203], a[32:35], a[24:27], v[200:203]\n"
    "v_accvgpr_read_b32 v15, a202\n"
    "v_accvgpr_read_b32 v16, a203\n"
    "v_fma_f32 v56, -v20, v10, v56\n"
    "v_mfma_f32_16x16x32_bf16 v[204:207], a[36:39], a[24:27], v[204:207]\n"
    "v_fma_f32 v57, -v20, v11, v57\n"
    "v_fma_f32 v58, -v20, v15, v58\n"
    "v_fma_f32 v59, -v20, v16, v59\n"
    "v_fma_f32 v84, -v21, v10, v84\n"
    "v_mfma_f32_16x16x32_bf16 v[208:211], a[40:43], a[24:27], v[208:211]\n"
    "v_fma_f32 v85, -v21, v11, v85\n"
    "v_fma_f32 v86, -v21, v15, v86\n"
    "v_fma_f32 v87, -v21, v16, v87\n"
    "ds_read_b128 v[32:35], v30 offset:0\n"
    "v_mfma_f32_16x16x32_bf16 v[212:215], a[44:47], a[24:27], v[212:215]\n"
    "v_fma_f32 v112, -v22, v10, v112\n"
    "v_fma_f32 v113, -v22, v11, v113\n"
    "v_fma_f32 v114, -v22, v15, v114\n"
    "ds_read_b128 v[36:39], v30 offset:64\n"
    "v_mfma_f32_16x16x32_bf16 v[216:219], a[48:51], a[24:27], v[216:219]\n"
    "v_fma_f32 v115, -v22, v16, v115\n"
    "v_fma_f32 v140, -v23, v10, v140\n"
    "v_fma_f32 v141, -v23, v11, v141\n"
    "v_fma_f32 v142, -v23, v15, v142\n"
    "ds_read_b128 v[40:43], v30 offset:128\n"
    "v_mfma_f32_16x16x32_bf16 v[220:223], a[52:55], a[24:27], v[220:223]\n"
    "v_fma_f32 v143, -v23, v16, v143\n"
    "v_permlane32_swap_b32 v56, v84\n"
    "v_permlane32_swap_b32 v57, v85\n"
    "ds_read_b128 v[44:47], v30 offset:192\n"
    "v_mfma_f32_16x16x32_bf16 v[224:227], a[56:59], a[24:27], v[224:227]\n"
    "v_permlane32_swap_b32 v58, v86\n"
    "v_permlane32_swap_b32 v59, v87\n"
    "v_mul_f32 v56, v56, v84\n"
    "v_mul_f32 v57, v57, v85\n"
    "ds_read_b128 v[48:51], v30 offset:256\n"
    "s_waitcnt lgkmcnt(5)\n"
    "v_mfma_f32_16x16x32_bf16 v[228:231], a[32:35], a[28:31], v[228:231]\n"
    "v_mul_f32 v58, v58, v86\n"
    "v_mul_f32 v59, v59, v87\n"
    "v_cvt_pk_bf16_f32 v56, v56, v57\n"
    "ds_read_b128 v[52:55], v30 offset:320\n"
    "v_mfma_f32_16x16x32_bf16 v[232:235], a[36:39], a[28:31], v[232:235]\n"
    "v_cvt_pk_bf16_f32 v57, v58, v59\n"
    "global_store_dwordx2 v29, v[56:57], s[78:79] offset:0 sc1\n"
    "v_permlane32_swap_b32 v112, v140\n"
    "ds_read2_b32 v[60:61], v31 offset0:64 offset1:80\n"
    "v_mfma_f32_16x16x32_bf16 v[236:239], a[40:43], a[28:31], v[236:239]\n"
    "v_permlane32_swap_b32 v113, v141\n"
    "v_permlane32_swap_b32 v114, v142\n"
    "v_permlane32_swap_b32 v115, v143\n"
    "v_mul_f32 v112, v112, v140\n"
    "ds_read2_b32 v[62:63], v31 offset0:96 offset1:112\n"
    "v_mfma_f32_16x16x32_bf16 v[240:243], a[44:47], a[28:31], v[240:243]\n"
    "v_mul_f32 v113, v113, v141\n"
    "v_mul_f32 v114, v114, v142\n"
    "v_mul_f32 v115, v115, v143\n"
    "v_mfma_f32_16x16x32_bf16 v[244:247], a[48:51], a[28:31], v[244:247]\n"
    "v_cvt_pk_bf16_f32 v112, v112, v113\n"
    "v_cvt_pk_bf16_f32 v113, v114, v115\n"
    "global_store_dwordx2 v29, v[112:113], s[78:79] offset:2048 sc1\n"
    "v_mfma_f32_16x16x32_bf16 v[248:251], a[52:55], a[28:31], v[248:251]\n"
    "v_mfma_f32_16x16x32_bf16 v[252:255], a[56:59], a[28:31], v[252:255]\n"
    "ds_read_b128 v[64:67], v30 offset:384\n"
    "s_waitcnt lgkmcnt(0)\n"
    "s_nop 7\n"
    :
      "=&{v[32:63]}"(ce), "=&{v[64:79]}"(cm),
      "=&{v[144:159]}"(c[7]), "=&{v[160:175]}"(c[8]), "=&{v[176:191]}"(c[9]), "=&{v[192:207]}"(c[10]), "=&{v[208:223]}"(c[11]), "=&{v[224:239]}"(c[12]), "=&{v[240:255]}"(c[13]),
      "+{v[2:17]}"(ia), "+{v[18:25]}"(ib), "+{v[26:29]}"(ic), "+{v[30:31]}"(id), "+{s[40:41]}"(pA), "+{s[42:43]}"(pB), "+{s[48:49]}"(pS), "+{s[74:75]}"(pE)
    : "{s44}"(m0A), "{s45}"(m0B), "{s47}"(lb + L_SCR), "{s[50:51]}"(pC), "{s[72:73]}"(A.wd), "{s[76:77]}"(pR), "{s[78:79]}"(ws)
    : "memory", "scc", "v80", "v81", "v82", "v83", "v84", "v85", "v86", "v87", "v88", "v89", "v90", "v91", "v92", "v93", "v94", "v95", "v96", "v97", "v98", "v99", "v100", "v101", "v102", "v103", "v104", "v105", "v106", "v107", "v108", "v109", "v110", "v111", "v112", "v113", "v114", "v115", "v116", "v117", "v118", "v119", "v120", "v121", "v122", "v123", "v124", "v125", "v126", "v127", "v128", "v129", "v130", "v131", "v132", "v133", "v134", "v135", "v136", "v137", "v138", "v139", "v140", "v141", "v142", "v143", "a0", "a1", "a2", "a3", "a4", "a5", "a6", "a7", "a8", "a9", "a10", "a11", "a12", "a13", "a14", "a15", "a16", "a17", "a18", "a19", "a20", "a21", "a22", "a23", "a24", "a25", "a26", "a27", "a28", "a29", "a30", "a31", "a32", "a33", "a34", "a35", "a36", "a37", "a38", "a39", "a40", "a41", "a42", "a43", "a44", "a45", "a46", "a47", "a48", "a49", "a50", "a51", "a52", "a53", "a54", "a55", "a56", "a57", "a58", "a59", "a60", "a61", "a62", "a63", "a64", "a65", "a66", "a67", "a68", "a69", "a70", "a71", "a72", "a73", "a74", "a75", "a76", "a77", "a78", "a79", "a80", "a81", "a82", "a83", "a84", "a85", "a86", "a87", "a88", "a89", "a90", "a91", "a92", "a93", "a94", "a95", "a96", "a97", "a98", "a99", "a100", "a101", "a102", "a103", "a104", "a105", "a106", "a107", "a108", "a109", "a110", "a111", "a112", "a113", "a114", "a115", "a116", "a117", "a118", "a119", "a120", "a121", "a122", "a123", "a124", "a125", "a126", "a127", "a128", "a129", "a130", "a131", "a132", "a133", "a134", "a135", "a136", "a137", "a138", "a139", "a140", "a141", "a142", "a143", "a144", "a145", "a146", "a147", "a148", "a149", "a150", "a151", "a152", "a153", "a154", "a155", "a156", "a157", "a158", "a159", "a160", "a161", "a162", "a163", "a164", "a165", "a166", "a167", "a168", "a169", "a170", "a171", "a172", "a173", "a174", "a175", "a176", "a177", "a178", "a179", "a180", "a181", "a182", "a183", "a184", "a185", "a186", "a187", "a188", "a189", "a190", "a191", "a192", "a193", "a194", "a195", "a196", "a197", "a198", "a199", "a200", "a201", "a202", "a203", "s46", "s70", "s71");
  } else {
    asm volatile(
    "s_nop 4\n"
    "s_add_u32 m0, s44, 0\n"
    "s_nop 0\n"
    "global_load_lds_dwordx4 v2, s[40:41] offset:0 sc1\n"
    "global_load_lds_dwordx4 v2, s[40:41] offset:1024 sc1\n"
    "global_load_lds_dwordx4 v2, s[40:41] offset:2048 sc1\n"
    "global_load_lds_dwordx4 v2, s[40:41] offset:3072 sc1\n"
    "s_add_u32 s40, s40, 8192\n"
    "s_addc_u32 s41, s41, 0\n"
    "s_add_u32 m0, s44, 16384\n"
    "s_nop 0\n"
    "global_load_lds_dwordx4 v2, s[40:41] offset:0 sc1\n"
    "global_load_lds_dwordx4 v2, s[40:41] offset:1024 sc1\n"
    "global_load_lds_dwordx4 v2, s[40:41] offset:2048 sc1\n"
    "global_load_lds_dwordx4 v2, s[40:41] offset:3072 sc1\n"
    "s_add_u32 s40, s40, 8192\n"
    "s_addc_u32 s41, s41, 0\n"
    "s_add_u32 m0, s44, 32768\n"
    "s_nop 0\n"
    "global_load_lds_dwordx4 v2, s[40:41] offset:0 sc1\n"
    "global_load_lds_dwordx4 v2, s[40:41] offset:1024 sc1\n"
    "global_load_lds_dwordx4 v2, s[40:41] offset:2048 sc1\n"
    "global_load_lds_dwordx4 v2, s[40:41] offset:3072 sc1\n"
    "s_add_u32 s40, s40, 8192\n"
    "s_addc_u32 s41, s41, 0\n"
    "v_accvgpr_write_b32 a124, -1\n"
    "v_accvgpr_write_b32 a125, -1\n"
    "v_accvgpr_write_b32 a126, -1\n"
    "v_accvgpr_write_b32 a127, -1\n"
    "s_waitcnt vmcnt(8)\n"
    "s_barrier\n"
    "ds_read_b64_tr_b16 a[32:33], v8 offset:0\n"
    "ds_read_b64_tr_b16 a[34:35], v8 offset:3584\n"
    "ds_read_b128 a[0:3], v7 offset:0\n"
    "ds_read_b64_tr_b16 a[36:37], v8 offset:7184\n"
    "ds_read_b64_tr_b16 a[38:39], v8 offset:10768\n"
    "ds_read_b128 a[4:7], v7 offset:1024\n"
    "ds_read_b64_tr_b16 a[40:41], v12 offset:0\n"
    "ds_read_b64_tr_b16 a[42:43], v12 offset:3584\n"
    "ds_read_b128 a[8:11], v7 offset:2048\n"
    "ds_read_b64_tr_b16 a[44:45], v13 offset:0\n"
    "ds_read_b64_tr_b16 a[46:47], v13 offset:3584\n"
    "ds_read_b128 a[12:15], v7 offset:3072\n"
    "ds_read_b64_tr_b16 a[48:49], v14 offset:0\n"
    "ds_read_b64_tr_b16 a[50:51], v14 offset:3584\n"
    "ds_read_b128 a[16:19], v7 offset:4096\n"
    "ds_read_b64_tr_b16 a[52:53], v14 offset:7184\n"
    "ds_read_b64_tr_b16 a[54:55], v14 offset:10768\n"
    "ds_read_b128 a[20:23], v7 offset:5120\n"
    "ds_read_b64_tr_b16 a[56:57], v9 offset:0\n"
    "ds_read_b64_tr_b16 a[58:59], v9 offset:3584\n"
    "ds_read_b128 a[24:27], v7 offset:6144\n"
    "ds_read_b128 a[28:31], v7 offset:7168\n"
    "s_add_u32 m0, s44, 49152\n"
    "s_nop 0\n"
    "global_load_lds_dwordx4 v2, s[40:41] offset:0 sc1\n"
    "global_load_lds_dwordx4 v2, s[40:41] offset:1024 sc1\n"
    "global_load_lds_dwordx4 v2, s[40:41] offset:2048 sc1\n"
    "global_load_lds_dwordx4 v2, s[40:41] offset:3072 sc1\n"
    "s_add_u32 s40, s40, 8192\n"
    "s_addc_u32 s41, s41, 0\n"
    "s_waitcnt vmcnt(8) lgkmcnt(0)\n"
    "s_barrier\n"
    "v_mfma_f32_16x16x32_bf16 v[32:35], a[32:35], a[0:3], 0\n"
    "s_add_u32 m0, s44, 0\n"
    "ds_read_b64_tr_b16 a[92:93], v8 offset:14464\n"
    "v_mfma_f32_16x16x32_bf16 v[36:39], a[36:39], a[0:3], 0\n"
    "global_load_lds_dwordx4 v2, s[40:41] offset:0 sc1\n"
    "v_mfma_f32_16x16x32_bf16 v[40:43], a[40:43], a[0:3], 0\n"
    "ds_read_b64_tr_b16 a[94:95], v8 offset:18048\n"
    "v_mfma_f32_16x16x32_bf16 v[44:47], a[44:47], a[0:3], 0\n"
    "v_mfma_f32_16x16x32_bf16 v[48:51], a[48:51], a[0:3], 0\n"
    "ds_read_b128 a[60:63], v7 offset:16384\n"
    "v_mfma_f32_16x16x32_bf16 v[52:55], a[52:55], a[0:3], 0\n"
    "v_mfma_f32_16x16x32_bf16 v[56:59], a[56:59], a[0:3], 0\n"
    "global_load_lds_dwordx4 v2, s[40:41] offset:1024 sc1\n"
    "ds_read_b64_tr_b16 a[96:97], v8 offset:21648\n"
    "v_mfma_f32_16x16x32_bf16 v[60:63], a[32:35], a[4:7], 0\n"
    "v_mfma_f32_16x16x32_bf16 v[64:67], a[36:39], a[4:7], 0\n"
    "ds_read_b64_tr_b16 a[98:99], v8 offset:25232\n"
    "v_mfma_f32_16x16x32_bf16 v[68:71], a[40:43], a[4:7], 0\n"
    "v_mfma_f32_16x16x32_bf16 v[72:75], a[44:47], a[4:7], 0\n"
    "ds_read_b128 a[64:67], v7 offset:17408\n"
    "v_mfma_f32_16x16x32_bf16 v[76:79], a[48:51], a[4:7], 0\n"
    "global_load_lds_dwordx4 v2, s[40:41] offset:2048 sc1\n"
    "v_mfma_f32_16x16x32_bf16 v[80:83], a[52:55], a[4:7], 0\n"
    "ds_read_b64_tr_b16 a[100:101], v12 offset:14464\n"
    "v_mfma_f32_16x16x32_bf16 v[84:87], a[56:59], a[4:7], 0\n"
    "v_mfma_f32_16x16x32_bf16 v[88:91], a[32:35], a[8:11], 0\n"
    "ds_read_b64_tr_b16 a[102:103], v12 offset:18048\n"
    "v_mfma_f32_16x16x32_bf16 v[92:95], a[36:39], a[8:11], 0\n"
    "v_mfma_f32_16x16x32_bf16 v[96:99], a[40:43], a[8:11], 0\n"
    "global_load_lds_dwordx4 v2, s[40:41] offset:3072 sc1\n"
    "s_add_u32 s40, s40, 8192\n"
    "s_addc_u32 s41, s41, 0\n"
    "ds_read_b128 a[68:71], v7 offset:18432\n"
    "v_mfma_f32_16x16x32_bf16 v[100:103], a[44:47], a[8:11], 0\n"
    "v_mfma_f32_16x16x32_bf16 v[104:107], a[48:51], a[8:11], 0\n"
    "ds_read_b64_tr_b16 a[104:105], v13 offset:14464\n"
    "v_mfma_f32_16x16x32_bf16 v[108:111], a[52:55], a[8:11], 0\n"
    "v_mfma_f32_16x16x32_bf16 v[112:115], a[56:59], a[8:11], 0\n"
    "s_add_u32 m0, s45, 0\n"
    "ds_read_b64_tr_b16 a[106:107], v13 offset:18048\n"
    "v_mfma_f32_16x16x32_bf16 v[116:119], a[32:35], a[12:15], 0\n"
    "global_load_lds_dwordx4 v3, s[42:43] offset:0\n"
    "v_mfma_f32_16x16x32_bf16 v[120:123], a[36:39], a[12:15], 0\n"
    "ds_read_b128 a[72:75], v7 offset:19456\n"
    "v_mfma_f32_16x16x32_bf16 v[124:127], a[40:43], a[12:15], 0\n"
    "v_mfma_f32_16x16x32_bf16 v[128:131], a[44:47], a[12:15], 0\n"
    "ds_read_b64_tr_b16 a[108:109], v14 offset:14464\n"
    "v_mfma_f32_16x16x32_bf16 v[132:135], a[48:51], a[12:15], 0\n"
    "v_mfma_f32_16x16x32_bf16 v[136:139], a[52:55], a[12:15], 0\n"
    "global_load_lds_dwordx4 v4, s[42:43] offset:1024\n"
    "ds_read_b64_tr_b16 a[110:111], v14 offset:18048\n"
    "v_mfma_f32_16x16x32_bf16 v[140:143], a[56:59], a[12:15], 0\n"
    "v_mfma_f32_16x16x32_bf16 v[144:147], a[32:35], a[16:19], 0\n"
    "ds_read_b128 a[76:79], v7 offset:20480\n"
    "v_mfma_f32_16x16x32_bf16 v[148:151], a[36:39], a[16:19], 0\n"
    "v_mfma_f32_16x16x32_bf16 v[152:155], a[40:43], a[16:19], 0\n"
    "ds_read_b64_tr_b16 a[112:113], v14 offset:21648\n"
    "v_mfma_f32_16x16x32_bf16 v[156:159], a[44:47], a[16:19], 0\n"
    "global_load_lds_dwordx4 v5, s[42:43] offset:2048\n"
    "v_mfma_f32_16x16x32_bf16 v[160:163], a[48:51], a[16:19], 0\n"
    "ds_read_b64_tr_b16 a[114:115], v14 offset:25232\n"
    "v_mfma_f32_16x16x32_bf16 v[164:167], a[52:55], a[16:19], 0\n"
    "v_mfma_f32_16x16x32_bf16 v[168:171], a[56:59], a[16:19], 0\n"
    "ds_read_b128 a[80:83], v7 offset:21504\n"
    "v_mfma_f32_16x16x32_bf16 v[172:175], a[32:35], a[20:23], 0\n"
    "v_mfma_f32_16x16x32_bf16 v[176:179], a[36:39], a[20:23], 0\n"
    "global_load_lds_dwordx4 v6, s[42:43] offset:3072\n"
    "s_add_u32 s42, s42, 229376\n"
    "s_addc_u32 s43, s43, 0\n"
    "ds_read_b64_tr_b16 a[116:117], v9 offset:14464\n"
    "v_mfma_f32_16x16x32_bf16 v[180:183], a[40:43], a[20:23], 0\n"
    "v_mfma_f32_16x16x32_bf16 v[184:187], a[44:47], a[20:23], 0\n"
    "ds_read_b64_tr_b16 a[118:119], v9 offset:18048\n"
    "v_mfma_f32_16x16x32_bf16 v[188:191], a[48:51], a[20:23], 0\n"
    "v_mfma_f32_16x16x32_bf16 v[192:195], a[52:55], a[20:23], 0\n"
    "ds_read_b128 a[84:87], v7 offset:22528\n"
    "v_mfma_f32_16x16x32_bf16 v[196:199], a[56:59], a[20:23], 0\n"
    "global_store_dwordx4 v17, a[124:127], s[74:75] sc1\n"
    "s_add_u32 s74, s74, 4096\n"
    "s_addc_u32 s75, s75, 0\n"
    "v_mfma_f32_16x16x32_bf16 v[200:203], a[32:35], a[24:27], 0\n"
    "ds_read_b128 a[88:91], v7 offset:23552\n"
    "v_mfma_f32_16x16x32_bf16 v[204:207], a[36:39], a[24:27], 0\n"
    "v_mfma_f32_16x16x32_bf16 v[208:211], a[40:43], a[24:27], 0\n"
    "v_mfma_f32_16x16x32_bf16 v[212:215], a[44:47], a[24:27], 0\n"
    "v_mfma_f32_16x16x32_bf16 v[216:219], a[48:51], a[24:27], 0\n"
    "v_mfma_f32_16x16x32_bf16 v[220:223], a[52:55], a[24:27], 0\n"
    "v_mfma_f32_16x16x32_bf16 v[224:227], a[56:59], a[24:27], 0\n"
    "v_mfma_f32_16x16x32_bf16 v[228:231], a[32:35], a[28:31], 0\n"
    "v_mfma_f32_16x16x32_bf16 v[232:235], a[36:39], a[28:31], 0\n"
    "v_mfma_f32_16x16x32_bf16 v[236:239], a[40:43], a[28:31], 0\n"
    "v_mfma_f32_16x16x32_bf16 v[240:243], a[44:47], a[28:31], 0\n"
    "s_waitcnt vmcnt(13) lgkmcnt(0)\n"
    "s_barrier\n"
    "v_mfma_f32_16x16x32_bf16 v[244:247], a[48:51], a[28:31], 0\n"
    "v_mfma_f32_16x16x32_bf16 v[248:251], a[52:55], a[28:31], 0\n"
    "v_mfma_f32_16x16x32_bf16 v[252:255], a[56:59], a[28:31], 0\n"
    "v_mfma_f32_16x16x32_bf16 v[32:35], a[92:95], a[60:63], v[32:35]\n"
    "s_add_u32 m0, s44, 16384\n"
    "ds_read_b64_tr_b16 a[32:33], v8 offset:28928\n"
    "v_mfma_f32_16x16x32_bf16 v[36:39], a[96:99], a[60:63], v[36:39]\n"
    "global_load_lds_dwordx4 v2, s[40:41] offset:0 sc1\n"
    "v_mfma_f32_16x16x32_bf16 v[40:43], a[100:103], a[60:63], v[40:43]\n"
    "ds_read_b64_tr_b16 a[34:35], v8 offset:32512\n"
    "v_mfma_f32_16x16x32_bf16 v[44:47], a[104:107], a[60:63], v[44:47]\n"
    "v_mfma_f32_16x16x32_bf16 v[48:51], a[108:111], a[60:63], v[48:51]\n"
    "ds_read_b128 a[0:3], v7 offset:32768\n"
    "v_mfma_f32_16x16x32_bf16 v[52:55], a[112:115], a[60:63], v[52:55]\n"
    "v_mfma_f32_16x16x32_bf16 v[56:59], a[116:119], a[60:63], v[56:59]\n"
    "global_load_lds_dwordx4 v2, s[40:41] offset:1024 sc1\n"
    "ds_read_b64_tr_b16 a[36:37], v8 offset:36112\n"
    "v_mfma_f32_16x16x32_bf16 v[60:63], a[92:95], a[64:67], v[60:63]\n"
    "v_mfma_f32_16x16x32_bf16 v[64:67], a[96:99], a[64:67], v[64:67]\n"
    "ds_read_b64_tr_b16 a[38:39], v8 offset:39696\n"
    "v_mfma_f32_16x16x32_bf16 v[68:71], a[100:103], a[64:67], v[68:71]\n"
    "v_mfma_f32_16x16x32_bf16 v[72:75], a[104:107], a[64:67], v[72:75]\n"
    "ds_read_b128 a[4:7], v7 offset:33792\n"
    "v_mfma_f32_16x16x32_bf16 v[76:79], a[108:111], a[64:67], v[76:79]\n"
    "global_load_lds_dwordx4 v2, s[40:41] offset:2048 sc1\n"
    "v_mfma_f32_16x16x32_bf16 v[80:83], a[112:115], a[64:67], v[80:83]\n"
    "ds_read_b64_tr_b16 a[40:41], v12 offset:28928\n"
    "v_mfma_f32_16x16x32_bf16 v[84:87], a[116:119], a[64:67], v[84:87]\n"
    "v_mfma_f32_16x16x32_bf16 v[88:91], a[92:95], a[68:71], v[88:91]\n"
    "ds_read_b64_tr_b16 a[42:43], v12 offset:32512\n"
    "v_mfma_f32_16x16x32_bf16 v[92:95], a[96:99], a[68:71], v[92:95]\n"
    "v_mfma_f32_16x16x32_bf16 v[96:99], a[100:103], a[68:71], v[96:99]\n"
    "global_load_lds_dwordx4 v2, s[40:41] offset:3072 sc1\n"
    "s_add_u32 s40, s40, 8192\n"
    "s_addc_u32 s41, s41, 0\n"
    "ds_read_b128 a[8:11], v7 offset:34816\n"
    "v_mfma_f32_16x16x32_bf16 v[100:103], a[104:107], a[68:71], v[100:103]\n"
    "v_mfma_f32_16x16x32_bf16 v[104:107], a[108:111], a[68:71], v[104:107]\n"
    "ds_read_b64_tr_b16 a[44:45], v13 offset:28928\n"
    "v_mfma_f32_16x16x32_bf16 v[108:111], a[112:115], a[68:71], v[108:111]\n"
    "v_mfma_f32_16x16x32_bf16 v[112:115], a[116:119], a[68:71], v[112:115]\n"
    "s_add_u32 m0, s45, 14464\n"
    "ds_read_b64_tr_b16 a[46:47], v13 offset:32512\n"
    "v_mfma_f32_16x16x32_bf16 v[116:119], a[92:95], a[72:75], v[116:119]\n"
    "global_load_lds_dwordx4 v3, s[42:43] offset:0\n"
    "v_mfma_f32_16x16x32_bf16 v[120:123], a[96:99], a[72:75], v[120:123]\n"
    "ds_read_b128 a[12:15], v7 offset:35840\n"
    "v_mfma_f32_16x16x32_bf16 v[124:127], a[100:103], a[72:75], v[124:127]\n"
    "v_mfma_f32_16x16x32_bf16 v[128:131], a[104:107], a[72:75], v[128:131]\n"
    "ds_read_b64_tr_b16 a[48:49], v14 offset:28928\n"
    "v_mfma_f32_16x16x32_bf16 v[132:135], a[108:111], a[72:75], v[132:135]\n"
    "v_mfma_f32_16x16x32_bf16 v[136:139], a[112:115], a[72:75], v[136:139]\n"
    "global_load_lds_dwordx4 v4, s[42:43] offset:1024\n"
    "ds_read_b64_tr_b16 a[50:51], v14 offset:32512\n"
    "v_mfma_f32_16x16x32_bf16 v[140:143], a[116:119], a[72:75], v[140:143]\n"
    "v_mfma_f32_16x16x32_bf16 v[144:147], a[92:95], a[76:79], v[144:147]\n"
    "ds_read_b128 a[16:19], v7 offset:36864\n"
    "v_mfma_f32_16x16x32_bf16 v[148:151], a[96:99], a[76:79], v[148:151]\n"
    "v_mfma_f32_16x16x32_bf16 v[152:155], a[100:103], a[76:79], v[152:155]\n"
    "ds_read_b64_tr_b16 a[52:53], v14 offset:36112\n"
    "v_mfma_f32_16x16x32_bf16 v[156:159], a[104:107], a[76:79], v[156:159]\n"
    "global_load_lds_dwordx4 v5, s[42:43] offset:2048\n"
    "v_mfma_f32_16x16x32_bf16 v[160:163], a[108:111], a[76:79], v[160:163]\n"
    "ds_read_b64_tr_b16 a[54:55], v14 offset:39696\n"
    "v_mfma_f32_16x16x32_bf16 v[164:167], a[112:115], a[76:79], v[164:167]\n"
    "v_mfma_f32_16x16x32_bf16 v[168:171], a[116:119], a[76:79], v[168:171]\n"
    "ds_read_b128 a[20:23], v7 offset:37888\n"
    "v_mfma_f32_16x16x32_bf16 v[172:175], a[92:95], a[80:83], v[172:175]\n"
    "v_mfma_f32_16x16x32_bf16 v[176:179], a[96:99], a[80:83], v[176:179]\n"
    "global_load_lds_dwordx4 v6, s[42:43] offset:3072\n"
    "s_add_u32 s42, s42, 229376\n"
    "s_addc_u32 s43, s43, 0\n"
    "ds_read_b64_tr_b16 a[56:57], v9 offset:28928\n"
    "v_mfma_f32_16x16x32_bf16 v[180:183], a[100:103], a[80:83], v[180:183]\n"
    "v_mfma_f32_16x16x32_bf16 v[184:187], a[104:107], a[80:83], v[184:187]\n"
    "ds_read_b64_tr_b16 a[58:59], v9 offset:32512\n"
    "v_mfma_f32_16x16x32_bf16 v[188:191], a[108:111], a[80:83], v[188:191]\n"
    "v_mfma_f32_16x16x32_bf16 v[192:195], a[112:115], a[80:83], v[192:195]\n"
    "ds_read_b128 a[24:27], v7 offset:38912\n"
    "v_mfma_f32_16x16x32_bf16 v[196:199], a[116:119], a[80:83], v[196:199]\n"
    "global_store_dwordx4 v17, a[124:127], s[74:75] sc1\n"
    "s_add_u32 s74, s74, 4096\n"
    "s_addc_u32 s75, s75, 0\n"
    "v_mfma_f32_16x16x32_bf16 v[200:203], a[92:95], a[84:87], v[200:203]\n"
    "ds_read_b128 a[28:31], v7 offset:39936\n"
    "v_mfma_f32_16x16x32_bf16 v[204:207], a[96:99], a[84:87], v[204:207]\n"
    "v_mfma_f32_16x16x32_bf16 v[208:211], a[100:103], a[84:87], v[208:211]\n"
    "v_mfma_f32_16x16x32_bf16 v[212:215], a[104:107], a[84:87], v[212:215]\n"
    "v_mfma_f32_16x16x32_bf16 v[216:219], a[108:111], a[84:87], v[216:219]\n"
    "v_mfma_f32_16x16x32_bf16 v[220:223], a[112:115], a[84:87], v[220:223]\n"
    "v_mfma_f32_16x16x32_bf16 v[224:227], a[116:119], a[84:87], v[224:227]\n"
    "v_mfma_f32_16x16x32_bf16 v[228:231], a[92:95], a[88:91], v[228:231]\n"
    "v_mfma_f32_16x16x32_bf16 v[232:235], a[96:99], a[88:91], v[232:235]\n"
    "v_mfma_f32_16x16x32_bf16 v[236:239], a[100:103], a[88:91], v[236:239]\n"
    "v_mfma_f32_16x16x32_bf16 v[240:243], a[104:107], a[88:91], v[240:243]\n"
    "s_waitcnt vmcnt(18) lgkmcnt(0)\n"
    "s_barrier\n"
    "v_mfma_f32_16x16x32_bf16 v[244:247], a[108:111], a[88:91], v[244:247]\n"
    "v_mfma_f32_16x16x32_bf16 v[248:251], a[112:115], a[88:91], v[248:251]\n"
    "v_mfma_f32_16x16x32_bf16 v[252:255], a[116:119], a[88:91], v[252:255]\n"
    "s_mov_b32 s46, 6\n"
    "1:\n"
    "v_mfma_f32_16x16x32_bf16 v[32:35], a[32:35], a[0:3], v[32:35]\n"
    "s_add_u32 m0, s44, 32768\n"
    "ds_read_b64_tr_b16 a[92:93], v8 offset:43392\n"
    "v_mfma_f32_16x16x32_bf16 v[36:39], a[36:39], a[0:3], v[36:39]\n"
    "global_load_lds_dwordx4 v2, s[40:41] offset:0 sc1\n"
    "v_mfma_f32_16x16x32_bf16 v[40:43], a[40:43], a[0:3], v[40:43]\n"
    "ds_read_b64_tr_b16 a[94:95], v8 offset:46976\n"
    "v_mfma_f32_16x16x32_bf16 v[44:47], a[44:47], a[0:3], v[44:47]\n"
    "v_mfma_f32_16x16x32_bf16 v[48:51], a[48:51], a[0:3], v[48:51]\n"
    "ds_read_b128 a[60:63], v7 offset:49152\n"
    "v_mfma_f32_16x16x32_bf16 v[52:55], a[52:55], a[0:3], v[52:55]\n"
    "v_mfma_f32_16x16x32_bf16 v[56:59], a[56:59], a[0:3], v[56:59]\n"
    "global_load_lds_dwordx4 v2, s[40:41] offset:1024 sc1\n"
    "ds_read_b64_tr_b16 a[96:97], v8 offset:50576\n"
    "v_mfma_f32_16x16x32_bf16 v[60:63], a[32:35], a[4:7], v[60:63]\n"
    "v_mfma_f32_16x16x32_bf16 v[64:67], a[36:39], a[4:7], v[64:67]\n"
    "ds_read_b64_tr_b16 a[98:99], v8 offset:54160\n"
    "v_mfma_f32_16x16x32_bf16 v[68:71], a[40:43], a[4:7], v[68:71]\n"
    "v_mfma_f32_16x16x32_bf16 v[72:75], a[44:47], a[4:7], v[72:75]\n"
    "ds_read_b128 a[64:67], v7 offset:50176\n"
    "v_mfma_f32_16x16x32_bf16 v[76:79], a[48:51], a[4:7], v[76:79]\n"
    "v_mfma_f32_16x16x32_bf16 v[80:83], a[52:55], a[4:7], v[80:83]\n"
    "global_load_lds_dwordx4 v2, s[40:41] offset:2048 sc1\n"
    "ds_read_b64_tr_b16 a[100:101], v12 offset:43392\n"
    "v_mfma_f32_16x16x32_bf16 v[84:87], a[56:59], a[4:7], v[84:87]\n"
    "v_mfma_f32_16x16x32_bf16 v[88:91], a[32:35], a[8:11], v[88:91]\n"
    "ds_read_b64_tr_b16 a[102:103], v12 offset:46976\n"
    "v_mfma_f32_16x16x32_bf16 v[92:95], a[36:39], a[8:11], v[92:95]\n"
    "v_mfma_f32_16x16x32_bf16 v[96:99], a[40:43], a[8:11], v[96:99]\n"
    "ds_read_b128 a[68:71], v7 offset:51200\n"
    "v_mfma_f32_16x16x32_bf16 v[100:103], a[44:47], a[8:11], v[100:103]\n"
    "global_load_lds_dwordx4 v2, s[40:41] offset:3072 sc1\n"
    "s_add_u32 s40, s40, 8192\n"
    "s_addc_u32 s41, s41, 0\n"
    "v_mfma_f32_16x16x32_bf16 v[104:107], a[48:51], a[8:11], v[104:107]\n"
    "ds_read_b64_tr_b16 a[104:105], v13 offset:43392\n"
    "v_mfma_f32_16x16x32_bf16 v[108:111], a[52:55], a[8:11], v[108:111]\n"
    "v_mfma_f32_16x16x32_bf16 v[112:115], a[56:59], a[8:11], v[112:115]\n"
    "ds_read_b64_tr_b16 a[106:107], v13 offset:46976\n"
    "v_mfma_f32_16x16x32_bf16 v[116:119], a[32:35], a[12:15], v[116:119]\n"
    "v_mfma_f32_16x16x32_bf16 v[120:123], a[36:39], a[12:15], v[120:123]\n"
    "s_add_u32 m0, s45, 28928\n"
    "ds_read_b128 a[72:75], v7 offset:52224\n"
    "v_mfma_f32_16x16x32_bf16 v[124:127], a[40:43], a[12:15], v[124:127]\n"
    "global_load_lds_dwordx4 v3, s[42:43] offset:0\n"
    "v_mfma_f32_16x16x32_bf16 v[128:131], a[44:47], a[12:15], v[128:131]\n"
    "ds_read_b64_tr_b16 a[108:109], v14 offset:43392\n"
    "v_mfma_f32_16x16x32_bf16 v[132:135], a[48:51], a[12:15], v[132:135]\n"
    "v_mfma_f32_16x16x32_bf16 v[136:139], a[52:55], a[12:15], v[136:139]\n"
    "ds_read_b64_tr_b16 a[110:111], v14 offset:46976\n"
    "v_mfma_f32_16x16x32_bf16 v[140:143], a[56:59], a[12:15], v[140:143]\n"
    "v_mfma_f32_16x16x32_bf16 v[144:147], a[32:35], a[16:19], v[144:147]\n"
    "ds_read_b128 a[76:79], v7 offset:53248\n"
    "v_mfma_f32_16x16x32_bf16 v[148:151], a[36:39], a[16:19], v[148:151]\n"
    "global_load_lds_dwordx4 v4, s[42:43] offset:1024\n"
    "v_mfma_f32_16x16x32_bf16 v[152:155], a[40:43], a[16:19], v[152:155]\n"
    "ds_read_b64_tr_b16 a[112:113], v14 offset:50576\n"
    "v_mfma_f32_16x16x32_bf16 v[156:159], a[44:47], a[16:19], v[156:159]\n"
    "v_mfma_f32_16x16x32_bf16 v[160:163], a[48:51], a[16:19], v[160:163]\n"
    "ds_read_b64_tr_b16 a[114:115], v14 offset:54160\n"
    "v_mfma_f32_16x16x32_bf16 v[164:167], a[52:55], a[16:19], v[164:167]\n"
    "v_mfma_f32_16x16x32_bf16 v[168:171], a[56:59], a[16:19], v[168:171]\n"
    "global_load_lds_dwordx4 v5, s[42:43] offset:2048\n"
    "ds_read_b128 a[80:83], v7 offset:54272\n"
    "v_mfma_f32_16x16x32_bf16 v[172:175], a[32:35], a[20:23], v[172:175]\n"
    "v_mfma_f32_16x16x32_bf16 v[176:179], a[36:39], a[20:23], v[176:179]\n"
    "ds_read_b64_tr_b16 a[116:117], v9 offset:43392\n"
    "v_mfma_f32_16x16x32_bf16 v[180:183], a[40:43], a[20:23], v[180:183]\n"
    "v_mfma_f32_16x16x32_bf16 v[184:187], a[44:47], a[20:23], v[184:187]\n"
    "ds_read_b64_tr_b16 a[118:119], v9 offset:46976\n"
    "v_mfma_f32_16x16x32_bf16 v[188:191], a[48:51], a[20:23], v[188:191]\n"
    "v_mfma_f32_16x16x32_bf16 v[192:195], a[52:55], a[20:23], v[192:195]\n"
    "global_load_lds_dwordx4 v6, s[42:43] offset:3072\n"
    "s_add_u32 s42, s42, 229376\n"
    "s_addc_u32 s43, s43, 0\n"
    "ds_read_b128 a[84:87], v7 offset:55296\n"
    "v_mfma_f32_16x16x32_bf16 v[196:199], a[56:59], a[20:23], v[196:199]\n"
    "v_mfma_f32_16x16x32_bf16 v[200:203], a[32:35], a[24:27], v[200:203]\n"
    "ds_read_b128 a[88:91], v7 offset:56320\n"
    "v_mfma_f32_16x16x32_bf16 v[204:207], a[36:39], a[24:27], v[204:207]\n"
    "v_mfma_f32_16x16x32_bf16 v[208:211], a[40:43], a[24:27], v[208:211]\n"
    "v_mfma_f32_16x16x32_bf16 v[212:215], a[44:47], a[24:27], v[212:215]\n"
    "v_mfma_f32_16x16x32_bf16 v[216:219], a[48:51], a[24:27], v[216:219]\n"
    "v_mfma_f32_16x16x32_bf16 v[220:223], a[52:55], a[24:27], v[220:223]\n"
    "v_mfma_f32_16x16x32_bf16 v[224:227], a[56:59], a[24:27], v[224:227]\n"
    "v_mfma_f32_16x16x32_bf16 v[228:231], a[32:35], a[28:31], v[228:231]\n"
    "v_mfma_f32_16x16x32_bf16 v[232:235], a[36:39], a[28:31], v[232:235]\n"
    "v_mfma_f32_16x16x32_bf16 v[236:239], a[40:43], a[28:31], v[236:239]\n"
    "v_mfma_f32_16x16x32_bf16 v[240:243], a[44:47], a[28:31], v[240:243]\n"
    "s_waitcnt vmcnt(16) lgkmcnt(0)\n"
    "s_barrier\n"
    "v_mfma_f32_16x16x32_bf16 v[244:247], a[48:51], a[28:31], v[244:247]\n"
    "v_mfma_f32_16x16x32_bf16 v[248:251], a[52:55], a[28:31], v[248:251]\n"
    "v_mfma_f32_16x16x32_bf16 v[252:255], a[56:59], a[28:31], v[252:255]\n"
    "v_mfma_f32_16x16x32_bf16 v[32:35], a[92:95], a[60:63], v[32:35]\n"
    "s_add_u32 m0, s44, 49152\n"
    "ds_read_b64_tr_b16 a[32:33], v8 offset:0\n"
    "v_mfma_f32_16x16x32_bf16 v[36:39], a[96:99], a[60:63], v[36:39]\n"
    "global_load_lds_dwordx4 v2, s[40:41] offset:0 sc1\n"
    "v_mfma_f32_16x16x32_bf16 v[40:43], a[100:103], a[60:63], v[40:43]\n"
    "ds_read_b64_tr_b16 a[34:35], v8 offset:3584\n"
    "v_mfma_f32_16x16x32_bf16 v[44:47], a[104:107], a[60:63], v[44:47]\n"
    "v_mfma_f32_16x16x32_bf16 v[48:51], a[108:111], a[60:63], v[48:51]\n"
    "ds_read_b128 a[0:3], v7 offset:0\n"
    "v_mfma_f32_16x16x32_bf16 v[52:55], a[112:115], a[60:63], v[52:55]\n"
    "v_mfma_f32_16x16x32_bf16 v[56:59], a[116:119], a[60:63], v[56:59]\n"
    "global_load_lds_dwordx4 v2, s[40:41] offset:1024 sc1\n"
    "ds_read_b64_tr_b16 a[36:37], v8 offset:7184\n"
    "v_mfma_f32_16x16x32_bf16 v[60:63], a[92:95], a[64:67], v[60:63]\n"
    "v_mfma_f32_16x16x32_bf16 v[64:67], a[96:99], a[64:67], v[64:67]\n"
    "ds_read_b64_tr_b16 a[38:39], v8 offset:10768\n"
    "v_mfma_f32_16x16x32_bf16 v[68:71], a[100:103], a[64:67], v[68:71]\n"
    "v_mfma_f32_16x16x32_bf16 v[72:75], a[104:107], a[64:67], v[72:75]\n"
    "ds_read_b128 a[4:7], v7 offset:1024\n"
    "v_mfma_f32_16x16x32_bf16 v[76:79], a[108:111], a[64:67], v[76:79]\n"
    "v_mfma_f32_16x16x32_bf16 v[80:83], a[112:115], a[64:67], v[80:83]\n"
    "global_load_lds_dwordx4 v2, s[40:41] offset:2048 sc1\n"
    "ds_read_b64_tr_b16 a[40:41], v12 offset:0\n"
    "v_mfma_f32_16x16x32_bf16 v[84:87], a[116:119], a[64:67], v[84:87]\n"
    "v_mfma_f32_16x16x32_bf16 v[88:91], a[92:95], a[68:71], v[88:91]\n"
    "ds_read_b64_tr_b16 a[42:43], v12 offset:3584\n"
    "v_mfma_f32_16x16x32_bf16 v[92:95], a[96:99], a[68:71], v[92:95]\n"
    "v_mfma_f32_16x16x32_bf16 v[96:99], a[100:103], a[68:71], v[96:99]\n"
    "ds_read_b128 a[8:11], v7 offset:2048\n"
    "v_mfma_f32_16x16x32_bf16 v[100:103], a[104:107], a[68:71], v[100:103]\n"
    "global_load_lds_dwordx4 v2, s[40:41] offset:3072 sc1\n"
    "s_add_u32 s40, s40, 8192\n"
    "s_addc_u32 s41, s41, 0\n"
    "v_mfma_f32_16x16x32_bf16 v[104:107], a[108:111], a[68:71], v[104:107]\n"
    "ds_read_b64_tr_b16 a[44:45], v13 offset:0\n"
    "v_mfma_f32_16x16x32_bf16 v[108:111], a[112:115], a[68:71], v[108:111]\n"
    "v_mfma_f32_16x16x32_bf16 v[112:115], a[116:119], a[68:71], v[112:115]\n"
    "ds_read_b64_tr_b16 a[46:47], v13 offset:3584\n"
    "v_mfma_f32_16x16x32_bf16 v[116:119], a[92:95], a[72:75], v[116:119]\n"
    "v_mfma_f32_16x16x32_bf16 v[120:123], a[96:99], a[72:75], v[120:123]\n"
    "s_add_u32 m0, s45, 43392\n"
    "ds_read_b128 a[12:15], v7 offset:3072\n"
    "v_mfma_f32_16x16x32_bf16 v[124:127], a[100:103], a[72:75], v[124:127]\n"
    "global_load_lds_dwordx4 v3, s[42:43] offset:0\n"
    "v_mfma_f32_16x16x32_bf16 v[128:131], a[104:107], a[72:75], v[128:131]\n"
    "ds_read_b64_tr_b16 a[48:49], v14 offset:0\n"
    "v_mfma_f32_16x16x32_bf16 v[132:135], a[108:111], a[72:75], v[132:135]\n"
    "v_mfma_f32_16x16x32_bf16 v[136:139], a[112:115], a[72:75], v[136:139]\n"
    "ds_read_b64_tr_b16 a[50:51], v14 offset:3584\n"
    "v_mfma_f32_16x16x32_bf16 v[140:143], a[116:119], a[72:75], v[140:143]\n"
    "v_mfma_f32_16x16x32_bf16 v[144:147], a[92:95], a[76:79], v[144:147]\n"
    "ds_read_b128 a[16:19], v7 offset:4096\n"
    "v_mfma_f32_16x16x32_bf16 v[148:151], a[96:99], a[76:79], v[148:151]\n"
    "global_load_lds_dwordx4 v4, s[42:43] offset:1024\n"
    "v_mfma_f32_16x16x32_bf16 v[152:155], a[100:103], a[76:79], v[152:155]\n"
    "ds_read_b64_tr_b16 a[52:53], v14 offset:7184\n"
    "v_mfma_f32_16x16x32_bf16 v[156:159], a[104:107], a[76:79], v[156:159]\n"
    "v_mfma_f32_16x16x32_bf16 v[160:163], a[108:111], a[76:79], v[160:163]\n"
    "ds_read_b64_tr_b16 a[54:55], v14 offset:10768\n"
    "v_mfma_f32_16x16x32_bf16 v[164:167], a[112:115], a[76:79], v[164:167]\n"
    "v_mfma_f32_16x16x32_bf16 v[168:171], a[116:119], a[76:79], v[168:171]\n"
    "global_load_lds_dwordx4 v5, s[42:43] offset:2048\n"
    "ds_read_b128 a[20:23], v7 offset:5120\n"
    "v_mfma_f32_16x16x32_bf16 v[172:175], a[92:95], a[80:83], v[172:175]\n"
    "v_mfma_f32_16x16x32_bf16 v[176:179], a[96:99], a[80:83], v[176:179]\n"
    "ds_read_b64_tr_b16 a[56:57], v9 offset:0\n"
    "v_mfma_f32_16x16x32_bf16 v[180:183], a[100:103], a[80:83], v[180:183]\n"
    "v_mfma_f32_16x16x32_bf16 v[184:187], a[104:107], a[80:83], v[184:187]\n"
    "ds_read_b64_tr_b16 a[58:59], v9 offset:3584\n"
    "v_mfma_f32_16x16x32_bf16 v[188:191], a[108:111], a[80:83], v[188:191]\n"
    "v_mfma_f32_16x16x32_bf16 v[192:195], a[112:115], a[80:83], v[192:195]\n"
    "global_load_lds_dwordx4 v6, s[42:43] offset:3072\n"
    "s_add_u32 s42, s42, 229376\n"
    "s_addc_u32 s43, s43, 0\n"
    "ds_read_b128 a[24:27], v7 offset:6144\n"
    "v_mfma_f32_16x16x32_bf16 v[196:199], a[116:119], a[80:83], v[196:199]\n"
    "v_mfma_f32_16x16x32_bf16 v[200:203], a[92:95], a[84:87], v[200:203]\n"
    "ds_read_b128 a[28:31], v7 offset:7168\n"
    "v_mfma_f32_16x16x32_bf16 v[204:207], a[96:99], a[84:87], v[204:207]\n"
    "v_mfma_f32_16x16x32_bf16 v[208:211], a[100:103], a[84:87], v[208:211]\n"
    "v_mfma_f32_16x16x32_bf16 v[212:215], a[104:107], a[84:87], v[212:215]\n"
    "v_mfma_f32_16x16x32_bf16 v[216:219], a[108:111], a[84:87], v[216:219]\n"
    "v_mfma_f32_16x16x32_bf16 v[220:223], a[112:115], a[84:87], v[220:223]\n"
    "v_mfma_f32_16x16x32_bf16 v[224:227], a[116:119], a[84:87], v[224:227]\n"
    "v_mfma_f32_16x16x32_bf16 v[228:231], a[92:95], a[88:91], v[228:231]\n"
    "v_mfma_f32_16x16x32_bf16 v[232:235], a[96:99], a[88:91], v[232:235]\n"
    "v_mfma_f32_16x16x32_bf16 v[236:239], a[100:103], a[88:91], v[236:239]\n"
    "v_mfma_f32_16x16x32_bf16 v[240:243], a[104:107], a[88:91], v[240:243]\n"
    "s_waitcnt vmcnt(16) lgkmcnt(0)\n"
    "s_barrier\n"
    "v_mfma_f32_16x16x32_bf16 v[244:247], a[108:111], a[88:91], v[244:247]\n"
    "v_mfma_f32_16x16x32_bf16 v[248:251], a[112:115], a[88:91], v[248:251]\n"
    "v_mfma_f32_16x16x32_bf16 v[252:255], a[116:119], a[88:91], v[252:255]\n"
    "v_mfma_f32_16x16x32_bf16 v[32:35], a[32:35], a[0:3], v[32:35]\n"
    "s_add_u32 m0, s44, 0\n"
    "ds_read_b64_tr_b16 a[92:93], v8 offset:14464\n"
    "v_mfma_f32_16x16x32_bf16 v[36:39], a[36:39], a[0:3], v[36:39]\n"
    "global_load_lds_dwordx4 v2, s[40:41] offset:0 sc1\n"
    "v_mfma_f32_16x16x32_bf16 v[40:43], a[40:43], a[0:3], v[40:43]\n"
    "ds_read_b64_tr_b16 a[94:95], v8 offset:18048\n"
    "v_mfma_f32_16x16x32_bf16 v[44:47], a[44:47], a[0:3], v[44:47]\n"
    "v_mfma_f32_16x16x32_bf16 v[48:51], a[48:51], a[0:3], v[48:51]\n"
    "ds_read_b128 a[60:63], v7 offset:16384\n"
    "v_mfma_f32_16x16x32_bf16 v[52:55], a[52:55], a[0:3], v[52:55]\n"
    "v_mfma_f32_16x16x32_bf16 v[56:59], a[56:59], a[0:3], v[56:59]\n"
    "global_load_lds_dwordx4 v2, s[40:41] offset:1024 sc1\n"
    "ds_read_b64_tr_b16 a[96:97], v8 offset:21648\n"
    "v_mfma_f32_16x16x32_bf16 v[60:63], a[32:35], a[4:7], v[60:63]\n"
    "v_mfma_f32_16x16x32_bf16 v[64:67], a[36:39], a[4:7], v[64:67]\n"
    "ds_read_b64_tr_b16 a[98:99], v8 offset:25232\n"
    "v_mfma_f32_16x16x32_bf16 v[68:71], a[40:43], a[4:7], v[68:71]\n"
    "v_mfma_f32_16x16x32_bf16 v[72:75], a[44:47], a[4:7], v[72:75]\n"
    "ds_read_b128 a[64:67], v7 offset:17408\n"
    "v_mfma_f32_16x16x32_bf16 v[76:79], a[48:51], a[4:7], v[76:79]\n"
    "v_mfma_f32_16x16x32_bf16 v[80:83], a[52:55], a[4:7], v[80:83]\n"
    "global_load_lds_dwordx4 v2, s[40:41] offset:2048 sc1\n"
    "ds_read_b64_tr_b16 a[100:101], v12 offset:14464\n"
    "v_mfma_f32_16x16x32_bf16 v[84:87], a[56:59], a[4:7], v[84:87]\n"
    "v_mfma_f32_16x16x32_bf16 v[88:91], a[32:35], a[8:11], v[88:91]\n"
    "ds_read_b64_tr_b16 a[102:103], v12 offset:18048\n"
    "v_mfma_f32_16x16x32_bf16 v[92:95], a[36:39], a[8:11], v[92:95]\n"
    "v_mfma_f32_16x16x32_bf16 v[96:99], a[40:43], a[8:11], v[96:99]\n"
    "ds_read_b128 a[68:71], v7 offset:18432\n"
    "v_mfma_f32_16x16x32_bf16 v[100:103], a[44:47], a[8:11], v[100:103]\n"
    "global_load_lds_dwordx4 v2, s[40:41] offset:3072 sc1\n"
    "s_add_u32 s40, s40, 8192\n"
    "s_addc_u32 s41, s41, 0\n"
    "v_mfma_f32_16x16x32_bf16 v[104:107], a[48:51], a[8:11], v[104:107]\n"
    "ds_read_b64_tr_b16 a[104:105], v13 offset:14464\n"
    "v_mfma_f32_16x16x32_bf16 v[108:111], a[52:55], a[8:11], v[108:111]\n"
    "v_mfma_f32_16x16x32_bf16 v[112:115], a[56:59], a[8:11], v[112:115]\n"
    "ds_read_b64_tr_b16 a[106:107], v13 offset:18048\n"
    "v_mfma_f32_16x16x32_bf16 v[116:119], a[32:35], a[12:15], v[116:119]\n"
    "v_mfma_f32_16x16x32_bf16 v[120:123], a[36:39], a[12:15], v[120:123]\n"
    "s_add_u32 m0, s45, 0\n"
    "ds_read_b128 a[72:75], v7 offset:19456\n"
    "v_mfma_f32_16x16x32_bf16 v[124:127], a[40:43], a[12:15], v[124:127]\n"
    "global_load_lds_dwordx4 v3, s[42:43] offset:0\n"
    "v_mfma_f32_16x16x32_bf16 v[128:131], a[44:47], a[12:15], v[128:131]\n"
    "ds_read_b64_tr_b16 a[108:109], v14 offset:14464\n"
    "v_mfma_f32_16x16x32_bf16 v[132:135], a[48:51], a[12:15], v[132:135]\n"
    "v_mfma_f32_16x16x32_bf16 v[136:139], a[52:55], a[12:15], v[136:139]\n"
    "ds_read_b64_tr_b16 a[110:111], v14 offset:18048\n"
    "v_mfma_f32_16x16x32_bf16 v[140:143], a[56:59], a[12:15], v[140:143]\n"
    "v_mfma_f32_16x16x32_bf16 v[144:147], a[32:35], a[16:19], v[144:147]\n"
    "ds_read_b128 a[76:79], v7 offset:20480\n"
    "v_mfma_f32_16x16x32_bf16 v[148:151], a[36:39], a[16:19], v[148:151]\n"
    "global_load_lds_dwordx4 v4, s[42:43] offset:1024\n"
    "v_mfma_f32_16x16x32_bf16 v[152:155], a[40:43], a[16:19], v[152:155]\n"
    "ds_read_b64_tr_b16 a[112:113], v14 offset:21648\n"
    "v_mfma_f32_16x16x32_bf16 v[156:159], a[44:47], a[16:19], v[156:159]\n"
    "v_mfma_f32_16x16x32_bf16 v[160:163], a[48:51], a[16:19], v[160:163]\n"
    "ds_read_b64_tr_b16 a[114:115], v14 offset:25232\n"
    "v_mfma_f32_16x16x32_bf16 v[164:167], a[52:55], a[16:19], v[164:167]\n"
    "v_mfma_f32_16x16x32_bf16 v[168:171], a[56:59], a[16:19], v[168:171]\n"
    "global_load_lds_dwordx4 v5, s[42:43] offset:2048\n"
    "ds_read_b128 a[80:83], v7 offset:21504\n"
    "v_mfma_f32_16x16x32_bf16 v[172:175], a[32:35], a[20:23], v[172:175]\n"
    "v_mfma_f32_16x16x32_bf16 v[176:179], a[36:39], a[20:23], v[176:179]\n"
    "ds_read_b64_tr_b16 a[116:117], v9 offset:14464\n"
    "v_mfma_f32_16x16x32_bf16 v[180:183], a[40:43], a[20:23], v[180:183]\n"
    "v_mfma_f32_16x16x32_bf16 v[184:187], a[44:47], a[20:23], v[184:187]\n"
    "ds_read_b64_tr_b16 a[118:119], v9 offset:18048\n"
    "v_mfma_f32_16x16x32_bf16 v[188:191], a[48:51], a[20:23], v[188:191]\n"
    "v_mfma_f32_16x16x32_bf16 v[192:195], a[52:55], a[20:23], v[192:195]\n"
    "global_load_lds_dwordx4 v6, s[42:43] offset:3072\n"
    "s_add_u32 s42, s42, 229376\n"
    "s_addc_u32 s43, s43, 0\n"
    "ds_read_b128 a[84:87], v7 offset:22528\n"
    "v_mfma_f32_16x16x32_bf16 v[196:199], a[56:59], a[20:23], v[196:199]\n"
    "v_mfma_f32_16x16x32_bf16 v[200:203], a[32:35], a[24:27], v[200:203]\n"
    "ds_read_b128 a[88:91], v7 offset:23552\n"
    "v_mfma_f32_16x16x32_bf16 v[204:207], a[36:39], a[24:27], v[204:207]\n"
    "v_mfma_f32_16x16x32_bf16 v[208:211], a[40:43], a[24:27], v[208:211]\n"
    "v_mfma_f32_16x16x32_bf16 v[212:215], a[44:47], a[24:27], v[212:215]\n"
    "v_mfma_f32_16x16x32_bf16 v[216:219], a[48:51], a[24:27], v[216:219]\n"
    "v_mfma_f32_16x16x32_bf16 v[220:223], a[52:55], a[24:27], v[220:223]\n"
    "v_mfma_f32_16x16x32_bf16 v[224:227], a[56:59], a[24:27], v[224:227]\n"
    "v_mfma_f32_16x16x32_bf16 v[228:231], a[32:35], a[28:31], v[228:231]\n"
    "v_mfma_f32_16x16x32_bf16 v[232:235], a[36:39], a[28:31], v[232:235]\n"
    "v_mfma_f32_16x16x32_bf16 v[236:239], a[40:43], a[28:31], v[236:239]\n"
    "v_mfma_f32_16x16x32_bf16 v[240:243], a[44:47], a[28:31], v[240:243]\n"
    "s_waitcnt vmcnt(16) lgkmcnt(0)\n"
    "s_barrier\n"
    "v_mfma_f32_16x16x32_bf16 v[244:247], a[48:51], a[28:31], v[244:247]\n"
    "v_mfma_f32_16x16x32_bf16 v[248:251], a[52:55], a[28:31], v[248:251]\n"
    "v_mfma_f32_16x16x32_bf16 v[252:255], a[56:59], a[28:31], v[252:255]\n"
    "v_mfma_f32_16x16x32_bf16 v[32:35], a[92:95], a[60:63], v[32:35]\n"
    "s_add_u32 m0, s44, 16384\n"
    "ds_read_b64_tr_b16 a[32:33], v8 offset:28928\n"
    "v_mfma_f32_16x16x32_bf16 v[36:39], a[96:99], a[60:63], v[36:39]\n"
    "global_load_lds_dwordx4 v2, s[40:41] offset:0 sc1\n"
    "v_mfma_f32_16x16x32_bf16 v[40:43], a[100:103], a[60:63], v[40:43]\n"
    "ds_read_b64_tr_b16 a[34:35], v8 offset:32512\n"
    "v_mfma_f32_16x16x32_bf16 v[44:47], a[104:107], a[60:63], v[44:47]\n"
    "v_mfma_f32_16x16x32_bf16 v[48:51], a[108:111], a[60:63], v[48:51]\n"
    "ds_read_b128 a[0:3], v7 offset:32768\n"
    "v_mfma_f32_16x16x32_bf16 v[52:55], a[112:115], a[60:63], v[52:55]\n"
    "v_mfma_f32_16x16x32_bf16 v[56:59], a[116:119], a[60:63], v[56:59]\n"
    "global_load_lds_dwordx4 v2, s[40:41] offset:1024 sc1\n"
    "ds_read_b64_tr_b16 a[36:37], v8 offset:36112\n"
    "v_mfma_f32_16x16x32_bf16 v[60:63], a[92:95], a[64:67], v[60:63]\n"
    "v_mfma_f32_16x16x32_bf16 v[64:67], a[96:99], a[64:67], v[64:67]\n"
    "ds_read_b64_tr_b16 a[38:39], v8 offset:39696\n"
    "v_mfma_f32_16x16x32_bf16 v[68:71], a[100:103], a[64:67], v[68:71]\n"
    "v_mfma_f32_16x16x32_bf16 v[72:75], a[104:107], a[64:67], v[72:75]\n"
    "ds_read_b128 a[4:7], v7 offset:33792\n"
    "v_mfma_f32_16x16x32_bf16 v[76:79], a[108:111], a[64:67], v[76:79]\n"
    "v_mfma_f32_16x16x32_bf16 v[80:83], a[112:115], a[64:67], v[80:83]\n"
    "global_load_lds_dwordx4 v2, s[40:41] offset:2048 sc1\n"
    "ds_read_b64_tr_b16 a[40:41], v12 offset:28928\n"
    "v_mfma_f32_16x16x32_bf16 v[84:87], a[116:119], a[64:67], v[84:87]\n"
    "v_mfma_f32_16x16x32_bf16 v[88:91], a[92:95], a[68:71], v[88:91]\n"
    "ds_read_b64_tr_b16 a[42:43], v12 offset:32512\n"
    "v_mfma_f32_16x16x32_bf16 v[92:95], a[96:99], a[68:71], v[92:95]\n"
    "v_mfma_f32_16x16x32_bf16 v[96:99], a[100:103], a[68:71], v[96:99]\n"
    "ds_read_b128 a[8:11], v7 offset:34816\n"
    "v_mfma_f32_16x16x32_bf16 v[100:103], a[104:107], a[68:71], v[100:103]\n"
    "global_load_lds_dwordx4 v2, s[40:41] offset:3072 sc1\n"
    "s_add_u32 s40, s40, 8192\n"
    "s_addc_u32 s41, s41, 0\n"
    "v_mfma_f32_16x16x32_bf16 v[104:107], a[108:111], a[68:71], v[104:107]\n"
    "ds_read_b64_tr_b16 a[44:45], v13 offset:28928\n"
    "v_mfma_f32_16x16x32_bf16 v[108:111], a[112:115], a[68:71], v[108:111]\n"
    "v_mfma_f32_16x16x32_bf16 v[112:115], a[116:119], a[68:71], v[112:115]\n"
    "ds_read_b64_tr_b16 a[46:47], v13 offset:32512\n"
    "v_mfma_f32_16x16x32_bf16 v[116:119], a[92:95], a[72:75], v[116:119]\n"
    "v_mfma_f32_16x16x32_bf16 v[120:123], a[96:99], a[72:75], v[120:123]\n"
    "s_add_u32 m0, s45, 14464\n"
    "ds_read_b128 a[12:15], v7 offset:35840\n"
    "v_mfma_f32_16x16x32_bf16 v[124:127], a[100:103], a[72:75], v[124:127]\n"
    "global_load_lds_dwordx4 v3, s[42:43] offset:0\n"
    "v_mfma_f32_16x16x32_bf16 v[128:131], a[104:107], a[72:75], v[128:131]\n"
    "ds_read_b64_tr_b16 a[48:49], v14 offset:28928\n"
    "v_mfma_f32_16x16x32_bf16 v[132:135], a[108:111], a[72:75], v[132:135]\n"
    "v_mfma_f32_16x16x32_bf16 v[136:139], a[112:115], a[72:75], v[136:139]\n"
    "ds_read_b64_tr_b16 a[50:51], v14 offset:32512\n"
    "v_mfma_f32_16x16x32_bf16 v[140:143], a[116:119], a[72:75], v[140:143]\n"
    "v_mfma_f32_16x16x32_bf16 v[144:147], a[92:95], a[76:79], v[144:147]\n"
    "ds_read_b128 a[16:19], v7 offset:36864\n"
    "v_mfma_f32_16x16x32_bf16 v[148:151], a[96:99], a[76:79], v[148:151]\n"
    "global_load_lds_dwordx4 v4, s[42:43] offset:1024\n"
    "v_mfma_f32_16x16x32_bf16 v[152:155], a[100:103], a[76:79], v[152:155]\n"
    "ds_read_b64_tr_b16 a[52:53], v14 offset:36112\n"
    "v_mfma_f32_16x16x32_bf16 v[156:159], a[104:107], a[76:79], v[156:159]\n"
    "v_mfma_f32_16x16x32_bf16 v[160:163], a[108:111], a[76:79], v[160:163]\n"
    "ds_read_b64_tr_b16 a[54:55], v14 offset:39696\n"
    "v_mfma_f32_16x16x32_bf16 v[164:167], a[112:115], a[76:79], v[164:167]\n"
    "v_mfma_f32_16x16x32_bf16 v[168:171], a[116:119], a[76:79], v[168:171]\n"
    "global_load_lds_dwordx4 v5, s[42:43] offset:2048\n"
    "ds_read_b128 a[20:23], v7 offset:37888\n"
    "v_mfma_f32_16x16x32_bf16 v[172:175], a[92:95], a[80:83], v[172:175]\n"
    "v_mfma_f32_16x16x32_bf16 v[176:179], a[96:99], a[80:83], v[176:179]\n"
    "ds_read_b64_tr_b16 a[56:57], v9 offset:28928\n"
    "v_mfma_f32_16x16x32_bf16 v[180:183], a[100:103], a[80:83], v[180:183]\n"
    "v_mfma_f32_16x16x32_bf16 v[184:187], a[104:107], a[80:83], v[184:187]\n"
    "ds_read_b64_tr_b16 a[58:59], v9 offset:32512\n"
    "v_mfma_f32_16x16x32_bf16 v[188:191], a[108:111], a[80:83], v[188:191]\n"
    "v_mfma_f32_16x16x32_bf16 v[192:195], a[112:115], a[80:83], v[192:195]\n"
    "global_load_lds_dwordx4 v6, s[42:43] offset:3072\n"
    "s_add_u32 s42, s42, 229376\n"
    "s_addc_u32 s43, s43, 0\n"
    "ds_read_b128 a[24:27], v7 offset:38912\n"
    "v_mfma_f32_16x16x32_bf16 v[196:199], a[116:119], a[80:83], v[196:199]\n"
    "v_mfma_f32_16x16x32_bf16 v[200:203], a[92:95], a[84:87], v[200:203]\n"
    "ds_read_b128 a[28:31], v7 offset:39936\n"
    "v_mfma_f32_16x16x32_bf16 v[204:207], a[96:99], a[84:87], v[204:207]\n"
    "v_mfma_f32_16x16x32_bf16 v[208:211], a[100:103], a[84:87], v[208:211]\n"
    "v_mfma_f32_16x16x32_bf16 v[212:215], a[104:107], a[84:87], v[212:215]\n"
    "v_mfma_f32_16x16x32_bf16 v[216:219], a[108:111], a[84:87], v[216:219]\n"
    "v_mfma_f32_16x16x32_bf16 v[220:223], a[112:115], a[84:87], v[220:223]\n"
    "v_mfma_f32_16x16x32_bf16 v[224:227], a[116:119], a[84:87], v[224:227]\n"
    "v_mfma_f32_16x16x32_bf16 v[228:231], a[92:95], a[88:91], v[228:231]\n"
    "v_mfma_f32_16x16x32_bf16 v[232:235], a[96:99], a[88:91], v[232:235]\n"
    "v_mfma_f32_16x16x32_bf16 v[236:239], a[100:103], a[88:91], v[236:239]\n"
    "v_mfma_f32_16x16x32_bf16 v[240:243], a[104:107], a[88:91], v[240:243]\n"
    "s_waitcnt vmcnt(16) lgkmcnt(0)\n"
    "s_barrier\n"
    "v_mfma_f32_16x16x32_bf16 v[244:247], a[108:111], a[88:91], v[244:247]\n"
    "v_mfma_f32_16x16x32_bf16 v[248:251], a[112:115], a[88:91], v[248:251]\n"
    "v_mfma_f32_16x16x32_bf16 v[252:255], a[116:119], a[88:91], v[252:255]\n"
    "s_sub_u32 s46, s46, 1\n"
    "s_cmp_lg_u32 s46, 0\n"
    "s_cbranch_scc1 1b\n"
    "v_mfma_f32_16x16x32_bf16 v[32:35], a[32:35], a[0:3], v[32:35]\n"
    "ds_read_b64_tr_b16 a[92:93], v8 offset:43392\n"
    "v_mfma_f32_16x16x32_bf16 v[36:39], a[36:39], a[0:3], v[36:39]\n"
    "global_store_dwordx4 v17, a[124:127], s[74:75] sc1\n"
    "s_add_u32 s74, s74, 4096\n"
    "s_addc_u32 s75, s75, 0\n"
    "v_mfma_f32_16x16x32_bf16 v[40:43], a[40:43], a[0:3], v[40:43]\n"
    "s_add_u32 m0, s44, 32768\n"
    "ds_read_b64_tr_b16 a[94:95], v8 offset:46976\n"
    "v_mfma_f32_16x16x32_bf16 v[44:47], a[44:47], a[0:3], v[44:47]\n"
    "global_load_lds_dwordx4 v2, s[40:41] offset:0 sc1\n"
    "v_mfma_f32_16x16x32_bf16 v[48:51], a[48:51], a[0:3], v[48:51]\n"
    "ds_read_b128 a[60:63], v7 offset:49152\n"
    "v_mfma_f32_16x16x32_bf16 v[52:55], a[52:55], a[0:3], v[52:55]\n"
    "v_mfma_f32_16x16x32_bf16 v[56:59], a[56:59], a[0:3], v[56:59]\n"
    "global_load_lds_dwordx4 v2, s[40:41] offset:1024 sc1\n"
    "ds_read_b64_tr_b16 a[96:97], v8 offset:50576\n"
    "v_mfma_f32_16x16x32_bf16 v[60:63], a[32:35], a[4:7], v[60:63]\n"
    "v_mfma_f32_16x16x32_bf16 v[64:67], a[36:39], a[4:7], v[64:67]\n"
    "global_load_lds_dwordx4 v2, s[40:41] offset:2048 sc1\n"
    "ds_read_b64_tr_b16 a[98:99], v8 offset:54160\n"
    "v_mfma_f32_16x16x32_bf16 v[68:71], a[40:43], a[4:7], v[68:71]\n"
    "v_mfma_f32_16x16x32_bf16 v[72:75], a[44:47], a[4:7], v[72:75]\n"
    "ds_read_b128 a[64:67], v7 offset:50176\n"
    "v_mfma_f32_16x16x32_bf16 v[76:79], a[48:51], a[4:7], v[76:79]\n"
    "global_load_lds_dwordx4 v2, s[40:41] offset:3072 sc1\n"
    "s_add_u32 s40, s40, 8192\n"
    "s_addc_u32 s41, s41, 0\n"
    "v_mfma_f32_16x16x32_bf16 v[80:83], a[52:55], a[4:7], v[80:83]\n"
    "s_add_u32 m0, s45, 28928\n"
    "ds_read_b64_tr_b16 a[100:101], v12 offset:43392\n"
    "v_mfma_f32_16x16x32_bf16 v[84:87], a[56:59], a[4:7], v[84:87]\n"
    "global_load_lds_dwordx4 v3, s[42:43] offset:0\n"
    "v_mfma_f32_16x16x32_bf16 v[88:91], a[32:35], a[8:11], v[88:91]\n"
    "ds_read_b64_tr_b16 a[102:103], v12 offset:46976\n"
    "v_mfma_f32_16x16x32_bf16 v[92:95], a[36:39], a[8:11], v[92:95]\n"
    "v_mfma_f32_16x16x32_bf16 v[96:99], a[40:43], a[8:11], v[96:99]\n"
    "global_load_lds_dwordx4 v4, s[42:43] offset:1024\n"
    "ds_read_b128 a[68:71], v7 offset:51200\n"
    "v_mfma_f32_16x16x32_bf16 v[100:103], a[44:47], a[8:11], v[100:103]\n"
    "v_mfma_f32_16x16x32_bf16 v[104:107], a[48:51], a[8:11], v[104:107]\n"
    "global_load_lds_dwordx4 v5, s[42:43] offset:2048\n"
    "ds_read_b64_tr_b16 a[104:105], v13 offset:43392\n"
    "v_mfma_f32_16x16x32_bf16 v[108:111], a[52:55], a[8:11], v[108:111]\n"
    "v_mfma_f32_16x16x32_bf16 v[112:115], a[56:59], a[8:11], v[112:115]\n"
    "ds_read_b64_tr_b16 a[106:107], v13 offset:46976\n"
    "v_mfma_f32_16x16x32_bf16 v[116:119], a[32:35], a[12:15], v[116:119]\n"
    "global_load_lds_dwordx4 v6, s[42:43] offset:3072\n"
    "s_add_u32 s42, s42, 229376\n"
    "s_addc_u32 s43, s43, 0\n"
    "v_mfma_f32_16x16x32_bf16 v[120:123], a[36:39], a[12:15], v[120:123]\n"
    "ds_read_b128 a[72:75], v7 offset:52224\n"
    "v_mfma_f32_16x16x32_bf16 v[124:127], a[40:43], a[12:15], v[124:127]\n"
    "global_load_dwordx2 a[128:129], v10, s[48:49] sc1\n"
    "s_add_u32 s48, s48, 16384\n"
    "s_addc_u32 s49, s49, 0\n"
    "v_mfma_f32_16x16x32_bf16 v[128:131], a[44:47], a[12:15], v[128:131]\n"
    "ds_read_b64_tr_b16 a[108:109], v14 offset:43392\n"
    "v_mfma_f32_16x16x32_bf16 v[132:135], a[48:51], a[12:15], v[132:135]\n"
    "v_mfma_f32_16x16x32_bf16 v[136:139], a[52:55], a[12:15], v[136:139]\n"
    "global_load_dwordx2 a[130:131], v10, s[48:49] sc1\n"
    "s_add_u32 s48, s48, 16384\n"
    "s_addc_u32 s49, s49, 0\n"
    "ds_read_b64_tr_b16 a[110:111], v14 offset:46976\n"
    "v_mfma_f32_16x16x32_bf16 v[140:143], a[56:59], a[12:15], v[140:143]\n"
    "v_mfma_f32_16x16x32_bf16 v[144:147], a[32:35], a[16:19], v[144:147]\n"
    "global_load_dwordx2 a[132:133], v10, s[48:49] sc1\n"
    "s_add_u32 s48, s48, 16384\n"
    "s_addc_u32 s49, s49, 0\n"
    "ds_read_b128 a[76:79], v7 offset:53248\n"
    "v_mfma_f32_16x16x32_bf16 v[148:151], a[36:39], a[16:19], v[148:151]\n"
    "v_mfma_f32_16x16x32_bf16 v[152:155], a[40:43], a[16:19], v[152:155]\n"
    "ds_read_b64_tr_b16 a[112:113], v14 offset:50576\n"
    "v_mfma_f32_16x16x32_bf16 v[156:159], a[44:47], a[16:19], v[156:159]\n"
    "global_load_dwordx2 a[134:135], v10, s[48:49] sc1\n"
    "s_add_u32 s48, s48, 16384\n"
    "s_addc_u32 s49, s49, 0\n"
    "v_mfma_f32_16x16x32_bf16 v[160:163], a[48:51], a[16:19], v[160:163]\n"
    "ds_read_b64_tr_b16 a[114:115], v14 offset:54160\n"
    "v_mfma_f32_16x16x32_bf16 v[164:167], a[52:55], a[16:19], v[164:167]\n"
    "global_load_dwordx2 a[136:137], v10, s[48:49] sc1\n"
    "s_add_u32 s48, s48, 16384\n"
    "s_addc_u32 s49, s49, 0\n"
    "v_mfma_f32_16x16x32_bf16 v[168:171], a[56:59], a[16:19], v[168:171]\n"
    "ds_read_b128 a[80:83], v7 offset:54272\n"
    "v_mfma_f32_16x16x32_bf16 v[172:175], a[32:35], a[20:23], v[172:175]\n"
    "v_mfma_f32_16x16x32_bf16 v[176:179], a[36:39], a[20:23], v[176:179]\n"
    "global_load_dwordx2 a[138:139], v10, s[48:49] sc1\n"
    "s_add_u32 s48, s48, 16384\n"
    "s_addc_u32 s49, s49, 0\n"
    "ds_read_b64_tr_b16 a[116:117], v9 offset:43392\n"
    "v_mfma_f32_16x16x32_bf16 v[180:183], a[40:43], a[20:23], v[180:183]\n"
    "v_mfma_f32_16x16x32_bf16 v[184:187], a[44:47], a[20:23], v[184:187]\n"
    "global_load_dwordx2 a[140:141], v10, s[48:49] sc1\n"
    "s_add_u32 s48, s48, 16384\n"
    "s_addc_u32 s49, s49, 0\n"
    "ds_read_b64_tr_b16 a[118:119], v9 offset:46976\n"
    "v_mfma_f32_16x16x32_bf16 v[188:191], a[48:51], a[20:23], v[188:191]\n"
    "v_mfma_f32_16x16x32_bf16 v[192:195], a[52:55], a[20:23], v[192:195]\n"
    "ds_read_b128 a[84:87], v7 offset:55296\n"
    "v_mfma_f32_16x16x32_bf16 v[196:199], a[56:59], a[20:23], v[196:199]\n"
    "global_load_dwordx2 a[142:143], v10, s[48:49] sc1\n"
    "s_add_u32 s48, s48, 16384\n"
    "s_addc_u32 s49, s49, 0\n"
    "v_mfma_f32_16x16x32_bf16 v[200:203], a[32:35], a[24:27], v[200:203]\n"
    "ds_read_b128 a[88:91], v7 offset:56320\n"
    "v_mfma_f32_16x16x32_bf16 v[204:207], a[36:39], a[24:27], v[204:207]\n"
    "global_load_dwordx2 a[144:145], v10, s[48:49] sc1\n"
    "s_add_u32 s48, s48, 16384\n"
    "s_addc_u32 s49, s49, 0\n"
    "v_mfma_f32_16x16x32_bf16 v[208:211], a[40:43], a[24:27], v[208:211]\n"
    "v_mfma_f32_16x16x32_bf16 v[212:215], a[44:47], a[24:27], v[212:215]\n"
    "v_mfma_f32_16x16x32_bf16 v[216:219], a[48:51], a[24:27], v[216:219]\n"
    "v_mfma_f32_16x16x32_bf16 v[220:223], a[52:55], a[24:27], v[220:223]\n"
    "v_mfma_f32_16x16x32_bf16 v[224:227], a[56:59], a[24:27], v[224:227]\n"
    "v_mfma_f32_16x16x32_bf16 v[228:231], a[32:35], a[28:31], v[228:231]\n"
    "v_mfma_f32_16x16x32_bf16 v[232:235], a[36:39], a[28:31], v[232:235]\n"
    "v_mfma_f32_16x16x32_bf16 v[236:239], a[40:43], a[28:31], v[236:239]\n"
    "v_mfma_f32_16x16x32_bf16 v[240:243], a[44:47], a[28:31], v[240:243]\n"
    "s_waitcnt vmcnt(26) lgkmcnt(0)\n"
    "s_barrier\n"
    "v_mfma_f32_16x16x32_bf16 v[244:247], a[48:51], a[28:31], v[244:247]\n"
    "v_mfma_f32_16x16x32_bf16 v[248:251], a[52:55], a[28:31], v[248:251]\n"
    "v_mfma_f32_16x16x32_bf16 v[252:255], a[56:59], a[28:31], v[252:255]\n"
    "v_mfma_f32_16x16x32_bf16 v[32:35], a[92:95], a[60:63], v[32:35]\n"
    "ds_read_b64_tr_b16 a[32:33], v8 offset:0\n"
    "v_mfma_f32_16x16x32_bf16 v[36:39], a[96:99], a[60:63], v[36:39]\n"
    "global_store_dwordx4 v17, a[124:127], s[74:75] sc1\n"
    "s_add_u32 s74, s74, 4096\n"
    "s_addc_u32 s75, s75, 0\n"
    "v_mfma_f32_16x16x32_bf16 v[40:43], a[100:103], a[60:63], v[40:43]\n"
    "s_add_u32 m0, s44, 49152\n"
    "ds_read_b64_tr_b16 a[34:35], v8 offset:3584\n"
    "v_mfma_f32_16x16x32_bf16 v[44:47], a[104:107], a[60:63], v[44:47]\n"
    "global_load_lds_dwordx4 v2, s[40:41] offset:0 sc1\n"
    "v_mfma_f32_16x16x32_bf16 v[48:51], a[108:111], a[60:63], v[48:51]\n"
    "ds_read_b128 a[0:3], v7 offset:0\n"
    "v_mfma_f32_16x16x32_bf16 v[52:55], a[112:115], a[60:63], v[52:55]\n"
    "v_mfma_f32_16x16x32_bf16 v[56:59], a[116:119], a[60:63], v[56:59]\n"
    "global_load_lds_dwordx4 v2, s[40:41] offset:1024 sc1\n"
    "ds_read_b64_tr_b16 a[36:37], v8 offset:7184\n"
    "v_mfma_f32_16x16x32_bf16 v[60:63], a[92:95], a[64:67], v[60:63]\n"
    "v_mfma_f32_16x16x32_bf16 v[64:67], a[96:99], a[64:67], v[64:67]\n"
    "global_load_lds_dwordx4 v2, s[40:41] offset:2048 sc1\n"
    "ds_read_b64_tr_b16 a[38:39], v8 offset:10768\n"
    "v_mfma_f32_16x16x32_bf16 v[68:71], a[100:103], a[64:67], v[68:71]\n"
    "v_mfma_f32_16x16x32_bf16 v[72:75], a[104:107], a[64:67], v[72:75]\n"
    "ds_read_b128 a[4:7], v7 offset:1024\n"
    "v_mfma_f32_16x16x32_bf16 v[76:79], a[108:111], a[64:67], v[76:79]\n"
    "global_load_lds_dwordx4 v2, s[40:41] offset:3072 sc1\n"
    "s_add_u32 s40, s40, 8192\n"
    "s_addc_u32 s41, s41, 0\n"
    "v_mfma_f32_16x16x32_bf16 v[80:83], a[112:115], a[64:67], v[80:83]\n"
    "ds_read_b64_tr_b16 a[40:41], v12 offset:0\n"
    "v_mfma_f32_16x16x32_bf16 v[84:87], a[116:119], a[64:67], v[84:87]\n"
    "s_add_u32 m0, s45, 43392\n"
    "v_mfma_f32_16x16x32_bf16 v[88:91], a[92:95], a[68:71], v[88:91]\n"
    "global_load_lds_dwordx4 v3, s[42:43] offset:0\n"
    "ds_read_b64_tr_b16 a[42:43], v12 offset:3584\n"
    "v_mfma_f32_16x16x32_bf16 v[92:95], a[96:99], a[68:71], v[92:95]\n"
    "v_mfma_f32_16x16x32_bf16 v[96:99], a[100:103], a[68:71], v[96:99]\n"
    "global_load_lds_dwordx4 v4, s[42:43] offset:1024\n"
    "ds_read_b128 a[8:11], v7 offset:2048\n"
    "v_mfma_f32_16x16x32_bf16 v[100:103], a[104:107], a[68:71], v[100:103]\n"
    "v_mfma_f32_16x16x32_bf16 v[104:107], a[108:111], a[68:71], v[104:107]\n"
    "ds_read_b64_tr_b16 a[44:45], v13 offset:0\n"
    "v_mfma_f32_16x16x32_bf16 v[108:111], a[112:115], a[68:71], v[108:111]\n"
    "global_load_lds_dwordx4 v5, s[42:43] offset:2048\n"
    "v_mfma_f32_16x16x32_bf16 v[112:115], a[116:119], a[68:71], v[112:115]\n"
    "ds_read_b64_tr_b16 a[46:47], v13 offset:3584\n"
    "v_mfma_f32_16x16x32_bf16 v[116:119], a[92:95], a[72:75], v[116:119]\n"
    "v_mfma_f32_16x16x32_bf16 v[120:123], a[96:99], a[72:75], v[120:123]\n"
    "global_load_lds_dwordx4 v6, s[42:43] offset:3072\n"
    "s_add_u32 s42, s42, 229376\n"
    "s_addc_u32 s43, s43, 0\n"
    "ds_read_b128 a[12:15], v7 offset:3072\n"
    "v_mfma_f32_16x16x32_bf16 v[124:127], a[100:103], a[72:75], v[124:127]\n"
    "v_mfma_f32_16x16x32_bf16 v[128:131], a[104:107], a[72:75], v[128:131]\n"
    "global_load_dwordx2 a[146:147], v10, s[48:49] sc1\n"
    "s_add_u32 s48, s48, 16384\n"
    "s_addc_u32 s49, s49, 0\n"
    "ds_read_b64_tr_b16 a[48:49], v14 offset:0\n"
    "v_mfma_f32_16x16x32_bf16 v[132:135], a[108:111], a[72:75], v[132:135]\n"
    "v_mfma_f32_16x16x32_bf16 v[136:139], a[112:115], a[72:75], v[136:139]\n"
    "ds_read_b64_tr_b16 a[50:51], v14 offset:3584\n"
    "v_mfma_f32_16x16x32_bf16 v[140:143], a[116:119], a[72:75], v[140:143]\n"
    "global_load_dwordx2 a[148:149], v10, s[48:49] sc1\n"
    "s_add_u32 s48, s48, 16384\n"
    "s_addc_u32 s49, s49, 0\n"
    "v_mfma_f32_16x16x32_bf16 v[144:147], a[92:95], a[76:79], v[144:147]\n"
    "ds_read_b128 a[16:19], v7 offset:4096\n"
    "v_mfma_f32_16x16x32_bf16 v[148:151], a[96:99], a[76:79], v[148:151]\n"
    "v_mfma_f32_16x16x32_bf16 v[152:155], a[100:103], a[76:79], v[152:155]\n"
    "global_load_dwordx2 a[150:151], v10, s[48:49] sc1\n"
    "s_add_u32 s48, s48, 16384\n"
    "s_addc_u32 s49, s49, 0\n"
    "ds_read_b64_tr_b16 a[52:53], v14 offset:7184\n"
    "v_mfma_f32_16x16x32_bf16 v[156:159], a[104:107], a[76:79], v[156:159]\n"
    "v_mfma_f32_16x16x32_bf16 v[160:163], a[108:111], a[76:79], v[160:163]\n"
    "global_load_dwordx2 a[152:153], v10, s[48:49] sc1\n"
    "s_add_u32 s48, s48, 16384\n"
    "s_addc_u32 s49, s49, 0\n"
    "ds_read_b64_tr_b16 a[54:55], v14 offset:10768\n"
    "v_mfma_f32_16x16x32_bf16 v[164:167], a[112:115], a[76:79], v[164:167]\n"
    "v_mfma_f32_16x16x32_bf16 v[168:171], a[116:119], a[76:79], v[168:171]\n"
    "ds_read_b128 a[20:23], v7 offset:5120\n"
    "v_mfma_f32_16x16x32_bf16 v[172:175], a[92:95], a[80:83], v[172:175]\n"
    "global_load_dwordx2 a[154:155], v10, s[48:49] sc1\n"
    "s_add_u32 s48, s48, 16384\n"
    "s_addc_u32 s49, s49, 0\n"
    "v_mfma_f32_16x16x32_bf16 v[176:179], a[96:99], a[80:83], v[176:179]\n"
    "ds_read_b64_tr_b16 a[56:57], v9 offset:0\n"
    "v_mfma_f32_16x16x32_bf16 v[180:183], a[100:103], a[80:83], v[180:183]\n"
    "v_mfma_f32_16x16x32_bf16 v[184:187], a[104:107], a[80:83], v[184:187]\n"
    "global_load_dwordx2 a[156:157], v10, s[48:49] sc1\n"
    "s_add_u32 s48, s48, 16384\n"
    "s_addc_u32 s49, s49, 0\n"
    "ds_read_b64_tr_b16 a[58:59], v9 offset:3584\n"
    "v_mfma_f32_16x16x32_bf16 v[188:191], a[108:111], a[80:83], v[188:191]\n"
    "v_mfma_f32_16x16x32_bf16 v[192:195], a[112:115], a[80:83], v[192:195]\n"
    "global_load_dwordx2 a[158:159], v10, s[48:49] sc1\n"
    "ds_read_b128 a[24:27], v7 offset:6144\n"
    "v_mfma_f32_16x16x32_bf16 v[196:199], a[116:119], a[80:83], v[196:199]\n"
    "v_mfma_f32_16x16x32_bf16 v[200:203], a[92:95], a[84:87], v[200:203]\n"
    "ds_read_b128 a[28:31], v7 offset:7168\n"
    "v_mfma_f32_16x16x32_bf16 v[204:207], a[96:99], a[84:87], v[204:207]\n"
    "global_load_dword a160, v11, s[50:51] sc1\n"
    "v_mfma_f32_16x16x32_bf16 v[208:211], a[100:103], a[84:87], v[208:211]\n"
    "v_mfma_f32_16x16x32_bf16 v[212:215], a[104:107], a[84:87], v[212:215]\n"
    "v_mfma_f32_16x16x32_bf16 v[216:219], a[108:111], a[84:87], v[216:219]\n"
    "v_mfma_f32_16x16x32_bf16 v[220:223], a[112:115], a[84:87], v[220:223]\n"
    "v_mfma_f32_16x16x32_bf16 v[224:227], a[116:119], a[84:87], v[224:227]\n"
    "v_mfma_f32_16x16x32_bf16 v[228:231], a[92:95], a[88:91], v[228:231]\n"
    "v_mfma_f32_16x16x32_bf16 v[232:235], a[96:99], a[88:91], v[232:235]\n"
    "v_mfma_f32_16x16x32_bf16 v[236:239], a[100:103], a[88:91], v[236:239]\n"
    "v_mfma_f32_16x16x32_bf16 v[240:243], a[104:107], a[88:91], v[240:243]\n"
    "s_waitcnt vmcnt(35) lgkmcnt(0)\n"
    "s_barrier\n"
    "v_mfma_f32_16x16x32_bf16 v[244:247], a[108:111], a[88:91], v[244:247]\n"
    "v_mfma_f32_16x16x32_bf16 v[248:251], a[112:115], a[88:91], v[248:251]\n"
    "v_mfma_f32_16x16x32_bf16 v[252:255], a[116:119], a[88:91], v[252:255]\n"
    "v_mfma_f32_16x16x32_bf16 v[32:35], a[32:35], a[0:3], v[32:35]\n"
    "s_getpc_b64 s[70:71]\n"
    "2:\n"
    "v_min_u32 v18, lin_end-2b-4, v15\n"
    "s_mov_b32 m0, s47\n"
    "ds_read_b64_tr_b16 a[92:93], v8 offset:14464\n"
    "v_mfma_f32_16x16x32_bf16 v[36:39], a[36:39], a[0:3], v[36:39]\n"
    "global_load_lds_dword v18, s[70:71]\n"
    "v_mfma_f32_16x16x32_bf16 v[40:43], a[40:43], a[0:3], v[40:43]\n"
    "ds_read_b64_tr_b16 a[94:95], v8 offset:18048\n"
    "v_mfma_f32_16x16x32_bf16 v[44:47], a[44:47], a[0:3], v[44:47]\n"
    "v_mfma_f32_16x16x32_bf16 v[48:51], a[48:51], a[0:3], v[48:51]\n"
    "ds_read_b128 a[60:63], v7 offset:16384\n"
    "v_mfma_f32_16x16x32_bf16 v[52:55], a[52:55], a[0:3], v[52:55]\n"
    "v_mfma_f32_16x16x32_bf16 v[56:59], a[56:59], a[0:3], v[56:59]\n"
    "ds_read_b64_tr_b16 a[96:97], v8 offset:21648\n"
    "v_mfma_f32_16x16x32_bf16 v[60:63], a[32:35], a[4:7], v[60:63]\n"
    "v_mfma_f32_16x16x32_bf16 v[64:67], a[36:39], a[4:7], v[64:67]\n"
    "ds_read_b64_tr_b16 a[98:99], v8 offset:25232\n"
    "v_mfma_f32_16x16x32_bf16 v[68:71], a[40:43], a[4:7], v[68:71]\n"
    "v_mfma_f32_16x16x32_bf16 v[72:75], a[44:47], a[4:7], v[72:75]\n"
    "ds_read_b128 a[64:67], v7 offset:17408\n"
    "v_mfma_f32_16x16x32_bf16 v[76:79], a[48:51], a[4:7], v[76:79]\n"
    "v_mfma_f32_16x16x32_bf16 v[80:83], a[52:55], a[4:7], v[80:83]\n"
    "ds_read_b64_tr_b16 a[100:101], v12 offset:14464\n"
    "v_mfma_f32_16x16x32_bf16 v[84:87], a[56:59], a[4:7], v[84:87]\n"
    "v_mfma_f32_16x16x32_bf16 v[88:91], a[32:35], a[8:11], v[88:91]\n"
    "ds_read_b64_tr_b16 a[102:103], v12 offset:18048\n"
    "v_mfma_f32_16x16x32_bf16 v[92:95], a[36:39], a[8:11], v[92:95]\n"
    "v_mfma_f32_16x16x32_bf16 v[96:99], a[40:43], a[8:11], v[96:99]\n"
    "ds_read_b128 a[68:71], v7 offset:18432\n"
    "v_mfma_f32_16x16x32_bf16 v[100:103], a[44:47], a[8:11], v[100:103]\n"
    "v_mfma_f32_16x16x32_bf16 v[104:107], a[48:51], a[8:11], v[104:107]\n"
    "ds_read_b64_tr_b16 a[104:105], v13 offset:14464\n"
    "v_mfma_f32_16x16x32_bf16 v[108:111], a[52:55], a[8:11], v[108:111]\n"
    "v_mfma_f32_16x16x32_bf16 v[112:115], a[56:59], a[8:11], v[112:115]\n"
    "ds_read_b64_tr_b16 a[106:107], v13 offset:18048\n"
    "v_mfma_f32_16x16x32_bf16 v[116:119], a[32:35], a[12:15], v[116:119]\n"
    "v_mfma_f32_16x16x32_bf16 v[120:123], a[36:39], a[12:15], v[120:123]\n"
    "ds_read_b128 a[72:75], v7 offset:19456\n"
    "v_mfma_f32_16x16x32_bf16 v[124:127], a[40:43], a[12:15], v[124:127]\n"
    "global_load_lds_dword v16, s[72:73]\n"
    "v_mfma_f32_16x16x32_bf16 v[128:131], a[44:47], a[12:15], v[128:131]\n"
    "ds_read_b64_tr_b16 a[108:109], v14 offset:14464\n"
    "v_mfma_f32_16x16x32_bf16 v[132:135], a[48:51], a[12:15], v[132:135]\n"
    "v_mfma_f32_16x16x32_bf16 v[136:139], a[52:55], a[12:15], v[136:139]\n"
    "ds_read_b64_tr_b16 a[110:111], v14 offset:18048\n"
    "v_mfma_f32_16x16x32_bf16 v[140:143], a[56:59], a[12:15], v[140:143]\n"
    "v_mfma_f32_16x16x32_bf16 v[144:147], a[32:35], a[16:19], v[144:147]\n"
    "ds_read_b128 a[76:79], v7 offset:20480\n"
    "v_mfma_f32_16x16x32_bf16 v[148:151], a[36:39], a[16:19], v[148:151]\n"
    "v_mfma_f32_16x16x32_bf16 v[152:155], a[40:43], a[16:19], v[152:155]\n"
    "ds_read_b64_tr_b16 a[112:113], v14 offset:21648\n"
    "v_mfma_f32_16x16x32_bf16 v[156:159], a[44:47], a[16:19], v[156:159]\n"
    "v_mfma_f32_16x16x32_bf16 v[160:163], a[48:51], a[16:19], v[160:163]\n"
    "ds_read_b64_tr_b16 a[114:115], v14 offset:25232\n"
    "v_mfma_f32_16x16x32_bf16 v[164:167], a[52:55], a[16:19], v[164:167]\n"
    "v_mfma_f32_16x16x32_bf16 v[168:171], a[56:59], a[16:19], v[168:171]\n"
    "ds_read_b128 a[80:83], v7 offset:21504\n"
    "v_mfma_f32_16x16x32_bf16 v[172:175], a[32:35], a[20:23], v[172:175]\n"
    "v_mfma_f32_16x16x32_bf16 v[176:179], a[36:39], a[20:23], v[176:179]\n"
    "ds_read_b64_tr_b16 a[116:117], v9 offset:14464\n"
    "v_mfma_f32_16x16x32_bf16 v[180:183], a[40:43], a[20:23], v[180:183]\n"
    "v_mfma_f32_16x16x32_bf16 v[184:187], a[44:47], a[20:23], v[184:187]\n"
    "ds_read_b64_tr_b16 a[118:119], v9 offset:18048\n"
    "v_mfma_f32_16x16x32_bf16 v[188:191], a[48:51], a[20:23], v[188:191]\n"
    "v_mfma_f32_16x16x32_bf16 v[192:195], a[52:55], a[20:23], v[192:195]\n"
    "ds_read_b128 a[84:87], v7 offset:22528\n"
    "v_mfma_f32_16x16x32_bf16 v[196:199], a[56:59], a[20:23], v[196:199]\n"
    "v_mfma_f32_16x16x32_bf16 v[200:203], a[32:35], a[24:27], v[200:203]\n"
    "ds_read_b128 a[88:91], v7 offset:23552\n"
    "v_mfma_f32_16x16x32_bf16 v[204:207], a[36:39], a[24:27], v[204:207]\n"
    "v_mfma_f32_16x16x32_bf16 v[208:211], a[40:43], a[24:27], v[208:211]\n"
    "v_mfma_f32_16x16x32_bf16 v[212:215], a[44:47], a[24:27], v[212:215]\n"
    "v_mfma_f32_16x16x32_bf16 v[216:219], a[48:51], a[24:27], v[216:219]\n"
    "v_mfma_f32_16x16x32_bf16 v[220:223], a[52:55], a[24:27], v[220:223]\n"
    "v_mfma_f32_16x16x32_bf16 v[224:227], a[56:59], a[24:27], v[224:227]\n"
    "v_mfma_f32_16x16x32_bf16 v[228:231], a[32:35], a[28:31], v[228:231]\n"
    "v_mfma_f32_16x16x32_bf16 v[232:235], a[36:39], a[28:31], v[232:235]\n"
    "v_mfma_f32_16x16x32_bf16 v[236:239], a[40:43], a[28:31], v[236:239]\n"
    "v_mfma_f32_16x16x32_bf16 v[240:243], a[44:47], a[28:31], v[240:243]\n"
    "s_waitcnt vmcnt(28) lgkmcnt(0)\n"
    "s_barrier\n"
    "v_mfma_f32_16x16x32_bf16 v[244:247], a[48:51], a[28:31], v[244:247]\n"
    "v_mfma_f32_16x16x32_bf16 v[248:251], a[52:55], a[28:31], v[248:251]\n"
    "v_mfma_f32_16x16x32_bf16 v[252:255], a[56:59], a[28:31], v[252:255]\n"
    "v_mfma_f32_16x16x32_bf16 v[32:35], a[92:95], a[60:63], v[32:35]\n"
    "ds_read_b128 a[0:3], v7 offset:32768\n"
    "s_waitcnt vmcnt(2)\n"
    "v_mfma_f32_16x16x32_bf16 v[36:39], a[96:99], a[60:63], v[36:39]\n"
    "ds_read_b64_tr_b16 a[32:33], v8 offset:28928\n"
    "v_accvgpr_read_b32 v25, a128\n"
    "v_accvgpr_read_b32 v10, a129\n"
    "v_mfma_f32_16x16x32_bf16 v[40:43], a[100:103], a[60:63], v[40:43]\n"
    "ds_read_b64_tr_b16 a[34:35], v8 offset:32512\n"
    "v_mov_b32 v15, 0\n"
    "v_mov_b32 v16, 0\n"
    "v_mfma_f32_16x16x32_bf16 v[44:47], a[104:107], a[60:63], v[44:47]\n"
    "ds_read_b64_tr_b16 a[36:37], v8 offset:36112\n"
    "v_accvgpr_read_b32 v18, a130\n"
    "v_accvgpr_read_b32 v11, a131\n"
    "v_mfma_f32_16x16x32_bf16 v[48:51], a[108:111], a[60:63], v[48:51]\n"
    "ds_read_b64_tr_b16 a[38:39], v8 offset:39696\n"
    "v_sub_f32 v18, v18, v25\n"
    "v_add_f32 v10, v10, v11\n"
    "v_mfma_f32_16x16x32_bf16 v[52:55], a[112:115], a[60:63], v[52:55]\n"
    "ds_read_b64_tr_b16 a[40:41], v12 offset:28928\n"
    "v_add_f32 v15, v15, v18\n"
    "v_fmac_f32 v16, v18, v18\n"
    "v_mfma_f32_16x16x32_bf16 v[56:59], a[116:119], a[60:63], v[56:59]\n"
    "ds_read_b64_tr_b16 a[42:43], v12 offset:32512\n"
    "v_accvgpr_read_b32 v18, a132\n"
    "v_accvgpr_read_b32 v11, a133\n"
    "v_mfma_f32_16x16x32_bf16 v[60:63], a[92:95], a[64:67], v[60:63]\n"
    "ds_read_b64_tr_b16 a[44:45], v13 offset:28928\n"
    "v_sub_f32 v18, v18, v25\n"
    "v_add_f32 v10, v10, v11\n"
    "v_mfma_f32_16x16x32_bf16 v[64:67], a[96:99], a[64:67], v[64:67]\n"
    "ds_read_b64_tr_b16 a[46:47], v13 offset:32512\n"
    "v_add_f32 v15, v15, v18\n"
    "v_fmac_f32 v16, v18, v18\n"
    "v_mfma_f32_16x16x32_bf16 v[68:71], a[100:103], a[64:67], v[68:71]\n"
    "v_accvgpr_read_b32 v18, a134\n"
    "v_mfma_f32_16x16x32_bf16 v[72:75], a[104:107], a[64:67], v[72:75]\n"
    "ds_read_b64_tr_b16 a[48:49], v14 offset:28928\n"
    "v_accvgpr_read_b32 v11, a135\n"
    "v_sub_f32 v18, v18, v25\n"
    "v_mfma_f32_16x16x32_bf16 v[76:79], a[108:111], a[64:67], v[76:79]\n"
    "ds_read_b64_tr_b16 a[50:51], v14 offset:32512\n"
    "v_add_f32 v10, v10, v11\n"
    "v_add_f32 v15, v15, v18\n"
    "v_mfma_f32_16x16x32_bf16 v[80:83], a[112:115], a[64:67], v[80:83]\n"
    "ds_read_b64_tr_b16 a[52:53], v14 offset:36112\n"
    "v_fmac_f32 v16, v18, v18\n"
    "v_accvgpr_read_b32 v18, a136\n"
    "v_mfma_f32_16x16x32_bf16 v[84:87], a[116:119], a[64:67], v[84:87]\n"
    "ds_read_b64_tr_b16 a[54:55], v14 offset:39696\n"
    "v_accvgpr_read_b32 v11, a137\n"
    "v_sub_f32 v18, v18, v25\n"
    "v_mfma_f32_16x16x32_bf16 v[88:91], a[92:95], a[68:71], v[88:91]\n"
    "ds_read_b64_tr_b16 a[56:57], v9 offset:28928\n"
    "v_add_f32 v10, v10, v11\n"
    "v_add_f32 v15, v15, v18\n"
    "v_mfma_f32_16x16x32_bf16 v[92:95], a[96:99], a[68:71], v[92:95]\n"
    "ds_read_b64_tr_b16 a[58:59], v9 offset:32512\n"
    "v_fmac_f32 v16, v18, v18\n"
    "v_accvgpr_read_b32 v18, a138\n"
    "v_mfma_f32_16x16x32_bf16 v[96:99], a[100:103], a[68:71], v[96:99]\n"
    "ds_read_b128 a[4:7], v7 offset:33792\n"
    "v_accvgpr_read_b32 v11, a139\n"
    "v_sub_f32 v18, v18, v25\n"
    "v_mfma_f32_16x16x32_bf16 v[100:103], a[104:107], a[68:71], v[100:103]\n"
    "ds_read_b128 a[8:11], v7 offset:34816\n"
    "v_add_f32 v10, v10, v11\n"
    "v_add_f32 v15, v15, v18\n"
    "v_mfma_f32_16x16x32_bf16 v[104:107], a[108:111], a[68:71], v[104:107]\n"
    "ds_read_b128 a[12:15], v7 offset:35840\n"
    "v_fmac_f32 v16, v18, v18\n"
    "v_mfma_f32_16x16x32_bf16 v[108:111], a[112:115], a[68:71], v[108:111]\n"
    "v_accvgpr_read_b32 v18, a140\n"
    "v_accvgpr_read_b32 v11, a141\n"
    "v_mfma_f32_16x16x32_bf16 v[112:115], a[116:119], a[68:71], v[112:115]\n"
    "v_sub_f32 v18, v18, v25\n"
    "v_add_f32 v10, v10, v11\n"
    "v_mfma_f32_16x16x32_bf16 v[116:119], a[92:95], a[72:75], v[116:119]\n"
    "v_add_f32 v15, v15, v18\n"
    "v_fmac_f32 v16, v18, v18\n"
    "v_mfma_f32_16x16x32_bf16 v[120:123], a[96:99], a[72:75], v[120:123]\n"
    "v_accvgpr_read_b32 v18, a142\n"
    "v_accvgpr_read_b32 v11, a143\n"
    "v_mfma_f32_16x16x32_bf16 v[124:127], a[100:103], a[72:75], v[124:127]\n"
    "v_sub_f32 v18, v18, v25\n"
    "v_add_f32 v10, v10, v11\n"
    "v_mfma_f32_16x16x32_bf16 v[128:131], a[104:107], a[72:75], v[128:131]\n"
    "v_add_f32 v15, v15, v18\n"
    "v_fmac_f32 v16, v18, v18\n"
    "v_mfma_f32_16x16x32_bf16 v[132:135], a[108:111], a[72:75], v[132:135]\n"
    "v_accvgpr_read_b32 v18, a144\n"
    "v_accvgpr_read_b32 v11, a145\n"
    "v_mfma_f32_16x16x32_bf16 v[136:139], a[112:115], a[72:75], v[136:139]\n"
    "v_sub_f32 v18, v18, v25\n"
    "v_add_f32 v10, v10, v11\n"
    "s_waitcnt vmcnt(10)\n"
    "s_barrier\n"
    "v_mfma_f32_16x16x32_bf16 v[140:143], a[116:119], a[72:75], v[140:143]\n"
    "v_add_f32 v15, v15, v18\n"
    "s_waitcnt lgkmcnt(15)\n"
    "v_mfma_f32_16x16x32_bf16 v[32:35], a[32:35], a[0:3], v[32:35]\n"
    "ds_read_b128 a[60:63], v7 offset:49152\n"
    "v_fmac_f32 v16, v18, v18\n"
    "v_accvgpr_read_b32 v18, a146\n"
    "s_waitcnt lgkmcnt(14)\n"
    "v_mfma_f32_16x16x32_bf16 v[36:39], a[36:39], a[0:3], v[36:39]\n"
    "ds_read_b64_tr_b16 a[92:93], v8 offset:43392\n"
    "v_accvgpr_read_b32 v11, a147\n"
    "v_sub_f32 v18, v18, v25\n"
    "s_waitcnt lgkmcnt(13)\n"
    "v_mfma_f32_16x16x32_bf16 v[40:43], a[40:43], a[0:3], v[40:43]\n"
    "ds_read_b64_tr_b16 a[94:95], v8 offset:46976\n"
    "v_add_f32 v10, v10, v11\n"
    "v_add_f32 v15, v15, v18\n"
    "s_waitcnt lgkmcnt(12)\n"
    "v_mfma_f32_16x16x32_bf16 v[44:47], a[44:47], a[0:3], v[44:47]\n"
    "ds_read_b64_tr_b16 a[96:97], v8 offset:50576\n"
    "v_fmac_f32 v16, v18, v18\n"
    "v_accvgpr_read_b32 v18, a148\n"
    "s_waitcnt lgkmcnt(11)\n"
    "v_mfma_f32_16x16x32_bf16 v[48:51], a[48:51], a[0:3], v[48:51]\n"
    "ds_read_b64_tr_b16 a[98:99], v8 offset:54160\n"
    "v_accvgpr_read_b32 v11, a149\n"
    "v_sub_f32 v18, v18, v25\n"
    "s_waitcnt lgkmcnt(10)\n"
    "v_mfma_f32_16x16x32_bf16 v[52:55], a[52:55], a[0:3], v[52:55]\n"
    "ds_read_b64_tr_b16 a[100:101], v12 offset:43392\n"
    "v_add_f32 v10, v10, v11\n"
    "s_waitcnt lgkmcnt(9)\n"
    "v_mfma_f32_16x16x32_bf16 v[56:59], a[56:59], a[0:3], v[56:59]\n"
    "ds_read_b64_tr_b16 a[102:103], v12 offset:46976\n"
    "v_add_f32 v15, v15, v18\n"
    "v_fmac_f32 v16, v18, v18\n"
    "s_waitcnt lgkmcnt(9)\n"
    "v_mfma_f32_16x16x32_bf16 v[60:63], a[32:35], a[4:7], v[60:63]\n"
    "ds_read_b64_tr_b16 a[104:105], v13 offset:43392\n"
    "v_accvgpr_read_b32 v18, a150\n"
    "v_accvgpr_read_b32 v11, a151\n"
    "v_mfma_f32_16x16x32_bf16 v[64:67], a[36:39], a[4:7], v[64:67]\n"
    "ds_read_b64_tr_b16 a[106:107], v13 offset:46976\n"
    "v_sub_f32 v18, v18, v25\n"
    "v_add_f32 v10, v10, v11\n"
    "v_mfma_f32_16x16x32_bf16 v[68:71], a[40:43], a[4:7], v[68:71]\n"
    "v_add_f32 v15, v15, v18\n"
    "v_fmac_f32 v16, v18, v18\n"
    "v_mfma_f32_16x16x32_bf16 v[72:75], a[44:47], a[4:7], v[72:75]\n"
    "ds_read_b64_tr_b16 a[108:109], v14 offset:43392\n"
    "v_accvgpr_read_b32 v18, a152\n"
    "v_accvgpr_read_b32 v11, a153\n"
    "v_mfma_f32_16x16x32_bf16 v[76:79], a[48:51], a[4:7], v[76:79]\n"
    "ds_read_b64_tr_b16 a[110:111], v14 offset:46976\n"
    "v_sub_f32 v18, v18, v25\n"
    "v_mfma_f32_16x16x32_bf16 v[80:83], a[52:55], a[4:7], v[80:83]\n"
    "ds_read_b64_tr_b16 a[112:113], v14 offset:50576\n"
    "v_add_f32 v10, v10, v11\n"
    "v_add_f32 v15, v15, v18\n"
    "v_mfma_f32_16x16x32_bf16 v[84:87], a[56:59], a[4:7], v[84:87]\n"
    "ds_read_b64_tr_b16 a[114:115], v14 offset:54160\n"
    "v_fmac_f32 v16, v18, v18\n"
    "v_accvgpr_read_b32 v18, a154\n"
    "s_waitcnt lgkmcnt(14)\n"
    "v_mfma_f32_16x16x32_bf16 v[88:91], a[32:35], a[8:11], v[88:91]\n"
    "ds_read_b64_tr_b16 a[116:117], v9 offset:43392\n"
    "v_accvgpr_read_b32 v11, a155\n"
    "v_sub_f32 v18, v18, v25\n"
    "v_mfma_f32_16x16x32_bf16 v[92:95], a[36:39], a[8:11], v[92:95]\n"
    "ds_read_b64_tr_b16 a[118:119], v9 offset:46976\n"
    "v_add_f32 v10, v10, v11\n"
    "v_add_f32 v15, v15, v18\n"
    "v_mfma_f32_16x16x32_bf16 v[96:99], a[40:43], a[8:11], v[96:99]\n"
    "ds_read_b128 a[64:67], v7 offset:50176\n"
    "v_fmac_f32 v16, v18, v18\n"
    "v_mfma_f32_16x16x32_bf16 v[100:103], a[44:47], a[8:11], v[100:103]\n"
    "ds_read_b128 a[68:71], v7 offset:51200\n"
    "v_accvgpr_read_b32 v18, a156\n"
    "v_accvgpr_read_b32 v11, a157\n"
    "v_mfma_f32_16x16x32_bf16 v[104:107], a[48:51], a[8:11], v[104:107]\n"
    "ds_read_b128 a[72:75], v7 offset:52224\n"
    "v_sub_f32 v18, v18, v25\n"
    "v_add_f32 v10, v10, v11\n"
    "v_mfma_f32_16x16x32_bf16 v[108:111], a[52:55], a[8:11], v[108:111]\n"
    "v_add_f32 v15, v15, v18\n"
    "v_fmac_f32 v16, v18, v18\n"
    "v_mfma_f32_16x16x32_bf16 v[112:115], a[56:59], a[8:11], v[112:115]\n"
    "v_accvgpr_read_b32 v18, a158\n"
    "v_accvgpr_read_b32 v11, a159\n"
    "s_waitcnt lgkmcnt(15)\n"
    "v_mfma_f32_16x16x32_bf16 v[116:119], a[32:35], a[12:15], v[116:119]\n"
    "v_sub_f32 v18, v18, v25\n"
    "v_add_f32 v10, v10, v11\n"
    "v_mfma_f32_16x16x32_bf16 v[120:123], a[36:39], a[12:15], v[120:123]\n"
    "v_add_f32 v15, v15, v18\n"
    "v_mfma_f32_16x16x32_bf16 v[124:127], a[40:43], a[12:15], v[124:127]\n"
    "v_fmac_f32 v16, v18, v18\n"
    "v_mul_f32 v15, 0x3d800000, v15\n"
    "v_mfma_f32_16x16x32_bf16 v[128:131], a[44:47], a[12:15], v[128:131]\n"
    "v_add_f32 v25, v25, v15\n"
    "v_mul_f32 v15, v15, v15\n"
    "v_mfma_f32_16x16x32_bf16 v[132:135], a[48:51], a[12:15], v[132:135]\n"
    "v_fmac_f32 v16, 0xc1800000, v15\n"
    "v_fmac_f32 v10, 0x42800000, v16\n"
    "v_mfma_f32_16x16x32_bf16 v[136:139], a[52:55], a[12:15], v[136:139]\n"
    "v_mov_b32 v11, 0x3727c5ac\n"
    "v_fmac_f32 v11, 0x3a800000, v10\n"
    "v_mfma_f32_16x16x32_bf16 v[140:143], a[56:59], a[12:15], v[140:143]\n"
    "v_rcp_f32 v11, v11\n"
    "v_mfma_f32_16x16x32_bf16 v[32:35], a[92:95], a[60:63], v[32:35]\n"
    "ds_write_b32 v19, v25\n"
    "s_waitcnt lgkmcnt(14)\n"
    "v_mfma_f32_16x16x32_bf16 v[36:39], a[96:99], a[60:63], v[36:39]\n"
    "global_store_dword v24, v11, s[76:77] sc1\n"
    "s_waitcnt lgkmcnt(12)\n"
    "v_mfma_f32_16x16x32_bf16 v[40:43], a[100:103], a[60:63], v[40:43]\n"
    "ds_read_b128 a[16:19], v7 offset:20480\n"
    "s_waitcnt lgkmcnt(11)\n"
    "v_mfma_f32_16x16x32_bf16 v[44:47], a[104:107], a[60:63], v[44:47]\n"
    "ds_read_b64_tr_b16 a[32:33], v8 offset:14464\n"
    "s_waitcnt lgkmcnt(10)\n"
    "v_mfma_f32_16x16x32_bf16 v[48:51], a[108:111], a[60:63], v[48:51]\n"
    "ds_read_b64_tr_b16 a[34:35], v8 offset:18048\n"
    "s_waitcnt lgkmcnt(9)\n"
    "v_mfma_f32_16x16x32_bf16 v[52:55], a[112:115], a[60:63], v[52:55]\n"
    "ds_read_b64_tr_b16 a[36:37], v8 offset:21648\n"
    "s_waitcnt lgkmcnt(8)\n"
    "v_mfma_f32_16x16x32_bf16 v[56:59], a[116:119], a[60:63], v[56:59]\n"
    "ds_read_b64_tr_b16 a[38:39], v8 offset:25232\n"
    "s_waitcnt lgkmcnt(8)\n"
    "v_mfma_f32_16x16x32_bf16 v[60:63], a[92:95], a[64:67], v[60:63]\n"
    "ds_read_b64_tr_b16 a[40:41], v12 offset:14464\n"
    "v_mfma_f32_16x16x32_bf16 v[64:67], a[96:99], a[64:67], v[64:67]\n"
    "ds_read_b64_tr_b16 a[42:43], v12 offset:18048\n"
    "v_mfma_f32_16x16x32_bf16 v[68:71], a[100:103], a[64:67], v[68:71]\n"
    "ds_read_b64_tr_b16 a[44:45], v13 offset:14464\n"
    "v_mfma_f32_16x16x32_bf16 v[72:75], a[104:107], a[64:67], v[72:75]\n"
    "ds_read_b64_tr_b16 a[46:47], v13 offset:18048\n"
    "v_mfma_f32_16x16x32_bf16 v[76:79], a[108:111], a[64:67], v[76:79]\n"
    "v_mfma_f32_16x16x32_bf16 v[80:83], a[112:115], a[64:67], v[80:83]\n"
    "ds_write_b32 v20, a160\n"
    "v_mfma_f32_16x16x32_bf16 v[84:87], a[116:119], a[64:67], v[84:87]\n"
    "s_waitcnt lgkmcnt(12)\n"
    "v_mfma_f32_16x16x32_bf16 v[88:91], a[92:95], a[68:71], v[88:91]\n"
    "ds_read_b64_tr_b16 a[48:49], v14 offset:14464\n"
    "v_mfma_f32_16x16x32_bf16 v[92:95], a[96:99], a[68:71], v[92:95]\n"
    "ds_read_b64_tr_b16 a[50:51], v14 offset:18048\n"
    "v_mfma_f32_16x16x32_bf16 v[96:99], a[100:103], a[68:71], v[96:99]\n"
    "ds_read_b64_tr_b16 a[52:53], v14 offset:21648\n"
    "v_mfma_f32_16x16x32_bf16 v[100:103], a[104:107], a[68:71], v[100:103]\n"
    "ds_write_b32 v21, a160\n"
    "ds_read_b64_tr_b16 a[54:55], v14 offset:25232\n"
    "v_mfma_f32_16x16x32_bf16 v[104:107], a[108:111], a[68:71], v[104:107]\n"
    "ds_read_b64_tr_b16 a[56:57], v9 offset:14464\n"
    "v_mfma_f32_16x16x32_bf16 v[108:111], a[112:115], a[68:71], v[108:111]\n"
    "ds_read_b64_tr_b16 a[58:59], v9 offset:18048\n"
    "v_mfma_f32_16x16x32_bf16 v[112:115], a[116:119], a[68:71], v[112:115]\n"
    "ds_read_b128 a[20:23], v7 offset:21504\n"
    "s_waitcnt lgkmcnt(15)\n"
    "v_mfma_f32_16x16x32_bf16 v[116:119], a[92:95], a[72:75], v[116:119]\n"
    "ds_read_b128 a[24:27], v7 offset:22528\n"
    "v_mfma_f32_16x16x32_bf16 v[120:123], a[96:99], a[72:75], v[120:123]\n"
    "ds_read_b128 a[28:31], v7 offset:23552\n"
    "v_mfma_f32_16x16x32_bf16 v[124:127], a[100:103], a[72:75], v[124:127]\n"
    "v_mfma_f32_16x16x32_bf16 v[128:131], a[104:107], a[72:75], v[128:131]\n"
    "v_mfma_f32_16x16x32_bf16 v[132:135], a[108:111], a[72:75], v[132:135]\n"
    "v_mfma_f32_16x16x32_bf16 v[136:139], a[112:115], a[72:75], v[136:139]\n"
    "v_mfma_f32_16x16x32_bf16 v[140:143], a[116:119], a[72:75], v[140:143]\n"
    "s_waitcnt lgkmcnt(0)\n"
    "s_barrier\n"
    "v_mfma_f32_16x16x32_bf16 v[144:147], a[32:35], a[16:19], v[144:147]\n"
    "ds_read2_b32 v[20:21], v31 offset1:16\n"
    "ds_read2_b32 v[22:23], v31 offset0:32 offset1:48\n"
    "ds_read_b128 a[176:179], v30 offset:0\n"
    "v_mfma_f32_16x16x32_bf16 v[148:151], a[36:39], a[16:19], v[148:151]\n"
    "ds_read_b128 a[180:183], v30 offset:64\n"
    "ds_read_b128 a[184:187], v30 offset:128\n"
    "ds_read_b128 a[188:191], v30 offset:192\n"
    "v_mfma_f32_16x16x32_bf16 v[152:155], a[40:43], a[16:19], v[152:155]\n"
    "ds_read_b128 a[192:195], v30 offset:256\n"
    "ds_read_b128 a[196:199], v30 offset:320\n"
    "ds_read_b128 a[200:203], v30 offset:384\n"
    "v_mfma_f32_16x16x32_bf16 v[156:159], a[44:47], a[16:19], v[156:159]\n"
    "ds_read_b128 a[76:79], v7 offset:36864\n"
    "ds_read_b64_tr_b16 a[92:93], v8 offset:28928\n"
    "v_mfma_f32_16x16x32_bf16 v[160:163], a[48:51], a[16:19], v[160:163]\n"
    "ds_read_b64_tr_b16 a[94:95], v8 offset:32512\n"
    "v_mfma_f32_16x16x32_bf16 v[164:167], a[52:55], a[16:19], v[164:167]\n"
    "ds_read_b64_tr_b16 a[96:97], v8 offset:36112\n"
    "v_mfma_f32_16x16x32_bf16 v[168:171], a[56:59], a[16:19], v[168:171]\n"
    "ds_read_b64_tr_b16 a[98:99], v8 offset:39696\n"
    "s_waitcnt lgkmcnt(11)\n"
    "v_accvgpr_read_b32 v10, a176\n"
    "v_accvgpr_read_b32 v11, a177\n"
    "v_accvgpr_read_b32 v15, a178\n"
    "v_accvgpr_read_b32 v16, a179\n"
    "v_mfma_f32_16x16x32_bf16 v[172:175], a[32:35], a[20:23], v[172:175]\n"
    "ds_read_b64_tr_b16 a[100:101], v12 offset:28928\n"
    "s_waitcnt lgkmcnt(11)\n"
    "v_accvgpr_read_b32 v17, a180\n"
    "v_accvgpr_read_b32 v18, a181\n"
    "v_accvgpr_read_b32 v24, a182\n"
    "v_mfma_f32_16x16x32_bf16 v[176:179], a[36:39], a[20:23], v[176:179]\n"
    "ds_read_b64_tr_b16 a[102:103], v12 offset:32512\n"
    "v_accvgpr_read_b32 v25, a183\n"
    "v_fma_f32 v32, -v20, v10, v32\n"
    "v_fma_f32 v33, -v20, v11, v33\n"
    "v_mfma_f32_16x16x32_bf16 v[180:183], a[40:43], a[20:23], v[180:183]\n"
    "ds_read_b64_tr_b16 a[104:105], v13 offset:28928\n"
    "v_fma_f32 v34, -v20, v15, v34\n"
    "v_fma_f32 v35, -v20, v16, v35\n"
    "v_fma_f32 v36, -v20, v17, v36\n"
    "v_mfma_f32_16x16x32_bf16 v[184:187], a[44:47], a[20:23], v[184:187]\n"
    "ds_read_b64_tr_b16 a[106:107], v13 offset:32512\n"
    "v_fma_f32 v37, -v20, v18, v37\n"
    "v_fma_f32 v38, -v20, v24, v38\n"
    "v_fma_f32 v39, -v20, v25, v39\n"
    "v_mfma_f32_16x16x32_bf16 v[188:191], a[48:51], a[20:23], v[188:191]\n"
    "ds_read_b64_tr_b16 a[108:109], v14 offset:28928\n"
    "v_mul_f32 v32, v32, v36\n"
    "v_mul_f32 v33, v33, v37\n"
    "v_mul_f32 v34, v34, v38\n"
    "v_mul_f32 v35, v35, v39\n"
    "v_mfma_f32_16x16x32_bf16 v[192:195], a[52:55], a[20:23], v[192:195]\n"
    "ds_read_b64_tr_b16 a[110:111], v14 offset:32512\n"
    "v_cvt_pk_bf16_f32 v32, v32, v33\n"
    "v_cvt_pk_bf16_f32 v33, v34, v35\n"
    "global_store_dwordx2 v26, v[32:33], s[78:79] offset:0 sc1\n"
    "v_mfma_f32_16x16x32_bf16 v[196:199], a[56:59], a[20:23], v[196:199]\n"
    "ds_read_b64_tr_b16 a[112:113], v14 offset:36112\n"
    "v_fma_f32 v60, -v21, v10, v60\n"
    "v_fma_f32 v61, -v21, v11, v61\n"
    "v_fma_f32 v62, -v21, v15, v62\n"
    "v_mfma_f32_16x16x32_bf16 v[200:203], a[32:35], a[24:27], v[200:203]\n"
    "ds_read_b64_tr_b16 a[114:115], v14 offset:39696\n"
    "v_fma_f32 v63, -v21, v16, v63\n"
    "v_fma_f32 v64, -v21, v17, v64\n"
    "v_fma_f32 v65, -v21, v18, v65\n"
    "v_mfma_f32_16x16x32_bf16 v[204:207], a[36:39], a[24:27], v[204:207]\n"
    "ds_read_b64_tr_b16 a[116:117], v9 offset:28928\n"
    "v_fma_f32 v66, -v21, v24, v66\n"
    "v_fma_f32 v67, -v21, v25, v67\n"
    "v_mul_f32 v60, v60, v64\n"
    "v_mfma_f32_16x16x32_bf16 v[208:211], a[40:43], a[24:27], v[208:211]\n"
    "ds_read_b64_tr_b16 a[118:119], v9 offset:32512\n"
    "v_mul_f32 v61, v61, v65\n"
    "v_mul_f32 v62, v62, v66\n"
    "v_mul_f32 v63, v63, v67\n"
    "v_mfma_f32_16x16x32_bf16 v[212:215], a[44:47], a[24:27], v[212:215]\n"
    "ds_read_b128 a[80:83], v7 offset:37888\n"
    "v_cvt_pk_bf16_f32 v60, v60, v61\n"
    "v_cvt_pk_bf16_f32 v61, v62, v63\n"
    "global_store_dwordx2 v26, v[60:61], s[78:79] offset:1024 sc1\n"
    "v_fma_f32 v88, -v22, v10, v88\n"
    "v_mfma_f32_16x16x32_bf16 v[216:219], a[48:51], a[24:27], v[216:219]\n"
    "ds_read_b128 a[84:87], v7 offset:38912\n"
    "v_fma_f32 v89, -v22, v11, v89\n"
    "v_fma_f32 v90, -v22, v15, v90\n"
    "v_fma_f32 v91, -v22, v16, v91\n"
    "v_mfma_f32_16x16x32_bf16 v[220:223], a[52:55], a[24:27], v[220:223]\n"
    "ds_read_b128 a[88:91], v7 offset:39936\n"
    "v_fma_f32 v92, -v22, v17, v92\n"
    "v_fma_f32 v93, -v22, v18, v93\n"
    "v_fma_f32 v94, -v22, v24, v94\n"
    "v_mfma_f32_16x16x32_bf16 v[224:227], a[56:59], a[24:27], v[224:227]\n"
    "v_fma_f32 v95, -v22, v25, v95\n"
    "v_mul_f32 v88, v88, v92\n"
    "v_mul_f32 v89, v89, v93\n"
    "v_mfma_f32_16x16x32_bf16 v[228:231], a[32:35], a[28:31], v[228:231]\n"
    "v_mul_f32 v90, v90, v94\n"
    "v_mul_f32 v91, v91, v95\n"
    "v_cvt_pk_bf16_f32 v88, v88, v89\n"
    "v_mfma_f32_16x16x32_bf16 v[232:235], a[36:39], a[28:31], v[232:235]\n"
    "v_cvt_pk_bf16_f32 v89, v90, v91\n"
    "global_store_dwordx2 v26, v[88:89], s[78:79] offset:2048 sc1\n"
    "v_fma_f32 v116, -v23, v10, v116\n"
    "v_fma_f32 v117, -v23, v11, v117\n"
    "v_mfma_f32_16x16x32_bf16 v[236:239], a[40:43], a[28:31], v[236:239]\n"
    "v_fma_f32 v118, -v23, v15, v118\n"
    "v_fma_f32 v119, -v23, v16, v119\n"
    "v_fma_f32 v120, -v23, v17, v120\n"
    "v_mfma_f32_16x16x32_bf16 v[240:243], a[44:47], a[28:31], v[240:243]\n"
    "v_fma_f32 v121, -v23, v18, v121\n"
    "v_fma_f32 v122, -v23, v24, v122\n"
    "v_fma_f32 v123, -v23, v25, v123\n"
    "v_mfma_f32_16x16x32_bf16 v[244:247], a[48:51], a[28:31], v[244:247]\n"
    "v_mul_f32 v116, v116, v120\n"
    "v_mul_f32 v117, v117, v121\n"
    "v_mul_f32 v118, v118, v122\n"
    "v_mfma_f32_16x16x32_bf16 v[248:251], a[52:55], a[28:31], v[248:251]\n"
    "v_mul_f32 v119, v119, v123\n"
    "v_cvt_pk_bf16_f32 v116, v116, v117\n"
    "v_cvt_pk_bf16_f32 v117, v118, v119\n"
    "v_mfma_f32_16x16x32_bf16 v[252:255], a[56:59], a[28:31], v[252:255]\n"
    "global_store_dwordx2 v26, v[116:117], s[78:79] offset:3072 sc1\n"
    "s_waitcnt lgkmcnt(15)\n"
    "v_accvgpr_read_b32 v10, a184\n"
    "v_accvgpr_read_b32 v11, a185\n"
    "v_mfma_f32_16x16x32_bf16 v[144:147], a[92:95], a[76:79], v[144:147]\n"
    "ds_read_b128 a[16:19], v7 offset:53248\n"
    "v_accvgpr_read_b32 v15, a186\n"
    "v_accvgpr_read_b32 v16, a187\n"
    "v_accvgpr_read_b32 v17, a188\n"
    "v_accvgpr_read_b32 v18, a189\n"
    "s_waitcnt lgkmcnt(14)\n"
    "v_mfma_f32_16x16x32_bf16 v[148:151], a[96:99], a[76:79], v[148:151]\n"
    "ds_read_b64_tr_b16 a[32:33], v8 offset:43392\n"
    "v_accvgpr_read_b32 v24, a190\n"
    "v_accvgpr_read_b32 v25, a191\n"
    "v_fma_f32 v40, -v20, v10, v40\n"
    "s_waitcnt lgkmcnt(13)\n"
    "v_mfma_f32_16x16x32_bf16 v[152:155], a[100:103], a[76:79], v[152:155]\n"
    "ds_read_b64_tr_b16 a[34:35], v8 offset:46976\n"
    "v_fma_f32 v41, -v20, v11, v41\n"
    "v_fma_f32 v42, -v20, v15, v42\n"
    "v_fma_f32 v43, -v20, v16, v43\n"
    "s_waitcnt lgkmcnt(12)\n"
    "v_mfma_f32_16x16x32_bf16 v[156:159], a[104:107], a[76:79], v[156:159]\n"
    "ds_read_b64_tr_b16 a[36:37], v8 offset:50576\n"
    "v_fma_f32 v44, -v20, v17, v44\n"
    "v_fma_f32 v45, -v20, v18, v45\n"
    "v_fma_f32 v46, -v20, v24, v46\n"
    "s_waitcnt lgkmcnt(11)\n"
    "v_mfma_f32_16x16x32_bf16 v[160:163], a[108:111], a[76:79], v[160:163]\n"
    "ds_read_b64_tr_b16 a[38:39], v8 offset:54160\n"
    "v_fma_f32 v47, -v20, v25, v47\n"
    "v_mul_f32 v40, v40, v44\n"
    "v_mul_f32 v41, v41, v45\n"
    "s_waitcnt lgkmcnt(10)\n"
    "v_mfma_f32_16x16x32_bf16 v[164:167], a[112:115], a[76:79], v[164:167]\n"
    "ds_read_b64_tr_b16 a[40:41], v12 offset:43392\n"
    "v_mul_f32 v42, v42, v46\n"
    "v_mul_f32 v43, v43, v47\n"
    "v_cvt_pk_bf16_f32 v40, v40, v41\n"
    "s_waitcnt lgkmcnt(9)\n"
    "v_mfma_f32_16x16x32_bf16 v[168:171], a[116:119], a[76:79], v[168:171]\n"
    "ds_read_b64_tr_b16 a[42:43], v12 offset:46976\n"
    "v_cvt_pk_bf16_f32 v41, v42, v43\n"
    "global_store_dwordx2 v27, v[40:41], s[78:79] offset:0 sc1\n"
    "v_fma_f32 v68, -v21, v10, v68\n"
    "s_waitcnt lgkmcnt(9)\n"
    "v_mfma_f32_16x16x32_bf16 v[172:175], a[92:95], a[80:83], v[172:175]\n"
    "ds_read_b64_tr_b16 a[44:45], v13 offset:43392\n"
    "v_fma_f32 v69, -v21, v11, v69\n"
    "v_fma_f32 v70, -v21, v15, v70\n"
    "v_fma_f32 v71, -v21, v16, v71\n"
    "v_fma_f32 v72, -v21, v17, v72\n"
    "v_mfma_f32_16x16x32_bf16 v[176:179], a[96:99], a[80:83], v[176:179]\n"
    "ds_read_b64_tr_b16 a[46:47], v13 offset:46976\n"
    "v_fma_f32 v73, -v21, v18, v73\n"
    "v_fma_f32 v74, -v21, v24, v74\n"
    "v_fma_f32 v75, -v21, v25, v75\n"
    "v_mfma_f32_16x16x32_bf16 v[180:183], a[100:103], a[80:83], v[180:183]\n"
    "v_mul_f32 v68, v68, v72\n"
    "v_mul_f32 v69, v69, v73\n"
    "v_mul_f32 v70, v70, v74\n"
    "v_mfma_f32_16x16x32_bf16 v[184:187], a[104:107], a[80:83], v[184:187]\n"
    "ds_read_b64_tr_b16 a[48:49], v14 offset:43392\n"
    "v_mul_f32 v71, v71, v75\n"
    "v_cvt_pk_bf16_f32 v68, v68, v69\n"
    "v_cvt_pk_bf16_f32 v69, v70, v71\n"
    "v_mfma_f32_16x16x32_bf16 v[188:191], a[108:111], a[80:83], v[188:191]\n"
    "ds_read_b64_tr_b16 a[50:51], v14 offset:46976\n"
    "global_store_dwordx2 v27, v[68:69], s[78:79] offset:1024 sc1\n"
    "v_fma_f32 v96, -v22, v10, v96\n"
    "v_fma_f32 v97, -v22, v11, v97\n"
    "v_mfma_f32_16x16x32_bf16 v[192:195], a[112:115], a[80:83], v[192:195]\n"
    "ds_read_b64_tr_b16 a[52:53], v14 offset:50576\n"
    "v_fma_f32 v98, -v22, v15, v98\n"
    "v_fma_f32 v99, -v22, v16, v99\n"
    "v_fma_f32 v100, -v22, v17, v100\n"
    "v_mfma_f32_16x16x32_bf16 v[196:199], a[116:119], a[80:83], v[196:199]\n"
    "ds_read_b64_tr_b16 a[54:55], v14 offset:54160\n"
    "v_fma_f32 v101, -v22, v18, v101\n"
    "v_fma_f32 v102, -v22, v24, v102\n"
    "v_fma_f32 v103, -v22, v25, v103\n"
    "s_waitcnt lgkmcnt(14)\n"
    "v_mfma_f32_16x16x32_bf16 v[200:203], a[92:95], a[84:87], v[200:203]\n"
    "ds_read_b64_tr_b16 a[56:57], v9 offset:43392\n"
    "v_mul_f32 v96, v96, v100\n"
    "v_mul_f32 v97, v97, v101\n"
    "v_mul_f32 v98, v98, v102\n"
    "v_mul_f32 v99, v99, v103\n"
    "v_mfma_f32_16x16x32_bf16 v[204:207], a[96:99], a[84:87], v[204:207]\n"
    "ds_read_b64_tr_b16 a[58:59], v9 offset:46976\n"
    "v_cvt_pk_bf16_f32 v96, v96, v97\n"
    "v_cvt_pk_bf16_f32 v97, v98, v99\n"
    "global_store_dwordx2 v27, v[96:97], s[78:79] offset:2048 sc1\n"
    "v_mfma_f32_16x16x32_bf16 v[208:211], a[100:103], a[84:87], v[208:211]\n"
    "ds_read_b128 a[20:23], v7 offset:54272\n"
    "v_fma_f32 v124, -v23, v10, v124\n"
    "v_fma_f32 v125, -v23, v11, v125\n"
    "v_fma_f32 v126, -v23, v15, v126\n"
    "v_mfma_f32_16x16x32_bf16 v[212:215], a[104:107], a[84:87], v[212:215]\n"
    "ds_read_b128 a[24:27], v7 offset:55296\n"
    "v_fma_f32 v127, -v23, v16, v127\n"
    "v_fma_f32 v128, -v23, v17, v128\n"
    "v_fma_f32 v129, -v23, v18, v129\n"
    "v_mfma_f32_16x16x32_bf16 v[216:219], a[108:111], a[84:87], v[216:219]\n"
    "ds_read_b128 a[28:31], v7 offset:56320\n"
    "v_fma_f32 v130, -v23, v24, v130\n"
    "v_fma_f32 v131, -v23, v25, v131\n"
    "v_mul_f32 v124, v124, v128\n"
    "v_mfma_f32_16x16x32_bf16 v[220:223], a[112:115], a[84:87], v[220:223]\n"
    "v_mul_f32 v125, v125, v129\n"
    "v_mul_f32 v126, v126, v130\n"
    "v_mul_f32 v127, v127, v131\n"
    "v_mfma_f32_16x16x32_bf16 v[224:227], a[116:119], a[84:87], v[224:227]\n"
    "v_cvt_pk_bf16_f32 v124, v124, v125\n"
    "v_cvt_pk_bf16_f32 v125, v126, v127\n"
    "global_store_dwordx2 v27, v[124:125], s[78:79] offset:3072 sc1\n"
    "s_waitcnt lgkmcnt(15)\n"
    "v_mfma_f32_16x16x32_bf16 v[228:231], a[92:95], a[88:91], v[228:231]\n"
    "v_accvgpr_read_b32 v10, a192\n"
    "v_accvgpr_read_b32 v11, a193\n"
    "v_accvgpr_read_b32 v15, a194\n"
    "v_accvgpr_read_b32 v16, a195\n"
    "v_mfma_f32_16x16x32_bf16 v[232:235], a[96:99], a[88:91], v[232:235]\n"
    "v_accvgpr_read_b32 v17, a196\n"
    "v_accvgpr_read_b32 v18, a197\n"
    "v_accvgpr_read_b32 v24, a198\n"
    "v_mfma_f32_16x16x32_bf16 v[236:239], a[100:103], a[88:91], v[236:239]\n"
    "v_accvgpr_read_b32 v25, a199\n"
    "v_fma_f32 v48, -v20, v10, v48\n"
    "v_fma_f32 v49, -v20, v11, v49\n"
    "v_mfma_f32_16x16x32_bf16 v[240:243], a[104:107], a[88:91], v[240:243]\n"
    "v_fma_f32 v50, -v20, v15, v50\n"
    "v_fma_f32 v51, -v20, v16, v51\n"
    "v_fma_f32 v52, -v20, v17, v52\n"
    "v_mfma_f32_16x16x32_bf16 v[244:247], a[108:111], a[88:91], v[244:247]\n"
    "v_fma_f32 v53, -v20, v18, v53\n"
    "v_fma_f32 v54, -v20, v24, v54\n"
    "v_fma_f32 v55, -v20, v25, v55\n"
    "v_mfma_f32_16x16x32_bf16 v[248:251], a[112:115], a[88:91], v[248:251]\n"
    "v_mul_f32 v48, v48, v52\n"
    "v_mul_f32 v49, v49, v53\n"
    "v_mul_f32 v50, v50, v54\n"
    "v_mfma_f32_16x16x32_bf16 v[252:255], a[116:119], a[88:91], v[252:255]\n"
    "v_mul_f32 v51, v51, v55\n"
    "v_cvt_pk_bf16_f32 v48, v48, v49\n"
    "v_cvt_pk_bf16_f32 v49, v50, v51\n"
    "v_mfma_f32_16x16x32_bf16 v[144:147], a[32:35], a[16:19], v[144:147]\n"
    "global_store_dwordx2 v28, v[48:49], s[78:79] offset:0 sc1\n"
    "v_fma_f32 v76, -v21, v10, v76\n"
    "v_fma_f32 v77, -v21, v11, v77\n"
    "v_fma_f32 v78, -v21, v15, v78\n"
    "s_waitcnt lgkmcnt(13)\n"
    "v_mfma_f32_16x16x32_bf16 v[148:151], a[36:39], a[16:19], v[148:151]\n"
    "v_fma_f32 v79, -v21, v16, v79\n"
    "v_fma_f32 v80, -v21, v17, v80\n"
    "v_fma_f32 v81, -v21, v18, v81\n"
    "s_waitcnt lgkmcnt(11)\n"
    "v_mfma_f32_16x16x32_bf16 v[152:155], a[40:43], a[16:19], v[152:155]\n"
    "v_fma_f32 v82, -v21, v24, v82\n"
    "v_fma_f32 v83, -v21, v25, v83\n"
    "v_mul_f32 v76, v76, v80\n"
    "v_mul_f32 v77, v77, v81\n"
    "s_waitcnt lgkmcnt(9)\n"
    "v_mfma_f32_16x16x32_bf16 v[156:159], a[44:47], a[16:19], v[156:159]\n"
    "v_mul_f32 v78, v78, v82\n"
    "v_mul_f32 v79, v79, v83\n"
    "v_cvt_pk_bf16_f32 v76, v76, v77\n"
    "s_waitcnt lgkmcnt(7)\n"
    "v_mfma_f32_16x16x32_bf16 v[160:163], a[48:51], a[16:19], v[160:163]\n"
    "v_cvt_pk_bf16_f32 v77, v78, v79\n"
    "global_store_dwordx2 v28, v[76:77], s[78:79] offset:1024 sc1\n"
    "v_fma_f32 v104, -v22, v10, v104\n"
    "s_waitcnt lgkmcnt(5)\n"
    "v_mfma_f32_16x16x32_bf16 v[164:167], a[52:55], a[16:19], v[164:167]\n"
    "v_fma_f32 v105, -v22, v11, v105\n"
    "v_fma_f32 v106, -v22, v15, v106\n"
    "v_fma_f32 v107, -v22, v16, v107\n"
    "v_fma_f32 v108, -v22, v17, v108\n"
    "s_waitcnt lgkmcnt(3)\n"
    "v_mfma_f32_16x16x32_bf16 v[168:171], a[56:59], a[16:19], v[168:171]\n"
    "v_fma_f32 v109, -v22, v18, v109\n"
    "v_fma_f32 v110, -v22, v24, v110\n"
    "v_fma_f32 v111, -v22, v25, v111\n"
    "s_waitcnt lgkmcnt(2)\n"
    "v_mfma_f32_16x16x32_bf16 v[172:175], a[32:35], a[20:23], v[172:175]\n"
    "v_mul_f32 v104, v104, v108\n"
    "v_mul_f32 v105, v105, v109\n"
    "v_mul_f32 v106, v106, v110\n"
    "v_mul_f32 v107, v107, v111\n"
    "v_mfma_f32_16x16x32_bf16 v[176:179], a[36:39], a[20:23], v[176:179]\n"
    "v_cvt_pk_bf16_f32 v104, v104, v105\n"
    "v_cvt_pk_bf16_f32 v105, v106, v107\n"
    "global_store_dwordx2 v28, v[104:105], s[78:79] offset:2048 sc1\n"
    "v_mfma_f32_16x16x32_bf16 v[180:183], a[40:43], a[20:23], v[180:183]\n"
    "v_fma_f32 v132, -v23, v10, v132\n"
    "v_fma_f32 v133, -v23, v11, v133\n"
    "v_fma_f32 v134, -v23, v15, v134\n"
    "v_mfma_f32_16x16x32_bf16 v[184:187], a[44:47], a[20:23], v[184:187]\n"
    "v_fma_f32 v135, -v23, v16, v135\n"
    "v_fma_f32 v136, -v23, v17, v136\n"
    "v_fma_f32 v137, -v23, v18, v137\n"
    "v_fma_f32 v138, -v23, v24, v138\n"
    "v_mfma_f32_16x16x32_bf16 v[188:191], a[48:51], a[20:23], v[188:191]\n"
    "v_fma_f32 v139, -v23, v25, v139\n"
    "v_mul_f32 v132, v132, v136\n"
    "v_mul_f32 v133, v133, v137\n"
    "v_mfma_f32_16x16x32_bf16 v[192:195], a[52:55], a[20:23], v[192:195]\n"
    "v_mul_f32 v134, v134, v138\n"
    "v_mul_f32 v135, v135, v139\n"
    "v_cvt_pk_bf16_f32 v132, v132, v133\n"
    "v_mfma_f32_16x16x32_bf16 v[196:199], a[56:59], a[20:23], v[196:199]\n"
    "v_cvt_pk_bf16_f32 v133, v134, v135\n"
    "global_store_dwordx2 v28, v[132:133], s[78:79] offset:3072 sc1\n"
    "v_accvgpr_read_b32 v10, a200\n"
    "v_accvgpr_read_b32 v11, a201\n"
    "s_waitcnt lgkmcnt(1)\n"
    "v_mfma_f32_16x16x32_bf16 v[200:203], a[32:35], a[24:27], v[200:203]\n"
    "v_accvgpr_read_b32 v15, a202\n"
    "v_accvgpr_read_b32 v16, a203\n"
    "v_fma_f32 v56, -v20, v10, v56\n"
    "v_mfma_f32_16x16x32_bf16 v[204:207], a[36:39], a[24:27], v[204:207]\n"
    "v_fma_f32 v57, -v20, v11, v57\n"
    "v_fma_f32 v58, -v20, v15, v58\n"
    "v_fma_f32 v59, -v20, v16, v59\n"
    "v_fma_f32 v84, -v21, v10, v84\n"
    "v_mfma_f32_16x16x32_bf16 v[208:211], a[40:43], a[24:27], v[208:211]\n"
    "v_fma_f32 v85, -v21, v11, v85\n"
    "v_fma_f32 v86, -v21, v15, v86\n"
    "v_fma_f32 v87, -v21, v16, v87\n"
    "ds_read_b128 v[32:35], v30 offset:0\n"
    "v_mfma_f32_16x16x32_bf16 v[212:215], a[44:47], a[24:27], v[212:215]\n"
    "v_fma_f32 v112, -v22, v10, v112\n"
    "v_fma_f32 v113, -v22, v11, v113\n"
    "v_fma_f32 v114, -v22, v15, v114\n"
    "ds_read_b128 v[36:39], v30 offset:64\n"
    "v_mfma_f32_16x16x32_bf16 v[216:219], a[48:51], a[24:27], v[216:219]\n"
    "v_fma_f32 v115, -v22, v16, v115\n"
    "v_fma_f32 v140, -v23, v10, v140\n"
    "v_fma_f32 v141, -v23, v11, v141\n"
    "v_fma_f32 v142, -v23, v15, v142\n"
    "ds_read_b128 v[40:43], v30 offset:128\n"
    "v_mfma_f32_16x16x32_bf16 v[220:223], a[52:55], a[24:27], v[220:223]\n"
    "v_fma_f32 v143, -v23, v16, v143\n"
    "v_permlane32_swap_b32 v56, v84\n"
    "v_permlane32_swap_b32 v57, v85\n"
    "ds_read_b128 v[44:47], v30 offset:192\n"
    "v_mfma_f32_16x16x32_bf16 v[224:227], a[56:59], a[24:27], v[224:227]\n"
    "v_permlane32_swap_b32 v58, v86\n"
    "v_permlane32_swap_b32 v59, v87\n"
    "v_mul_f32 v56, v56, v84\n"
    "v_mul_f32 v57, v57, v85\n"
    "ds_read_b128 v[48:51], v30 offset:256\n"
    "s_waitcnt lgkmcnt(5)\n"
    "v_mfma_f32_16x16x32_bf16 v[228:231], a[32:35], a[28:31], v[228:231]\n"
    "v_mul_f32 v58, v58, v86\n"
    "v_mul_f32 v59, v59, v87\n"
    "v_cvt_pk_bf16_f32 v56, v56, v57\n"
    "ds_read_b128 v[52:55], v30 offset:320\n"
    "v_mfma_f32_16x16x32_bf16 v[232:235], a[36:39], a[28:31], v[232:235]\n"
    "v_cvt_pk_bf16_f32 v57, v58, v59\n"
    "global_store_dwordx2 v29, v[56:57], s[78:79] offset:0 sc1\n"
    "v_permlane32_swap_b32 v112, v140\n"
    "ds_read2_b32 v[60:61], v31 offset0:64 offset1:80\n"
    "v_mfma_f32_16x16x32_bf16 v[236:239], a[40:43], a[28:31], v[236:239]\n"
    "v_permlane32_swap_b32 v113, v141\n"
    "v_permlane32_swap_b32 v114, v142\n"
    "v_permlane32_swap_b32 v115, v143\n"
    "v_mul_f32 v112, v112, v140\n"
    "ds_read2_b32 v[62:63], v31 offset0:96 offset1:112\n"
    "v_mfma_f32_16x16x32_bf16 v[240:243], a[44:47], a[28:31], v[240:243]\n"
    "v_mul_f32 v113, v113, v141\n"
    "v_mul_f32 v114, v114, v142\n"
    "v_mul_f32 v115, v115, v143\n"
    "v_mfma_f32_16x16x32_bf16 v[244:247], a[48:51], a[28:31], v[244:247]\n"
    "v_cvt_pk_bf16_f32 v112, v112, v113\n"
    "v_cvt_pk_bf16_f32 v113, v114, v115\n"
    "global_store_dwordx2 v29, v[112:113], s[78:79] offset:2048 sc1\n"
    "v_mfma_f32_16x16x32_bf16 v[248:251], a[52:55], a[28:31], v[248:251]\n"
    "v_mfma_f32_16x16x32_bf16 v[252:255], a[56:59], a[28:31], v[252:255]\n"
    "ds_read_b128 v[64:67], v30 offset:384\n"
    "s_waitcnt lgkmcnt(0)\n"
    "s_nop 7\n"
    :
      "=&{v[32:63]}"(ce), "=&{v[64:79]}"(cm),
      "=&{v[144:159]}"(c[7]), "=&{v[160:175]}"(c[8]), "=&{v[176:191]}"(c[9]), "=&{v[192:207]}"(c[10]), "=&{v[208:223]}"(c[11]), "=&{v[224:239]}"(c[12]), "=&{v[240:255]}"(c[13]),
      "+{v[2:17]}"(ia), "+{v[18:25]}"(ib), "+{v[26:29]}"(ic), "+{v[30:31]}"(id), "+{s[40:41]}"(pA), "+{s[42:43]}"(pB), "+{s[48:49]}"(pS), "+{s[74:75]}"(pE)
    : "{s44}"(m0A), "{s45}"(m0B), "{s47}"(lb + L_SCR), "{s[50:51]}"(pC), "{s[72:73]}"(A.wd), "{s[76:77]}"(pR), "{s[78:79]}"(ws)
    : "memory", "scc", "v80", "v81", "v82", "v83", "v84", "v85", "v86", "v87", "v88", "v89", "v90", "v91", "v92", "v93", "v94", "v95", "v96", "v97", "v98", "v99", "v100", "v101", "v102", "v103", "v104", "v105", "v106", "v107", "v108", "v109", "v110", "v111", "v112", "v113", "v114", "v115", "v116", "v117", "v118", "v119", "v120", "v121", "v122", "v123", "v124", "v125", "v126", "v127", "v128", "v129", "v130", "v131", "v132", "v133", "v134", "v135", "v136", "v137", "v138", "v139", "v140", "v141", "v142", "v143", "a0", "a1", "a2", "a3", "a4", "a5", "a6", "a7", "a8", "a9", "a10", "a11", "a12", "a13", "a14", "a15", "a16", "a17", "a18", "a19", "a20", "a21", "a22", "a23", "a24", "a25", "a26", "a27", "a28", "a29", "a30", "a31", "a32", "a33", "a34", "a35", "a36", "a37", "a38", "a39", "a40", "a41", "a42", "a43", "a44", "a45", "a46", "a47", "a48", "a49", "a50", "a51", "a52", "a53", "a54", "a55", "a56", "a57", "a58", "a59", "a60", "a61", "a62", "a63", "a64", "a65", "a66", "a67", "a68", "a69", "a70", "a71", "a72", "a73", "a74", "a75", "a76", "a77", "a78", "a79", "a80", "a81", "a82", "a83", "a84", "a85", "a86", "a87", "a88", "a89", "a90", "a91", "a92", "a93", "a94", "a95", "a96", "a97", "a98", "a99", "a100", "a101", "a102", "a103", "a104", "a105", "a106", "a107", "a108", "a109", "a110", "a111", "a112", "a113", "a114", "a115", "a116", "a117", "a118", "a119", "a120", "a121", "a122", "a123", "a124", "a125", "a126", "a127", "a128", "a129", "a130", "a131", "a132", "a133", "a134", "a135", "a136", "a137", "a138", "a139", "a140", "a141", "a142", "a143", "a144", "a145", "a146", "a147", "a148", "a149", "a150", "a151", "a152", "a153", "a154", "a155", "a156", "a157", "a158", "a159", "a160", "a161", "a162", "a163", "a164", "a165", "a166", "a167", "a168", "a169", "a170", "a171", "a172", "a173", "a174", "a175", "a176", "a177", "a178", "a179", "a180", "a181", "a182", "a183", "a184", "a185", "a186", "a187", "a188", "a189", "a190", "a191", "a192", "a193", "a194", "a195", "a196", "a197", "a198", "a199", "a200", "a201", "a202", "a203", "s46", "s70", "s71");
  }
  {
  LANE_IDS
  const rsrc_t rw = make_rsrc(ws, WS_END);
  auto io = [&](int i) { return __float_as_uint(i < 24 ? ce[i] : cm[i - 24]); };
  if (layer == 0 && pm == 0 && wm == 0 && r == 0) {
#pragma unroll
    for (int n = 0; n < 7; ++n)
      st16(rw, WS_CS + 16 * 3 * D * 4 + pn * 448 + (n < 6 ? (n & 1) * F + wn * 64 + (n >> 1) * 16 + 4 * g : (g >> 1) * F + 48 + 8 * wn + 4 * (g & 1)) * 4,
           (u32x4){io(4 * n), io(4 * n + 1), io(4 * n + 2), io(4 * n + 3)});
  }
  float mean[8], cs[28];
#pragma unroll
  for (int i = 0; i < 28; ++i) cs[i] = __uint_as_float(io(i));
#pragma unroll
  for (int m = 4; m < 8; ++m) mean[m] = ce[24 + m];
  const unsigned hg[3] = {ic[0], ic[1], ic[2]}, hmix = ic[3];
  auto acc = [&](int m, int i) { return c[(28 * m + i) >> 4][(28 * m + i) & 15]; };
  f32x4 xprev;
#pragma unroll
  for (int m = 4; m < 8; ++m) {
#pragma unroll
    for (int i = 0; i < 3; ++i) {
      float v[4];
#pragma unroll
      for (int j = 0; j < 4; ++j) v[j] = (acc(m, 8 * i + j) - mean[m] * cs[8 * i + j]) * (acc(m, 8 * i + 4 + j) - mean[m] * cs[8 * i + 4 + j]);
      st8(rw, hg[i] + m * 1024, (u32x2){pk_bf16(v[0], v[1]), pk_bf16(v[2], v[3])});
    }
    f32x4 x;
#pragma unroll
    for (int j = 0; j < 4; ++j) x[j] = acc(m, 24 + j) - mean[m] * cs[24 + j];
    if (m & 1) {
      float v[4];
#pragma unroll
      for (int j = 0; j < 4; ++j) {
        auto s = __builtin_amdgcn_permlane32_swap(__float_as_uint(xprev[j]), __float_as_uint(x[j]), false, false);
        v[j] = __uint_as_float(s[0]) * __uint_as_float(s[1]);
      }
      st8(rw, hmix + (m - 1) * 1024, (u32x2){pk_bf16(v[0], v[1]), pk_bf16(v[2], v[3])});
    }
    xprev = x;
  }
  PUBLISH(stage_down, NB_4, if (tid < 5) set_word(ws, tid == 4 ? flag(layer, FL_G + bx) : flag(layer, FL_H + (pm * 2 + (pn >> 4)) * 4 + tid, pn & 15)))
  }
}

DI void phase_down(const Args& A, LAS char* lds, int layer) {
  int w = A.w; unsigned bx = blockIdx.x; char* ws = A.ws;
  asm volatile("" : "+s"(w), "+s"(bx), "+s"(ws));
  LANE_IDS
  const unsigned lb = (unsigned)(size_t)lds;
  const int xcd = bx & 7, bi = bx >> 3, ks = xcd & 1, pm = DOWN_PM, pn = DOWN_PN;
  const int wm = w >> 1, wn = w & 1, rt0 = 4 * wm + 2 * ks;
  u16* out = layer ? A.out : nullptr;
  const char* pA = ws + WS_HT + layer * HB + (pm * 112 + ks * 56) * 8192;
  const char* pB = (const char*)A.wd + (size_t)ks * 1792 * 2048 + pn * 256 - 1024 + 8 * 65536;
  const char* pX = ws + WS_XT + (2 * layer + 1) * XB + (pm * 32 + 4 * pn + 2 * wn) * 8192;
  const char* pR = ws + WS_R2 + (layer * 2 + ks) * S * 4 + pm * 512;
  const unsigned voffA = lane * 16 + w * 2048, voffX = lane * 16 + rt0 * 1024;
  const unsigned voffB = down_voffB(lane, w);
  const unsigned dsA = lb + lane * 8;
  const unsigned sw = 2 * (g & 1) + (q >> 1), rowo = lb + 8192 + (g >> 1) * 2048 + wn * 1024 + (4 * (g & 1) + q) * 128 + p * 8;
  const unsigned b0 = rowo + (0 ^ sw) * 32, b1 = rowo + (1 ^ sw) * 32, b2 = rowo + (2 ^ sw) * 32, b3 = rowo + (3 ^ sw) * 32;
  f32x32 keep, ex;
  await(w, ws, lane >= 16 ? flag(layer, FL_G + (bx ^ 1)) : flag(layer, FL_H + ((pm >> 1) * 2 + ks) * 4 + (pn & 3), lane));
  asm volatile(
    "s_nop 4\n"
    "s_add_u32 m0, s44, 0\n"
    "s_nop 0\n"
    "global_load_lds_dwordx4 v32, s[40:41] offset:0 sc1\n"
    "global_load_lds_dwordx4 v32, s[40:41] offset:1024 sc1\n"
    "s_add_u32 s40, s40, 8192\n"
    "s_addc_u32 s41, s41, 0\n"
    "s_add_u32 m0, s44, 16384\n"
    "s_nop 0\n"
    "global_load_lds_dwordx4 v32, s[40:41] offset:0 sc1\n"
    "global_load_lds_dwordx4 v32, s[40:41] offset:1024 sc1\n"
    "s_add_u32 s40, s40, 8192\n"
    "s_addc_u32 s41, s41, 0\n"
    "s_add_u32 m0, s44, 32768\n"
    "s_nop 0\n"
    "global_load_lds_dwordx4 v32, s[40:41] offset:0 sc1\n"
    "global_load_lds_dwordx4 v32, s[40:41] offset:1024 sc1\n"
    "s_add_u32 s40, s40, 8192\n"
    "s_addc_u32 s41, s41, 0\n"
    "s_waitcnt vmcnt(4)\n"
    "s_barrier\n"
    "ds_read_b64_tr_b16 v[144:145], v38 offset:0\n"
    "ds_read_b64_tr_b16 v[146:147], v38 offset:4096\n"
    "ds_read2st64_b64 v[128:131], v36 offset0:0 offset1:1\n"
    "ds_read_b64_tr_b16 v[148:149], v39 offset:0\n"
    "ds_read_b64_tr_b16 v[150:151], v39 offset:4096\n"
    "ds_read2st64_b64 v[132:135], v36 offset0:2 offset1:3\n"
    "ds_read_b64_tr_b16 v[152:153], v40 offset:0\n"
    "ds_read_b64_tr_b16 v[154:155], v40 offset:4096\n"
    "ds_read2st64_b64 v[136:139], v37 offset0:0 offset1:1\n"
    "ds_read_b64_tr_b16 v[156:157], v41 offset:0\n"
    "ds_read_b64_tr_b16 v[158:159], v41 offset:4096\n"
    "ds_read2st64_b64 v[140:143], v37 offset0:2 offset1:3\n"
    "s_add_u32 m0, s44, 49152\n"
    "s_nop 0\n"
    "global_load_lds_dwordx4 v32, s[40:41] offset:0 sc1\n"
    "global_load_lds_dwordx4 v32, s[40:41] offset:1024 sc1\n"
    "s_add_u32 s40, s40, 8192\n"
    "s_addc_u32 s41, s41, 0\n"
    "s_waitcnt vmcnt(4) lgkmcnt(0)\n"
    "s_barrier\n"
    "v_mfma_f32_16x16x32_bf16 v[192:195], v[144:147], v[128:131], 0\n"
    "s_add_u32 m0, s44, 65536\n"
    "ds_read_b64_tr_b16 v[176:177], v38 offset:16384\n"
    "ds_read_b64_tr_b16 v[178:179], v38 offset:20480\n"
    "v_mfma_f32_16x16x32_bf16 v[196:199], v[148:151], v[128:131], 0\n"
    "global_load_lds_dwordx4 v32, s[40:41] offset:0 sc1\n"
    "ds_read2st64_b64 v[160:163], v36 offset0:32 offset1:33\n"
    "v_mfma_f32_16x16x32_bf16 v[200:203], v[152:155], v[128:131], 0\n"
    "global_load_lds_dwordx4 v32, s[40:41] offset:1024 sc1\n"
    "s_add_u32 s40, s40, 8192\n"
    "s_addc_u32 s41, s41, 0\n"
    "s_add_u32 m0, s44, 81920\n"
    "ds_read_b64_tr_b16 v[180:181], v39 offset:16384\n"
    "v_mfma_f32_16x16x32_bf16 v[204:207], v[156:159], v[128:131], 0\n"
    "global_load_lds_dwordx4 v32, s[40:41] offset:0 sc1\n"
    "ds_read_b64_tr_b16 v[182:183], v39 offset:20480\n"
    "v_mfma_f32_16x16x32_bf16 v[208:211], v[144:147], v[132:135], 0\n"
    "ds_read2st64_b64 v[164:167], v36 offset0:34 offset1:35\n"
    "v_mfma_f32_16x16x32_bf16 v[212:215], v[148:151], v[132:135], 0\n"
    "global_load_lds_dwordx4 v32, s[40:41] offset:1024 sc1\n"
    "s_add_u32 s40, s40, 8192\n"
    "s_addc_u32 s41, s41, 0\n"
    "s_add_u32 m0, s44, 98304\n"
    "ds_read_b64_tr_b16 v[184:185], v40 offset:16384\n"
    "ds_read_b64_tr_b16 v[186:187], v40 offset:20480\n"
    "v_mfma_f32_16x16x32_bf16 v[216:219], v[152:155], v[132:135], 0\n"
    "global_load_lds_dwordx4 v32, s[40:41] offset:0 sc1\n"
    "ds_read2st64_b64 v[168:171], v37 offset0:32 offset1:33\n"
    "v_mfma_f32_16x16x32_bf16 v[220:223], v[156:159], v[132:135], 0\n"
    "global_load_lds_dwordx4 v32, s[40:41] offset:1024 sc1\n"
    "s_add_u32 s40, s40, 8192\n"
    "s_addc_u32 s41, s41, 0\n"
    "ds_read_b64_tr_b16 v[188:189], v41 offset:16384\n"
    "v_mfma_f32_16x16x32_bf16 v[224:227], v[144:147], v[136:139], 0\n"
    "s_add_u32 m0, s44, 114688\n"
    "ds_read_b64_tr_b16 v[190:191], v41 offset:20480\n"
    "v_mfma_f32_16x16x32_bf16 v[228:231], v[148:151], v[136:139], 0\n"
    "global_load_lds_dwordx4 v32, s[40:41] offset:0 sc1\n"
    "ds_read2st64_b64 v[172:175], v37 offset0:34 offset1:35\n"
    "v_mfma_f32_16x16x32_bf16 v[232:235], v[152:155], v[136:139], 0\n"
    "global_load_lds_dwordx4 v32, s[40:41] offset:1024 sc1\n"
    "s_add_u32 s40, s40, 8192\n"
    "s_addc_u32 s41, s41, 0\n"
    "v_mfma_f32_16x16x32_bf16 v[236:239], v[156:159], v[136:139], 0\n"
    "v_mfma_f32_16x16x32_bf16 v[240:243], v[144:147], v[140:143], 0\n"
    "v_mfma_f32_16x16x32_bf16 v[244:247], v[148:151], v[140:143], 0\n"
    "v_mfma_f32_16x16x32_bf16 v[248:251], v[152:155], v[140:143], 0\n"
    "s_waitcnt vmcnt(10) lgkmcnt(0)\n"
    "s_barrier\n"
    "v_mfma_f32_16x16x32_bf16 v[252:255], v[156:159], v[140:143], 0\n"
    "v_mfma_f32_16x16x32_bf16 v[192:195], v[176:179], v[160:163], v[192:195]\n"
    "s_add_u32 m0, s44, 0\n"
    "ds_read_b64_tr_b16 v[144:145], v38 offset:32768\n"
    "ds_read_b64_tr_b16 v[146:147], v38 offset:36864\n"
    "v_mfma_f32_16x16x32_bf16 v[196:199], v[180:183], v[160:163], v[196:199]\n"
    "global_load_lds_dwordx4 v32, s[40:41] offset:0 sc1\n"
    "ds_read2st64_b64 v[128:131], v36 offset0:64 offset1:65\n"
    "v_mfma_f32_16x16x32_bf16 v[200:203], v[184:187], v[160:163], v[200:203]\n"
    "global_load_lds_dwordx4 v32, s[40:41] offset:1024 sc1\n"
    "s_add_u32 s40, s40, 8192\n"
    "s_addc_u32 s41, s41, 0\n"
    "s_add_u32 m0, s44, 8192\n"
    "ds_read_b64_tr_b16 v[148:149], v39 offset:32768\n"
    "v_mfma_f32_16x16x32_bf16 v[204:207], v[188:191], v[160:163], v[204:207]\n"
    "global_load_lds_dwordx4 v34, s[42:43] offset:0\n"
    "ds_read_b64_tr_b16 v[150:151], v39 offset:36864\n"
    "v_mfma_f32_16x16x32_bf16 v[208:211], v[176:179], v[164:167], v[208:211]\n"
    "ds_read2st64_b64 v[132:135], v36 offset0:66 offset1:67\n"
    "v_mfma_f32_16x16x32_bf16 v[212:215], v[180:183], v[164:167], v[212:215]\n"
    "global_load_lds_dwordx4 v35, s[42:43] offset:1024\n"
    "s_add_u32 s42, s42, 65536\n"
    "s_addc_u32 s43, s43, 0\n"
    "s_add_u32 m0, s44, 16384\n"
    "ds_read_b64_tr_b16 v[152:153], v40 offset:32768\n"
    "ds_read_b64_tr_b16 v[154:155], v40 offset:36864\n"
    "v_mfma_f32_16x16x32_bf16 v[216:219], v[184:187], v[164:167], v[216:219]\n"
    "global_load_lds_dwordx4 v32, s[40:41] offset:0 sc1\n"
    "ds_read2st64_b64 v[136:139], v37 offset0:64 offset1:65\n"
    "v_mfma_f32_16x16x32_bf16 v[220:223], v[188:191], v[164:167], v[220:223]\n"
    "global_load_lds_dwordx4 v32, s[40:41] offset:1024 sc1\n"
    "s_add_u32 s40, s40, 8192\n"
    "s_addc_u32 s41, s41, 0\n"
    "ds_read_b64_tr_b16 v[156:157], v41 offset:32768\n"
    "v_mfma_f32_16x16x32_bf16 v[224:227], v[176:179], v[168:171], v[224:227]\n"
    "s_add_u32 m0, s44, 24576\n"
    "ds_read_b64_tr_b16 v[158:159], v41 offset:36864\n"
    "v_mfma_f32_16x16x32_bf16 v[228:231], v[180:183], v[168:171], v[228:231]\n"
    "global_load_lds_dwordx4 v34, s[42:43] offset:0\n"
    "ds_read2st64_b64 v[140:143], v37 offset0:66 offset1:67\n"
    "v_mfma_f32_16x16x32_bf16 v[232:235], v[184:187], v[168:171], v[232:235]\n"
    "global_load_lds_dwordx4 v35, s[42:43] offset:1024\n"
    "s_add_u32 s42, s42, 65536\n"
    "s_addc_u32 s43, s43, 0\n"
    "v_mfma_f32_16x16x32_bf16 v[236:239], v[188:191], v[168:171], v[236:239]\n"
    "v_mfma_f32_16x16x32_bf16 v[240:243], v[176:179], v[172:175], v[240:243]\n"
    "v_mfma_f32_16x16x32_bf16 v[244:247], v[180:183], v[172:175], v[244:247]\n"
    "v_mfma_f32_16x16x32_bf16 v[248:251], v[184:187], v[172:175], v[248:251]\n"
    "s_waitcnt vmcnt(16) lgkmcnt(0)\n"
    "s_barrier\n"
    "v_mfma_f32_16x16x32_bf16 v[252:255], v[188:191], v[172:175], v[252:255]\n"
    "v_mfma_f32_16x16x32_bf16 v[192:195], v[144:147], v[128:131], v[192:195]\n"
    "s_add_u32 m0, s44, 32768\n"
    "ds_read_b64_tr_b16 v[176:177], v38 offset:49152\n"
    "ds_read_b64_tr_b16 v[178:179], v38 offset:53248\n"
    "v_mfma_f32_16x16x32_bf16 v[196:199], v[148:151], v[128:131], v[196:199]\n"
    "global_load_lds_dwordx4 v32, s[40:41] offset:0 sc1\n"
    "ds_read2st64_b64 v[160:163], v36 offset0:96 offset1:97\n"
    "v_mfma_f32_16x16x32_bf16 v[200:203], v[152:155], v[128:131], v[200:203]\n"
    "ds_read_b64_tr_b16 v[180:181], v39 offset:49152\n"
    "v_mfma_f32_16x16x32_bf16 v[204:207], v[156:159], v[128:131], v[204:207]\n"
    "global_load_lds_dwordx4 v32, s[40:41] offset:1024 sc1\n"
    "s_add_u32 s40, s40, 8192\n"
    "s_addc_u32 s41, s41, 0\n"
    "ds_read_b64_tr_b16 v[182:183], v39 offset:53248\n"
    "v_mfma_f32_16x16x32_bf16 v[208:211], v[144:147], v[132:135], v[208:211]\n"
    "ds_read2st64_b64 v[164:167], v36 offset0:98 offset1:99\n"
    "v_mfma_f32_16x16x32_bf16 v[212:215], v[148:151], v[132:135], v[212:215]\n"
    "s_add_u32 m0, s44, 40960\n"
    "ds_read_b64_tr_b16 v[184:185], v40 offset:49152\n"
    "ds_read_b64_tr_b16 v[186:187], v40 offset:53248\n"
    "v_mfma_f32_16x16x32_bf16 v[216:219], v[152:155], v[132:135], v[216:219]\n"
    "global_load_lds_dwordx4 v34, s[42:43] offset:0\n"
    "ds_read2st64_b64 v[168:171], v37 offset0:96 offset1:97\n"
    "v_mfma_f32_16x16x32_bf16 v[220:223], v[156:159], v[132:135], v[220:223]\n"
    "ds_read_b64_tr_b16 v[188:189], v41 offset:49152\n"
    "v_mfma_f32_16x16x32_bf16 v[224:227], v[144:147], v[136:139], v[224:227]\n"
    "ds_read_b64_tr_b16 v[190:191], v41 offset:53248\n"
    "v_mfma_f32_16x16x32_bf16 v[228:231], v[148:151], v[136:139], v[228:231]\n"
    "global_load_lds_dwordx4 v35, s[42:43] offset:1024\n"
    "s_add_u32 s42, s42, 65536\n"
    "s_addc_u32 s43, s43, 0\n"
    "ds_read2st64_b64 v[172:175], v37 offset0:98 offset1:99\n"
    "v_mfma_f32_16x16x32_bf16 v[232:235], v[152:155], v[136:139], v[232:235]\n"
    "v_mfma_f32_16x16x32_bf16 v[236:239], v[156:159], v[136:139], v[236:239]\n"
    "v_mfma_f32_16x16x32_bf16 v[240:243], v[144:147], v[140:143], v[240:243]\n"
    "v_mfma_f32_16x16x32_bf16 v[244:247], v[148:151], v[140:143], v[244:247]\n"
    "v_mfma_f32_16x16x32_bf16 v[248:251], v[152:155], v[140:143], v[248:251]\n"
    "s_waitcnt vmcnt(18) lgkmcnt(0)\n"
    "s_barrier\n"
    "v_mfma_f32_16x16x32_bf16 v[252:255], v[156:159], v[140:143], v[252:255]\n"
    "v_mfma_f32_16x16x32_bf16 v[192:195], v[176:179], v[160:163], v[192:195]\n"
    "s_add_u32 m0, s44, 49152\n"
    "ds_read_b64_tr_b16 v[144:145], v42 offset:0\n"
    "ds_read_b64_tr_b16 v[146:147], v42 offset:4096\n"
    "v_mfma_f32_16x16x32_bf16 v[196:199], v[180:183], v[160:163], v[196:199]\n"
    "global_load_lds_dwordx4 v32, s[40:41] offset:0 sc1\n"
    "ds_read2st64_b64 v[128:131], v36 offset0:128 offset1:129\n"
    "v_mfma_f32_16x16x32_bf16 v[200:203], v[184:187], v[160:163], v[200:203]\n"
    "ds_read_b64_tr_b16 v[148:149], v43 offset:0\n"
    "v_mfma_f32_16x16x32_bf16 v[204:207], v[188:191], v[160:163], v[204:207]\n"
    "global_load_lds_dwordx4 v32, s[40:41] offset:1024 sc1\n"
    "s_add_u32 s40, s40, 8192\n"
    "s_addc_u32 s41, s41, 0\n"
    "ds_read_b64_tr_b16 v[150:151], v43 offset:4096\n"
    "v_mfma_f32_16x16x32_bf16 v[208:211], v[176:179], v[164:167], v[208:211]\n"
    "ds_read2st64_b64 v[132:135], v36 offset0:130 offset1:131\n"
    "v_mfma_f32_16x16x32_bf16 v[212:215], v[180:183], v[164:167], v[212:215]\n"
    "s_add_u32 m0, s44, 57344\n"
    "ds_read_b64_tr_b16 v[152:153], v44 offset:0\n"
    "ds_read_b64_tr_b16 v[154:155], v44 offset:4096\n"
    "v_mfma_f32_16x16x32_bf16 v[216:219], v[184:187], v[164:167], v[216:219]\n"
    "global_load_lds_dwordx4 v34, s[42:43] offset:0\n"
    "ds_read2st64_b64 v[136:139], v37 offset0:128 offset1:129\n"
    "v_mfma_f32_16x16x32_bf16 v[220:223], v[188:191], v[164:167], v[220:223]\n"
    "ds_read_b64_tr_b16 v[156:157], v45 offset:0\n"
    "v_mfma_f32_16x16x32_bf16 v[224:227], v[176:179], v[168:171], v[224:227]\n"
    "ds_read_b64_tr_b16 v[158:159], v45 offset:4096\n"
    "v_mfma_f32_16x16x32_bf16 v[228:231], v[180:183], v[168:171], v[228:231]\n"
    "global_load_lds_dwordx4 v35, s[42:43] offset:1024\n"
    "s_add_u32 s42, s42, 65536\n"
    "s_addc_u32 s43, s43, 0\n"
    "ds_read2st64_b64 v[140:143], v37 offset0:130 offset1:131\n"
    "v_mfma_f32_16x16x32_bf16 v[232:235], v[184:187], v[168:171], v[232:235]\n"
    "v_mfma_f32_16x16x32_bf16 v[236:239], v[188:191], v[168:171], v[236:239]\n"
    "v_mfma_f32_16x16x32_bf16 v[240:243], v[176:179], v[172:175], v[240:243]\n"
    "v_mfma_f32_16x16x32_bf16 v[244:247], v[180:183], v[172:175], v[244:247]\n"
    "v_mfma_f32_16x16x32_bf16 v[248:251], v[184:187], v[172:175], v[248:251]\n"
    "s_waitcnt vmcnt(20) lgkmcnt(0)\n"
    "s_barrier\n"
    "v_mfma_f32_16x16x32_bf16 v[252:255], v[188:191], v[172:175], v[252:255]\n"
    "v_mfma_f32_16x16x32_bf16 v[192:195], v[144:147], v[128:131], v[192:195]\n"
    "s_add_u32 m0, s44, 65536\n"
    "ds_read_b64_tr_b16 v[176:177], v42 offset:16384\n"
    "ds_read_b64_tr_b16 v[178:179], v42 offset:20480\n"
    "v_mfma_f32_16x16x32_bf16 v[196:199], v[148:151], v[128:131], v[196:199]\n"
    "global_load_lds_dwordx4 v32, s[40:41] offset:0 sc1\n"
    "ds_read2st64_b64 v[160:163], v36 offset0:160 offset1:161\n"
    "v_mfma_f32_16x16x32_bf16 v[200:203], v[152:155], v[128:131], v[200:203]\n"
    "ds_read_b64_tr_b16 v[180:181], v43 offset:16384\n"
    "v_mfma_f32_16x16x32_bf16 v[204:207], v[156:159], v[128:131], v[204:207]\n"
    "global_load_lds_dwordx4 v32, s[40:41] offset:1024 sc1\n"
    "s_add_u32 s40, s40, 8192\n"
    "s_addc_u32 s41, s41, 0\n"
    "ds_read_b64_tr_b16 v[182:183], v43 offset:20480\n"
    "v_mfma_f32_16x16x32_bf16 v[208:211], v[144:147], v[132:135], v[208:211]\n"
    "ds_read2st64_b64 v[164:167], v36 offset0:162 offset1:163\n"
    "v_mfma_f32_16x16x32_bf16 v[212:215], v[148:151], v[132:135], v[212:215]\n"
    "s_add_u32 m0, s44, 73728\n"
    "ds_read_b64_tr_b16 v[184:185], v44 offset:16384\n"
    "ds_read_b64_tr_b16 v[186:187], v44 offset:20480\n"
    "v_mfma_f32_16x16x32_bf16 v[216:219], v[152:155], v[132:135], v[216:219]\n"
    "global_load_lds_dwordx4 v34, s[42:43] offset:0\n"
    "ds_read2st64_b64 v[168:171], v37 offset0:160 offset1:161\n"
    "v_mfma_f32_16x16x32_bf16 v[220:223], v[156:159], v[132:135], v[220:223]\n"
    "ds_read_b64_tr_b16 v[188:189], v45 offset:16384\n"
    "v_mfma_f32_16x16x32_bf16 v[224:227], v[144:147], v[136:139], v[224:227]\n"
    "ds_read_b64_tr_b16 v[190:191], v45 offset:20480\n"
    "v_mfma_f32_16x16x32_bf16 v[228:231], v[148:151], v[136:139], v[228:231]\n"
    "global_load_lds_dwordx4 v35, s[42:43] offset:1024\n"
    "s_add_u32 s42, s42, 65536\n"
    "s_addc_u32 s43, s43, 0\n"
    "ds_read2st64_b64 v[172:175], v37 offset0:162 offset1:163\n"
    "v_mfma_f32_16x16x32_bf16 v[232:235], v[152:155], v[136:139], v[232:235]\n"
    "v_mfma_f32_16x16x32_bf16 v[236:239], v[156:159], v[136:139], v[236:239]\n"
    "v_mfma_f32_16x16x32_bf16 v[240:243], v[144:147], v[140:143], v[240:243]\n"
    "v_mfma_f32_16x16x32_bf16 v[244:247], v[148:151], v[140:143], v[244:247]\n"
    "v_mfma_f32_16x16x32_bf16 v[248:251], v[152:155], v[140:143], v[248:251]\n"
    "s_waitcnt vmcnt(22) lgkmcnt(0)\n"
    "s_barrier\n"
    "v_mfma_f32_16x16x32_bf16 v[252:255], v[156:159], v[140:143], v[252:255]\n"
    "v_mfma_f32_16x16x32_bf16 v[192:195], v[176:179], v[160:163], v[192:195]\n"
    "s_add_u32 m0, s44, 81920\n"
    "ds_read_b64_tr_b16 v[144:145], v42 offset:32768\n"
    "ds_read_b64_tr_b16 v[146:147], v42 offset:36864\n"
    "v_mfma_f32_16x16x32_bf16 v[196:199], v[180:183], v[160:163], v[196:199]\n"
    "global_load_lds_dwordx4 v32, s[40:41] offset:0 sc1\n"
    "ds_read2st64_b64 v[128:131], v36 offset0:192 offset1:193\n"
    "v_mfma_f32_16x16x32_bf16 v[200:203], v[184:187], v[160:163], v[200:203]\n"
    "ds_read_b64_tr_b16 v[148:149], v43 offset:32768\n"
    "v_mfma_f32_16x16x32_bf16 v[204:207], v[188:191], v[160:163], v[204:207]\n"
    "global_load_lds_dwordx4 v32, s[40:41] offset:1024 sc1\n"
    "s_add_u32 s40, s40, 8192\n"
    "s_addc_u32 s41, s41, 0\n"
    "ds_read_b64_tr_b16 v[150:151], v43 offset:36864\n"
    "v_mfma_f32_16x16x32_bf16 v[208:211], v[176:179], v[164:167], v[208:211]\n"
    "ds_read2st64_b64 v[132:135], v36 offset0:194 offset1:195\n"
    "v_mfma_f32_16x16x32_bf16 v[212:215], v[180:183], v[164:167], v[212:215]\n"
    "s_add_u32 m0, s44, 90112\n"
    "ds_read_b64_tr_b16 v[152:153], v44 offset:32768\n"
    "ds_read_b64_tr_b16 v[154:155], v44 offset:36864\n"
    "v_mfma_f32_16x16x32_bf16 v[216:219], v[184:187], v[164:167], v[216:219]\n"
    "global_load_lds_dwordx4 v34, s[42:43] offset:0\n"
    "ds_read2st64_b64 v[136:139], v37 offset0:192 offset1:193\n"
    "v_mfma_f32_16x16x32_bf16 v[220:223], v[188:191], v[164:167], v[220:223]\n"
    "ds_read_b64_tr_b16 v[156:157], v45 offset:32768\n"
    "v_mfma_f32_16x16x32_bf16 v[224:227], v[176:179], v[168:171], v[224:227]\n"
    "ds_read_b64_tr_b16 v[158:159], v45 offset:36864\n"
    "v_mfma_f32_16x16x32_bf16 v[228:231], v[180:183], v[168:171], v[228:231]\n"
    "global_load_lds_dwordx4 v35, s[42:43] offset:1024\n"
    "s_add_u32 s42, s42, 65536\n"
    "s_addc_u32 s43, s43, 0\n"
    "ds_read2st64_b64 v[140:143], v37 offset0:194 offset1:195\n"
    "v_mfma_f32_16x16x32_bf16 v[232:235], v[184:187], v[168:171], v[232:235]\n"
    "v_mfma_f32_16x16x32_bf16 v[236:239], v[188:191], v[168:171], v[236:239]\n"
    "v_mfma_f32_16x16x32_bf16 v[240:243], v[176:179], v[172:175], v[240:243]\n"
    "v_mfma_f32_16x16x32_bf16 v[244:247], v[180:183], v[172:175], v[244:247]\n"
    "v_mfma_f32_16x16x32_bf16 v[248:251], v[184:187], v[172:175], v[248:251]\n"
    "s_waitcnt vmcnt(24) lgkmcnt(0)\n"
    "s_barrier\n"
    "v_mfma_f32_16x16x32_bf16 v[252:255], v[188:191], v[172:175], v[252:255]\n"
    "s_mov_b32 s46, 5\n"
    "1:\n"
    "v_mfma_f32_16x16x32_bf16 v[192:195], v[144:147], v[128:131], v[192:195]\n"
    "s_add_u32 m0, s44, 98304\n"
    "ds_read_b64_tr_b16 v[176:177], v42 offset:49152\n"
    "ds_read_b64_tr_b16 v[178:179], v42 offset:53248\n"
    "v_mfma_f32_16x16x32_bf16 v[196:199], v[148:151], v[128:131], v[196:199]\n"
    "global_load_lds_dwordx4 v32, s[40:41] offset:0 sc1\n"
    "ds_read2st64_b64 v[160:163], v36 offset0:224 offset1:225\n"
    "v_mfma_f32_16x16x32_bf16 v[200:203], v[152:155], v[128:131], v[200:203]\n"
    "ds_read_b64_tr_b16 v[180:181], v43 offset:49152\n"
    "v_mfma_f32_16x16x32_bf16 v[204:207], v[156:159], v[128:131], v[204:207]\n"
    "global_load_lds_dwordx4 v32, s[40:41] offset:1024 sc1\n"
    "s_add_u32 s40, s40, 8192\n"
    "s_addc_u32 s41, s41, 0\n"
    "ds_read_b64_tr_b16 v[182:183], v43 offset:53248\n"
    "v_mfma_f32_16x16x32_bf16 v[208:211], v[144:147], v[132:135], v[208:211]\n"
    "ds_read2st64_b64 v[164:167], v36 offset0:226 offset1:227\n"
    "v_mfma_f32_16x16x32_bf16 v[212:215], v[148:151], v[132:135], v[212:215]\n"
    "s_add_u32 m0, s44, 106496\n"
    "ds_read_b64_tr_b16 v[184:185], v44 offset:49152\n"
    "ds_read_b64_tr_b16 v[186:187], v44 offset:53248\n"
    "v_mfma_f32_16x16x32_bf16 v[216:219], v[152:155], v[132:135], v[216:219]\n"
    "global_load_lds_dwordx4 v34, s[42:43] offset:0\n"
    "ds_read2st64_b64 v[168:171], v37 offset0:224 offset1:225\n"
    "v_mfma_f32_16x16x32_bf16 v[220:223], v[156:159], v[132:135], v[220:223]\n"
    "ds_read_b64_tr_b16 v[188:189], v45 offset:49152\n"
    "v_mfma_f32_16x16x32_bf16 v[224:227], v[144:147], v[136:139], v[224:227]\n"
    "ds_read_b64_tr_b16 v[190:191], v45 offset:53248\n"
    "v_mfma_f32_16x16x32_bf16 v[228:231], v[148:151], v[136:139], v[228:231]\n"
    "global_load_lds_dwordx4 v35, s[42:43] offset:1024\n"
    "s_add_u32 s42, s42, 65536\n"
    "s_addc_u32 s43, s43, 0\n"
    "ds_read2st64_b64 v[172:175], v37 offset0:226 offset1:227\n"
    "v_mfma_f32_16x16x32_bf16 v[232:235], v[152:155], v[136:139], v[232:235]\n"
    "v_mfma_f32_16x16x32_bf16 v[236:239], v[156:159], v[136:139], v[236:239]\n"
    "v_mfma_f32_16x16x32_bf16 v[240:243], v[144:147], v[140:143], v[240:243]\n"
    "v_mfma_f32_16x16x32_bf16 v[244:247], v[148:151], v[140:143], v[244:247]\n"
    "v_mfma_f32_16x16x32_bf16 v[248:251], v[152:155], v[140:143], v[248:251]\n"
    "s_waitcnt vmcnt(24) lgkmcnt(0)\n"
    "s_barrier\n"
    "v_mfma_f32_16x16x32_bf16 v[252:255], v[156:159], v[140:143], v[252:255]\n"
    "v_mfma_f32_16x16x32_bf16 v[192:195], v[176:179], v[160:163], v[192:195]\n"
    "s_add_u32 m0, s44, 114688\n"
    "ds_read_b64_tr_b16 v[144:145], v38 offset:0\n"
    "ds_read_b64_tr_b16 v[146:147], v38 offset:4096\n"
    "v_mfma_f32_16x16x32_bf16 v[196:199], v[180:183], v[160:163], v[196:199]\n"
    "global_load_lds_dwordx4 v32, s[40:41] offset:0 sc1\n"
    "ds_read2st64_b64 v[128:131], v36 offset0:0 offset1:1\n"
    "v_mfma_f32_16x16x32_bf16 v[200:203], v[184:187], v[160:163], v[200:203]\n"
    "ds_read_b64_tr_b16 v[148:149], v39 offset:0\n"
    "v_mfma_f32_16x16x32_bf16 v[204:207], v[188:191], v[160:163], v[204:207]\n"
    "global_load_lds_dwordx4 v32, s[40:41] offset:1024 sc1\n"
    "s_add_u32 s40, s40, 8192\n"
    "s_addc_u32 s41, s41, 0\n"
    "ds_read_b64_tr_b16 v[150:151], v39 offset:4096\n"
    "v_mfma_f32_16x16x32_bf16 v[208:211], v[176:179], v[164:167], v[208:211]\n"
    "ds_read2st64_b64 v[132:135], v36 offset0:2 offset1:3\n"
    "v_mfma_f32_16x16x32_bf16 v[212:215], v[180:183], v[164:167], v[212:215]\n"
    "s_add_u32 m0, s44, 122880\n"
    "ds_read_b64_tr_b16 v[152:153], v40 offset:0\n"
    "ds_read_b64_tr_b16 v[154:155], v40 offset:4096\n"
    "v_mfma_f32_16x16x32_bf16 v[216:219], v[184:187], v[164:167], v[216:219]\n"
    "global_load_lds_dwordx4 v34, s[42:43] offset:0\n"
    "ds_read2st64_b64 v[136:139], v37 offset0:0 offset1:1\n"
    "v_mfma_f32_16x16x32_bf16 v[220:223], v[188:191], v[164:167], v[220:223]\n"
    "ds_read_b64_tr_b16 v[156:157], v41 offset:0\n"
    "v_mfma_f32_16x16x32_bf16 v[224:227], v[176:179], v[168:171], v[224:227]\n"
    "ds_read_b64_tr_b16 v[158:159], v41 offset:4096\n"
    "v_mfma_f32_16x16x32_bf16 v[228:231], v[180:183], v[168:171], v[228:231]\n"
    "global_load_lds_dwordx4 v35, s[42:43] offset:1024\n"
    "s_add_u32 s42, s42, 65536\n"
    "s_addc_u32 s43, s43, 0\n"
    "ds_read2st64_b64 v[140:143], v37 offset0:2 offset1:3\n"
    "v_mfma_f32_16x16x32_bf16 v[232:235], v[184:187], v[168:171], v[232:235]\n"
    "v_mfma_f32_16x16x32_bf16 v[236:239], v[188:191], v[168:171], v[236:239]\n"
    "v_mfma_f32_16x16x32_bf16 v[240:243], v[176:179], v[172:175], v[240:243]\n"
    "v_mfma_f32_16x16x32_bf16 v[244:247], v[180:183], v[172:175], v[244:247]\n"
    "v_mfma_f32_16x16x32_bf16 v[248:251], v[184:187], v[172:175], v[248:251]\n"
    "s_waitcnt vmcnt(24) lgkmcnt(0)\n"
    "s_barrier\n"
    "v_mfma_f32_16x16x32_bf16 v[252:255], v[188:191], v[172:175], v[252:255]\n"
    "v_mfma_f32_16x16x32_bf16 v[192:195], v[144:147], v[128:131], v[192:195]\n"
    "s_add_u32 m0, s44, 0\n"
    "ds_read_b64_tr_b16 v[176:177], v38 offset:16384\n"
    "ds_read_b64_tr_b16 v[178:179], v38 offset:20480\n"
    "v_mfma_f32_16x16x32_bf16 v[196:199], v[148:151], v[128:131], v[196:199]\n"
    "global_load_lds_dwordx4 v32, s[40:41] offset:0 sc1\n"
    "ds_read2st64_b64 v[160:163], v36 offset0:32 offset1:33\n"
    "v_mfma_f32_16x16x32_bf16 v[200:203], v[152:155], v[128:131], v[200:203]\n"
    "ds_read_b64_tr_b16 v[180:181], v39 offset:16384\n"
    "v_mfma_f32_16x16x32_bf16 v[204:207], v[156:159], v[128:131], v[204:207]\n"
    "global_load_lds_dwordx4 v32, s[40:41] offset:1024 sc1\n"
    "s_add_u32 s40, s40, 8192\n"
    "s_addc_u32 s41, s41, 0\n"
    "ds_read_b64_tr_b16 v[182:183], v39 offset:20480\n"
    "v_mfma_f32_16x16x32_bf16 v[208:211], v[144:147], v[132:135], v[208:211]\n"
    "ds_read2st64_b64 v[164:167], v36 offset0:34 offset1:35\n"
    "v_mfma_f32_16x16x32_bf16 v[212:215], v[148:151], v[132:135], v[212:215]\n"
    "s_add_u32 m0, s44, 8192\n"
    "ds_read_b64_tr_b16 v[184:185], v40 offset:16384\n"
    "ds_read_b64_tr_b16 v[186:187], v40 offset:20480\n"
    "v_mfma_f32_16x16x32_bf16 v[216:219], v[152:155], v[132:135], v[216:219]\n"
    "global_load_lds_dwordx4 v34, s[42:43] offset:0\n"
    "ds_read2st64_b64 v[168:171], v37 offset0:32 offset1:33\n"
    "v_mfma_f32_16x16x32_bf16 v[220:223], v[156:159], v[132:135], v[220:223]\n"
    "ds_read_b64_tr_b16 v[188:189], v41 offset:16384\n"
    "v_mfma_f32_16x16x32_bf16 v[224:227], v[144:147], v[136:139], v[224:227]\n"
    "ds_read_b64_tr_b16 v[190:191], v41 offset:20480\n"
    "v_mfma_f32_16x16x32_bf16 v[228:231], v[148:151], v[136:139], v[228:231]\n"
    "global_load_lds_dwordx4 v35, s[42:43] offset:1024\n"
    "s_add_u32 s42, s42, 65536\n"
    "s_addc_u32 s43, s43, 0\n"
    "ds_read2st64_b64 v[172:175], v37 offset0:34 offset1:35\n"
    "v_mfma_f32_16x16x32_bf16 v[232:235], v[152:155], v[136:139], v[232:235]\n"
    "v_mfma_f32_16x16x32_bf16 v[236:239], v[156:159], v[136:139], v[236:239]\n"
    "v_mfma_f32_16x16x32_bf16 v[240:243], v[144:147], v[140:143], v[240:243]\n"
    "v_mfma_f32_16x16x32_bf16 v[244:247], v[148:151], v[140:143], v[244:247]\n"
    "v_mfma_f32_16x16x32_bf16 v[248:251], v[152:155], v[140:143], v[248:251]\n"
    "s_waitcnt vmcnt(24) lgkmcnt(0)\n"
    "s_barrier\n"
    "v_mfma_f32_16x16x32_bf16 v[252:255], v[156:159], v[140:143], v[252:255]\n"
    "v_mfma_f32_16x16x32_bf16 v[192:195], v[176:179], v[160:163], v[192:195]\n"
    "s_add_u32 m0, s44, 16384\n"
    "ds_read_b64_tr_b16 v[144:145], v38 offset:32768\n"
    "ds_read_b64_tr_b16 v[146:147], v38 offset:36864\n"
    "v_mfma_f32_16x16x32_bf16 v[196:199], v[180:183], v[160:163], v[196:199]\n"
    "global_load_lds_dwordx4 v32, s[40:41] offset:0 sc1\n"
    "ds_read2st64_b64 v[128:131], v36 offset0:64 offset1:65\n"
    "v_mfma_f32_16x16x32_bf16 v[200:203], v[184:187], v[160:163], v[200:203]\n"
    "ds_read_b64_tr_b16 v[148:149], v39 offset:32768\n"
    "v_mfma_f32_16x16x32_bf16 v[204:207], v[188:191], v[160:163], v[204:207]\n"
    "global_load_lds_dwordx4 v32, s[40:41] offset:1024 sc1\n"
    "s_add_u32 s40, s40, 8192\n"
    "s_addc_u32 s41, s41, 0\n"
    "ds_read_b64_tr_b16 v[150:151], v39 offset:36864\n"
    "v_mfma_f32_16x16x32_bf16 v[208:211], v[176:179], v[164:167], v[208:211]\n"
    "ds_read2st64_b64 v[132:135], v36 offset0:66 offset1:67\n"
    "v_mfma_f32_16x16x32_bf16 v[212:215], v[180:183], v[164:167], v[212:215]\n"
    "s_add_u32 m0, s44, 24576\n"
    "ds_read_b64_tr_b16 v[152:153], v40 offset:32768\n"
    "ds_read_b64_tr_b16 v[154:155], v40 offset:36864\n"
    "v_mfma_f32_16x16x32_bf16 v[216:219], v[184:187], v[164:167], v[216:219]\n"
    "global_load_lds_dwordx4 v34, s[42:43] offset:0\n"
    "ds_read2st64_b64 v[136:139], v37 offset0:64 offset1:65\n"
    "v_mfma_f32_16x16x32_bf16 v[220:223], v[188:191], v[164:167], v[220:223]\n"
    "ds_read_b64_tr_b16 v[156:157], v41 offset:32768\n"
    "v_mfma_f32_16x16x32_bf16 v[224:227], v[176:179], v[168:171], v[224:227]\n"
    "ds_read_b64_tr_b16 v[158:159], v41 offset:36864\n"
    "v_mfma_f32_16x16x32_bf16 v[228:231], v[180:183], v[168:171], v[228:231]\n"
    "global_load_lds_dwordx4 v35, s[42:43] offset:1024\n"
    "s_add_u32 s42, s42, 65536\n"
    "s_addc_u32 s43, s43, 0\n"
    "ds_read2st64_b64 v[140:143], v37 offset0:66 offset1:67\n"
    "v_mfma_f32_16x16x32_bf16 v[232:235], v[184:187], v[168:171], v[232:235]\n"
    "v_mfma_f32_16x16x32_bf16 v[236:239], v[188:191], v[168:171], v[236:239]\n"
    "v_mfma_f32_16x16x32_bf16 v[240:243], v[176:179], v[172:175], v[240:243]\n"
    "v_mfma_f32_16x16x32_bf16 v[244:247], v[180:183], v[172:175], v[244:247]\n"
    "v_mfma_f32_16x16x32_bf16 v[248:251], v[184:187], v[172:175], v[248:251]\n"
    "s_waitcnt vmcnt(24) lgkmcnt(0)\n"
    "s_barrier\n"
    "v_mfma_f32_16x16x32_bf16 v[252:255], v[188:191], v[172:175], v[252:255]\n"
    "v_mfma_f32_16x16x32_bf16 v[192:195], v[144:147], v[128:131], v[192:195]\n"
    "s_add_u32 m0, s44, 32768\n"
    "ds_read_b64_tr_b16 v[176:177], v38 offset:49152\n"
    "ds_read_b64_tr_b16 v[178:179], v38 offset:53248\n"
    "v_mfma_f32_16x16x32_bf16 v[196:199], v[148:151], v[128:131], v[196:199]\n"
    "global_load_lds_dwordx4 v32, s[40:41] offset:0 sc1\n"
    "ds_read2st64_b64 v[160:163], v36 offset0:96 offset1:97\n"
    "v_mfma_f32_16x16x32_bf16 v[200:203], v[152:155], v[128:131], v[200:203]\n"
    "ds_read_b64_tr_b16 v[180:181], v39 offset:49152\n"
    "v_mfma_f32_16x16x32_bf16 v[204:207], v[156:159], v[128:131], v[204:207]\n"
    "global_load_lds_dwordx4 v32, s[40:41] offset:1024 sc1\n"
    "s_add_u32 s40, s40, 8192\n"
    "s_addc_u32 s41, s41, 0\n"
    "ds_read_b64_tr_b16 v[182:183], v39 offset:53248\n"
    "v_mfma_f32_16x16x32_bf16 v[208:211], v[144:147], v[132:135], v[208:211]\n"
    "ds_read2st64_b64 v[164:167], v36 offset0:98 offset1:99\n"
    "v_mfma_f32_16x16x32_bf16 v[212:215], v[148:151], v[132:135], v[212:215]\n"
    "s_add_u32 m0, s44, 40960\n"
    "ds_read_b64_tr_b16 v[184:185], v40 offset:49152\n"
    "ds_read_b64_tr_b16 v[186:187], v40 offset:53248\n"
    "v_mfma_f32_16x16x32_bf16 v[216:219], v[152:155], v[132:135], v[216:219]\n"
    "global_load_lds_dwordx4 v34, s[42:43] offset:0\n"
    "ds_read2st64_b64 v[168:171], v37 offset0:96 offset1:97\n"
    "v_mfma_f32_16x16x32_bf16 v[220:223], v[156:159], v[132:135], v[220:223]\n"
    "ds_read_b64_tr_b16 v[188:189], v41 offset:49152\n"
    "v_mfma_f32_16x16x32_bf16 v[224:227], v[144:147], v[136:139], v[224:227]\n"
    "ds_read_b64_tr_b16 v[190:191], v41 offset:53248\n"
    "v_mfma_f32_16x16x32_bf16 v[228:231], v[148:151], v[136:139], v[228:231]\n"
    "global_load_lds_dwordx4 v35, s[42:43] offset:1024\n"
    "s_add_u32 s42, s42, 65536\n"
    "s_addc_u32 s43, s43, 0\n"
    "ds_read2st64_b64 v[172:175], v37 offset0:98 offset1:99\n"
    "v_mfma_f32_16x16x32_bf16 v[232:235], v[152:155], v[136:139], v[232:235]\n"
    "v_mfma_f32_16x16x32_bf16 v[236:239], v[156:159], v[136:139], v[236:239]\n"
    "v_mfma_f32_16x16x32_bf16 v[240:243], v[144:147], v[140:143], v[240:243]\n"
    "v_mfma_f32_16x16x32_bf16 v[244:247], v[148:151], v[140:143], v[244:247]\n"
    "v_mfma_f32_16x16x32_bf16 v[248:251], v[152:155], v[140:143], v[248:251]\n"
    "s_waitcnt vmcnt(24) lgkmcnt(0)\n"
    "s_barrier\n"
    "v_mfma_f32_16x16x32_bf16 v[252:255], v[156:159], v[140:143], v[252:255]\n"
    "v_mfma_f32_16x16x32_bf16 v[192:195], v[176:179], v[160:163], v[192:195]\n"
    "s_add_u32 m0, s44, 49152\n"
    "ds_read_b64_tr_b16 v[144:145], v42 offset:0\n"
    "ds_read_b64_tr_b16 v[146:147], v42 offset:4096\n"
    "v_mfma_f32_16x16x32_bf16 v[196:199], v[180:183], v[160:163], v[196:199]\n"
    "global_load_lds_dwordx4 v32, s[40:41] offset:0 sc1\n"
    "ds_read2st64_b64 v[128:131], v36 offset0:128 offset1:129\n"
    "v_mfma_f32_16x16x32_bf16 v[200:203], v[184:187], v[160:163], v[200:203]\n"
    "ds_read_b64_tr_b16 v[148:149], v43 offset:0\n"
    "v_mfma_f32_16x16x32_bf16 v[204:207], v[188:191], v[160:163], v[204:207]\n"
    "global_load_lds_dwordx4 v32, s[40:41] offset:1024 sc1\n"
    "s_add_u32 s40, s40, 8192\n"
    "s_addc_u32 s41, s41, 0\n"
    "ds_read_b64_tr_b16 v[150:151], v43 offset:4096\n"
    "v_mfma_f32_16x16x32_bf16 v[208:211], v[176:179], v[164:167], v[208:211]\n"
    "ds_read2st64_b64 v[132:135], v36 offset0:130 offset1:131\n"
    "v_mfma_f32_16x16x32_bf16 v[212:215], v[180:183], v[164:167], v[212:215]\n"
    "s_add_u32 m0, s44, 57344\n"
    "ds_read_b64_tr_b16 v[152:153], v44 offset:0\n"
    "ds_read_b64_tr_b16 v[154:155], v44 offset:4096\n"
    "v_mfma_f32_16x16x32_bf16 v[216:219], v[184:187], v[164:167], v[216:219]\n"
    "global_load_lds_dwordx4 v34, s[42:43] offset:0\n"
    "ds_read2st64_b64 v[136:139], v37 offset0:128 offset1:129\n"
    "v_mfma_f32_16x16x32_bf16 v[220:223], v[188:191], v[164:167], v[220:223]\n"
    "ds_read_b64_tr_b16 v[156:157], v45 offset:0\n"
    "v_mfma_f32_16x16x32_bf16 v[224:227], v[176:179], v[168:171], v[224:227]\n"
    "ds_read_b64_tr_b16 v[158:159], v45 offset:4096\n"
    "v_mfma_f32_16x16x32_bf16 v[228:231], v[180:183], v[168:171], v[228:231]\n"
    "global_load_lds_dwordx4 v35, s[42:43] offset:1024\n"
    "s_add_u32 s42, s42, 65536\n"
    "s_addc_u32 s43, s43, 0\n"
    "ds_read2st64_b64 v[140:143], v37 offset0:130 offset1:131\n"
    "v_mfma_f32_16x16x32_bf16 v[232:235], v[184:187], v[168:171], v[232:235]\n"
    "v_mfma_f32_16x16x32_bf16 v[236:239], v[188:191], v[168:171], v[236:239]\n"
    "v_mfma_f32_16x16x32_bf16 v[240:243], v[176:179], v[172:175], v[240:243]\n"
    "v_mfma_f32_16x16x32_bf16 v[244:247], v[180:183], v[172:175], v[244:247]\n"
    "v_mfma_f32_16x16x32_bf16 v[248:251], v[184:187], v[172:175], v[248:251]\n"
    "s_waitcnt vmcnt(24) lgkmcnt(0)\n"
    "s_barrier\n"
    "v_mfma_f32_16x16x32_bf16 v[252:255], v[188:191], v[172:175], v[252:255]\n"
    "v_mfma_f32_16x16x32_bf16 v[192:195], v[144:147], v[128:131], v[192:195]\n"
    "s_add_u32 m0, s44, 65536\n"
    "ds_read_b64_tr_b16 v[176:177], v42 offset:16384\n"
    "ds_read_b64_tr_b16 v[178:179], v42 offset:20480\n"
    "v_mfma_f32_16x16x32_bf16 v[196:199], v[148:151], v[128:131], v[196:199]\n"
    "global_load_lds_dwordx4 v32, s[40:41] offset:0 sc1\n"
    "ds_read2st64_b64 v[160:163], v36 offset0:160 offset1:161\n"
    "v_mfma_f32_16x16x32_bf16 v[200:203], v[152:155], v[128:131], v[200:203]\n"
    "ds_read_b64_tr_b16 v[180:181], v43 offset:16384\n"
    "v_mfma_f32_16x16x32_bf16 v[204:207], v[156:159], v[128:131], v[204:207]\n"
    "global_load_lds_dwordx4 v32, s[40:41] offset:1024 sc1\n"
    "s_add_u32 s40, s40, 8192\n"
    "s_addc_u32 s41, s41, 0\n"
    "ds_read_b64_tr_b16 v[182:183], v43 offset:20480\n"
    "v_mfma_f32_16x16x32_bf16 v[208:211], v[144:147], v[132:135], v[208:211]\n"
    "ds_read2st64_b64 v[164:167], v36 offset0:162 offset1:163\n"
    "v_mfma_f32_16x16x32_bf16 v[212:215], v[148:151], v[132:135], v[212:215]\n"
    "s_add_u32 m0, s44, 73728\n"
    "ds_read_b64_tr_b16 v[184:185], v44 offset:16384\n"
    "ds_read_b64_tr_b16 v[186:187], v44 offset:20480\n"
    "v_mfma_f32_16x16x32_bf16 v[216:219], v[152:155], v[132:135], v[216:219]\n"
    "global_load_lds_dwordx4 v34, s[42:43] offset:0\n"
    "ds_read2st64_b64 v[168:171], v37 offset0:160 offset1:161\n"
    "v_mfma_f32_16x16x32_bf16 v[220:223], v[156:159], v[132:135], v[220:223]\n"
    "ds_read_b64_tr_b16 v[188:189], v45 offset:16384\n"
    "v_mfma_f32_16x16x32_bf16 v[224:227], v[144:147], v[136:139], v[224:227]\n"
    "ds_read_b64_tr_b16 v[190:191], v45 offset:20480\n"
    "v_mfma_f32_16x16x32_bf16 v[228:231], v[148:151], v[136:139], v[228:231]\n"
    "global_load_lds_dwordx4 v35, s[42:43] offset:1024\n"
    "s_add_u32 s42, s42, 65536\n"
    "s_addc_u32 s43, s43, 0\n"
    "ds_read2st64_b64 v[172:175], v37 offset0:162 offset1:163\n"
    "v_mfma_f32_16x16x32_bf16 v[232:235], v[152:155], v[136:139], v[232:235]\n"
    "v_mfma_f32_16x16x32_bf16 v[236:239], v[156:159], v[136:139], v[236:239]\n"
    "v_mfma_f32_16x16x32_bf16 v[240:243], v[144:147], v[140:143], v[240:243]\n"
    "v_mfma_f32_16x16x32_bf16 v[244:247], v[148:151], v[140:143], v[244:247]\n"
    "v_mfma_f32_16x16x32_bf16 v[248:251], v[152:155], v[140:143], v[248:251]\n"
    "s_waitcnt vmcnt(24) lgkmcnt(0)\n"
    "s_barrier\n"
    "v_mfma_f32_16x16x32_bf16 v[252:255], v[156:159], v[140:143], v[252:255]\n"
    "v_mfma_f32_16x16x32_bf16 v[192:195], v[176:179], v[160:163], v[192:195]\n"
    "s_add_u32 m0, s44, 81920\n"
    "ds_read_b64_tr_b16 v[144:145], v42 offset:32768\n"
    "ds_read_b64_tr_b16 v[146:147], v42 offset:36864\n"
    "v_mfma_f32_16x16x32_bf16 v[196:199], v[180:183], v[160:163], v[196:199]\n"
    "global_load_lds_dwordx4 v32, s[40:41] offset:0 sc1\n"
    "ds_read2st64_b64 v[128:131], v36 offset0:192 offset1:193\n"
    "v_mfma_f32_16x16x32_bf16 v[200:203], v[184:187], v[160:163], v[200:203]\n"
    "ds_read_b64_tr_b16 v[148:149], v43 offset:32768\n"
    "v_mfma_f32_16x16x32_bf16 v[204:207], v[188:191], v[160:163], v[204:207]\n"
    "global_load_lds_dwordx4 v32, s[40:41] offset:1024 sc1\n"
    "s_add_u32 s40, s40, 8192\n"
    "s_addc_u32 s41, s41, 0\n"
    "ds_read_b64_tr_b16 v[150:151], v43 offset:36864\n"
    "v_mfma_f32_16x16x32_bf16 v[208:211], v[176:179], v[164:167], v[208:211]\n"
    "ds_read2st64_b64 v[132:135], v36 offset0:194 offset1:195\n"
    "v_mfma_f32_16x16x32_bf16 v[212:215], v[180:183], v[164:167], v[212:215]\n"
    "s_add_u32 m0, s44, 90112\n"
    "ds_read_b64_tr_b16 v[152:153], v44 offset:32768\n"
    "ds_read_b64_tr_b16 v[154:155], v44 offset:36864\n"
    "v_mfma_f32_16x16x32_bf16 v[216:219], v[184:187], v[164:167], v[216:219]\n"
    "global_load_lds_dwordx4 v34, s[42:43] offset:0\n"
    "ds_read2st64_b64 v[136:139], v37 offset0:192 offset1:193\n"
    "v_mfma_f32_16x16x32_bf16 v[220:223], v[188:191], v[164:167], v[220:223]\n"
    "ds_read_b64_tr_b16 v[156:157], v45 offset:32768\n"
    "v_mfma_f32_16x16x32_bf16 v[224:227], v[176:179], v[168:171], v[224:227]\n"
    "ds_read_b64_tr_b16 v[158:159], v45 offset:36864\n"
    "v_mfma_f32_16x16x32_bf16 v[228:231], v[180:183], v[168:171], v[228:231]\n"
    "global_load_lds_dwordx4 v35, s[42:43] offset:1024\n"
    "s_add_u32 s42, s42, 65536\n"
    "s_addc_u32 s43, s43, 0\n"
    "ds_read2st64_b64 v[140:143], v37 offset0:194 offset1:195\n"
    "v_mfma_f32_16x16x32_bf16 v[232:235], v[184:187], v[168:171], v[232:235]\n"
    "v_mfma_f32_16x16x32_bf16 v[236:239], v[188:191], v[168:171], v[236:239]\n"
    "v_mfma_f32_16x16x32_bf16 v[240:243], v[176:179], v[172:175], v[240:243]\n"
    "v_mfma_f32_16x16x32_bf16 v[244:247], v[180:183], v[172:175], v[244:247]\n"
    "v_mfma_f32_16x16x32_bf16 v[248:251], v[184:187], v[172:175], v[248:251]\n"
    "s_waitcnt vmcnt(24) lgkmcnt(0)\n"
    "s_barrier\n"
    "v_mfma_f32_16x16x32_bf16 v[252:255], v[188:191], v[172:175], v[252:255]\n"
    "s_sub_u32 s46, s46, 1\n"
    "s_cmp_lg_u32 s46, 0\n"
    "s_cbranch_scc1 1b\n"
    "v_mfma_f32_16x16x32_bf16 v[192:195], v[144:147], v[128:131], v[192:195]\n"
    "s_add_u32 m0, s44, 98304\n"
    "ds_read_b64_tr_b16 v[176:177], v42 offset:49152\n"
    "ds_read_b64_tr_b16 v[178:179], v42 offset:53248\n"
    "v_mfma_f32_16x16x32_bf16 v[196:199], v[148:151], v[128:131], v[196:199]\n"
    "global_load_lds_dwordx4 v32, s[40:41] offset:0 sc1\n"
    "ds_read2st64_b64 v[160:163], v36 offset0:224 offset1:225\n"
    "v_mfma_f32_16x16x32_bf16 v[200:203], v[152:155], v[128:131], v[200:203]\n"
    "ds_read_b64_tr_b16 v[180:181], v43 offset:49152\n"
    "v_mfma_f32_16x16x32_bf16 v[204:207], v[156:159], v[128:131], v[204:207]\n"
    "global_load_lds_dwordx4 v32, s[40:41] offset:1024 sc1\n"
    "s_add_u32 s40, s40, 8192\n"
    "s_addc_u32 s41, s41, 0\n"
    "ds_read_b64_tr_b16 v[182:183], v43 offset:53248\n"
    "v_mfma_f32_16x16x32_bf16 v[208:211], v[144:147], v[132:135], v[208:211]\n"
    "ds_read2st64_b64 v[164:167], v36 offset0:226 offset1:227\n"
    "v_mfma_f32_16x16x32_bf16 v[212:215], v[148:151], v[132:135], v[212:215]\n"
    "s_add_u32 m0, s44, 106496\n"
    "ds_read_b64_tr_b16 v[184:185], v44 offset:49152\n"
    "ds_read_b64_tr_b16 v[186:187], v44 offset:53248\n"
    "v_mfma_f32_16x16x32_bf16 v[216:219], v[152:155], v[132:135], v[216:219]\n"
    "global_load_lds_dwordx4 v34, s[42:43] offset:0\n"
    "ds_read2st64_b64 v[168:171], v37 offset0:224 offset1:225\n"
    "v_mfma_f32_16x16x32_bf16 v[220:223], v[156:159], v[132:135], v[220:223]\n"
    "ds_read_b64_tr_b16 v[188:189], v45 offset:49152\n"
    "v_mfma_f32_16x16x32_bf16 v[224:227], v[144:147], v[136:139], v[224:227]\n"
    "ds_read_b64_tr_b16 v[190:191], v45 offset:53248\n"
    "v_mfma_f32_16x16x32_bf16 v[228:231], v[148:151], v[136:139], v[228:231]\n"
    "global_load_lds_dwordx4 v35, s[42:43] offset:1024\n"
    "s_add_u32 s42, s42, 65536\n"
    "s_addc_u32 s43, s43, 0\n"
    "ds_read2st64_b64 v[172:175], v37 offset0:226 offset1:227\n"
    "v_mfma_f32_16x16x32_bf16 v[232:235], v[152:155], v[136:139], v[232:235]\n"
    "v_mfma_f32_16x16x32_bf16 v[236:239], v[156:159], v[136:139], v[236:239]\n"
    "v_mfma_f32_16x16x32_bf16 v[240:243], v[144:147], v[140:143], v[240:243]\n"
    "v_mfma_f32_16x16x32_bf16 v[244:247], v[148:151], v[140:143], v[244:247]\n"
    "v_mfma_f32_16x16x32_bf16 v[248:251], v[152:155], v[140:143], v[248:251]\n"
    "s_waitcnt vmcnt(24) lgkmcnt(0)\n"
    "s_barrier\n"
    "v_mfma_f32_16x16x32_bf16 v[252:255], v[156:159], v[140:143], v[252:255]\n"
    "v_mfma_f32_16x16x32_bf16 v[192:195], v[176:179], v[160:163], v[192:195]\n"
    "s_add_u32 m0, s44, 114688\n"
    "ds_read_b64_tr_b16 v[144:145], v38 offset:0\n"
    "ds_read_b64_tr_b16 v[146:147], v38 offset:4096\n"
    "v_mfma_f32_16x16x32_bf16 v[196:199], v[180:183], v[160:163], v[196:199]\n"
    "global_load_lds_dwordx4 v32, s[40:41] offset:0 sc1\n"
    "ds_read2st64_b64 v[128:131], v36 offset0:0 offset1:1\n"
    "v_mfma_f32_16x16x32_bf16 v[200:203], v[184:187], v[160:163], v[200:203]\n"
    "ds_read_b64_tr_b16 v[148:149], v39 offset:0\n"
    "v_mfma_f32_16x16x32_bf16 v[204:207], v[188:191], v[160:163], v[204:207]\n"
    "global_load_lds_dwordx4 v32, s[40:41] offset:1024 sc1\n"
    "s_add_u32 s40, s40, 8192\n"
    "s_addc_u32 s41, s41, 0\n"
    "ds_read_b64_tr_b16 v[150:151], v39 offset:4096\n"
    "v_mfma_f32_16x16x32_bf16 v[208:211], v[176:179], v[164:167], v[208:211]\n"
    "ds_read2st64_b64 v[132:135], v36 offset0:2 offset1:3\n"
    "v_mfma_f32_16x16x32_bf16 v[212:215], v[180:183], v[164:167], v[212:215]\n"
    "s_add_u32 m0, s44, 122880\n"
    "ds_read_b64_tr_b16 v[152:153], v40 offset:0\n"
    "ds_read_b64_tr_b16 v[154:155], v40 offset:4096\n"
    "v_mfma_f32_16x16x32_bf16 v[216:219], v[184:187], v[164:167], v[216:219]\n"
    "global_load_lds_dwordx4 v34, s[42:43] offset:0\n"
    "ds_read2st64_b64 v[136:139], v37 offset0:0 offset1:1\n"
    "v_mfma_f32_16x16x32_bf16 v[220:223], v[188:191], v[164:167], v[220:223]\n"
    "ds_read_b64_tr_b16 v[156:157], v41 offset:0\n"
    "v_mfma_f32_16x16x32_bf16 v[224:227], v[176:179], v[168:171], v[224:227]\n"
    "ds_read_b64_tr_b16 v[158:159], v41 offset:4096\n"
    "v_mfma_f32_16x16x32_bf16 v[228:231], v[180:183], v[168:171], v[228:231]\n"
    "global_load_lds_dwordx4 v35, s[42:43] offset:1024\n"
    "s_add_u32 s42, s42, 65536\n"
    "s_addc_u32 s43, s43, 0\n"
    "ds_read2st64_b64 v[140:143], v37 offset0:2 offset1:3\n"
    "v_mfma_f32_16x16x32_bf16 v[232:235], v[184:187], v[168:171], v[232:235]\n"
    "v_mfma_f32_16x16x32_bf16 v[236:239], v[188:191], v[168:171], v[236:239]\n"
    "v_mfma_f32_16x16x32_bf16 v[240:243], v[176:179], v[172:175], v[240:243]\n"
    "v_mfma_f32_16x16x32_bf16 v[244:247], v[180:183], v[172:175], v[244:247]\n"
    "v_mfma_f32_16x16x32_bf16 v[248:251], v[184:187], v[172:175], v[248:251]\n"
    "s_waitcnt vmcnt(24) lgkmcnt(0)\n"
    "s_barrier\n"
    "v_mfma_f32_16x16x32_bf16 v[252:255], v[188:191], v[172:175], v[252:255]\n"
    "v_mfma_f32_16x16x32_bf16 v[192:195], v[144:147], v[128:131], v[192:195]\n"
    "ds_read_b64_tr_b16 v[176:177], v38 offset:16384\n"
    "ds_read_b64_tr_b16 v[178:179], v38 offset:20480\n"
    "v_mfma_f32_16x16x32_bf16 v[196:199], v[148:151], v[128:131], v[196:199]\n"
    "global_load_dwordx4 v[96:99], v46, s[48:49] sc1\n"
    "ds_read2st64_b64 v[160:163], v36 offset0:32 offset1:33\n"
    "v_mfma_f32_16x16x32_bf16 v[200:203], v[152:155], v[128:131], v[200:203]\n"
    "global_load_dwordx4 v[100:103], v46, s[48:49] offset:1024 sc1\n"
    "ds_read_b64_tr_b16 v[180:181], v39 offset:16384\n"
    "v_mfma_f32_16x16x32_bf16 v[204:207], v[156:159], v[128:131], v[204:207]\n"
    "global_load_dwordx4 v[104:107], v47, s[48:49] sc1\n"
    "ds_read_b64_tr_b16 v[182:183], v39 offset:20480\n"
    "v_mfma_f32_16x16x32_bf16 v[208:211], v[144:147], v[132:135], v[208:211]\n"
    "ds_read2st64_b64 v[164:167], v36 offset0:34 offset1:35\n"
    "v_mfma_f32_16x16x32_bf16 v[212:215], v[148:151], v[132:135], v[212:215]\n"
    "global_load_dwordx4 v[108:111], v47, s[48:49] offset:1024 sc1\n"
    "ds_read_b64_tr_b16 v[184:185], v40 offset:16384\n"
    "ds_read_b64_tr_b16 v[186:187], v40 offset:20480\n"
    "v_mfma_f32_16x16x32_bf16 v[216:219], v[152:155], v[132:135], v[216:219]\n"
    "global_load_dword v112, v48, s[50:51] sc1\n"
    "ds_read2st64_b64 v[168:171], v37 offset0:32 offset1:33\n"
    "v_mfma_f32_16x16x32_bf16 v[220:223], v[156:159], v[132:135], v[220:223]\n"
    "global_load_dword v113, v48, s[50:51] offset:64 sc1\n"
    "ds_read_b64_tr_b16 v[188:189], v41 offset:16384\n"
    "v_mfma_f32_16x16x32_bf16 v[224:227], v[144:147], v[136:139], v[224:227]\n"
    "s_getpc_b64 s[70:71]\n"
    "2:\n"
    "v_min_u32 v31, lin_end-2b-4, v33\n"
    "s_mov_b32 m0, s47\n"
    "ds_read_b64_tr_b16 v[190:191], v41 offset:20480\n"
    "v_mfma_f32_16x16x32_bf16 v[228:231], v[148:151], v[136:139], v[228:231]\n"
    "global_load_lds_dword v31, s[70:71]\n"
    "s_getpc_b64 s[70:71]\n"
    "s_add_u32 s70, s70, lin_main@rel32@lo+4\n"
    "s_addc_u32 s71, s71, lin_main@rel32@hi+12\n"
    "v_min_u32 v31, lin_end-lin_main-4, v33\n"
    "s_mov_b32 m0, s47\n"
    "ds_read2st64_b64 v[172:175], v37 offset0:34 offset1:35\n"
    "v_mfma_f32_16x16x32_bf16 v[232:235], v[152:155], v[136:139], v[232:235]\n"
    "global_load_lds_dword v31, s[70:71]\n"
    "v_mfma_f32_16x16x32_bf16 v[236:239], v[156:159], v[136:139], v[236:239]\n"
    "v_mfma_f32_16x16x32_bf16 v[240:243], v[144:147], v[140:143], v[240:243]\n"
    "v_mfma_f32_16x16x32_bf16 v[244:247], v[148:151], v[140:143], v[244:247]\n"
    "v_mfma_f32_16x16x32_bf16 v[248:251], v[152:155], v[140:143], v[248:251]\n"
    "s_waitcnt vmcnt(28) lgkmcnt(0)\n"
    "s_barrier\n"
    "v_mfma_f32_16x16x32_bf16 v[252:255], v[156:159], v[140:143], v[252:255]\n"
    "v_mfma_f32_16x16x32_bf16 v[224:227], v[176:179], v[168:171], v[224:227]\n"
    "ds_read2st64_b64 v[136:139], v37 offset0:64 offset1:65\n"
    "ds_read_b64_tr_b16 v[144:145], v38 offset:32768\n"
    "v_mfma_f32_16x16x32_bf16 v[228:231], v[180:183], v[168:171], v[228:231]\n"
    "ds_read_b64_tr_b16 v[146:147], v38 offset:36864\n"
    "ds_read_b64_tr_b16 v[148:149], v39 offset:32768\n"
    "v_mfma_f32_16x16x32_bf16 v[232:235], v[184:187], v[168:171], v[232:235]\n"
    "ds_read_b64_tr_b16 v[150:151], v39 offset:36864\n"
    "v_mfma_f32_16x16x32_bf16 v[236:239], v[188:191], v[168:171], v[236:239]\n"
    "ds_read_b64_tr_b16 v[152:153], v40 offset:32768\n"
    "ds_read_b64_tr_b16 v[154:155], v40 offset:36864\n"
    "v_mfma_f32_16x16x32_bf16 v[240:243], v[176:179], v[172:175], v[240:243]\n"
    "ds_read_b64_tr_b16 v[156:157], v41 offset:32768\n"
    "ds_read_b64_tr_b16 v[158:159], v41 offset:36864\n"
    "v_mfma_f32_16x16x32_bf16 v[244:247], v[180:183], v[172:175], v[244:247]\n"
    "ds_read2st64_b64 v[140:143], v37 offset0:66 offset1:67\n"
    "v_mfma_f32_16x16x32_bf16 v[248:251], v[184:187], v[172:175], v[248:251]\n"
    "v_mfma_f32_16x16x32_bf16 v[252:255], v[188:191], v[172:175], v[252:255]\n"
    "s_waitcnt vmcnt(24)\n"
    "s_barrier\n"
    "s_waitcnt lgkmcnt(7)\n"
    "v_mfma_f32_16x16x32_bf16 v[224:227], v[144:147], v[136:139], v[224:227]\n"
    "ds_read2st64_b64 v[168:171], v37 offset0:96 offset1:97\n"
    "ds_read_b64_tr_b16 v[176:177], v38 offset:49152\n"
    "s_waitcnt lgkmcnt(7)\n"
    "v_mfma_f32_16x16x32_bf16 v[228:231], v[148:151], v[136:139], v[228:231]\n"
    "ds_read_b64_tr_b16 v[178:179], v38 offset:53248\n"
    "ds_read_b64_tr_b16 v[180:181], v39 offset:49152\n"
    "s_waitcnt lgkmcnt(7)\n"
    "v_mfma_f32_16x16x32_bf16 v[232:235], v[152:155], v[136:139], v[232:235]\n"
    "ds_read_b64_tr_b16 v[182:183], v39 offset:53248\n"
    "s_waitcnt lgkmcnt(6)\n"
    "v_mfma_f32_16x16x32_bf16 v[236:239], v[156:159], v[136:139], v[236:239]\n"
    "ds_read_b64_tr_b16 v[184:185], v40 offset:49152\n"
    "ds_read_b64_tr_b16 v[186:187], v40 offset:53248\n"
    "s_waitcnt lgkmcnt(7)\n"
    "v_mfma_f32_16x16x32_bf16 v[240:243], v[144:147], v[140:143], v[240:243]\n"
    "ds_read_b64_tr_b16 v[188:189], v41 offset:49152\n"
    "ds_read_b64_tr_b16 v[190:191], v41 offset:53248\n"
    "v_mfma_f32_16x16x32_bf16 v[244:247], v[148:151], v[140:143], v[244:247]\n"
    "ds_read2st64_b64 v[172:175], v37 offset0:98 offset1:99\n"
    "v_mfma_f32_16x16x32_bf16 v[248:251], v[152:155], v[140:143], v[248:251]\n"
    "v_mfma_f32_16x16x32_bf16 v[252:255], v[156:159], v[140:143], v[252:255]\n"
    "s_waitcnt vmcnt(20)\n"
    "s_barrier\n"
    "s_waitcnt lgkmcnt(7)\n"
    "v_mfma_f32_16x16x32_bf16 v[224:227], v[176:179], v[168:171], v[224:227]\n"
    "ds_read2st64_b64 v[136:139], v37 offset0:128 offset1:129\n"
    "ds_read_b64_tr_b16 v[144:145], v42 offset:0\n"
    "s_waitcnt lgkmcnt(7)\n"
    "v_mfma_f32_16x16x32_bf16 v[228:231], v[180:183], v[168:171], v[228:231]\n"
    "ds_read_b64_tr_b16 v[146:147], v42 offset:4096\n"
    "ds_read_b64_tr_b16 v[148:149], v43 offset:0\n"
    "s_waitcnt lgkmcnt(7)\n"
    "v_mfma_f32_16x16x32_bf16 v[232:235], v[184:187], v[168:171], v[232:235]\n"
    "ds_read_b64_tr_b16 v[150:151], v43 offset:4096\n"
    "s_waitcnt lgkmcnt(6)\n"
    "v_mfma_f32_16x16x32_bf16 v[236:239], v[188:191], v[168:171], v[236:239]\n"
    "ds_read_b64_tr_b16 v[152:153], v44 offset:0\n"
    "ds_read_b64_tr_b16 v[154:155], v44 offset:4096\n"
    "s_waitcnt lgkmcnt(7)\n"
    "v_mfma_f32_16x16x32_bf16 v[240:243], v[176:179], v[172:175], v[240:243]\n"
    "ds_read_b64_tr_b16 v[156:157], v45 offset:0\n"
    "ds_read_b64_tr_b16 v[158:159], v45 offset:4096\n"
    "v_mfma_f32_16x16x32_bf16 v[244:247], v[180:183], v[172:175], v[244:247]\n"
    "ds_read2st64_b64 v[140:143], v37 offset0:130 offset1:131\n"
    "v_mfma_f32_16x16x32_bf16 v[248:251], v[184:187], v[172:175], v[248:251]\n"
    "v_mfma_f32_16x16x32_bf16 v[252:255], v[188:191], v[172:175], v[252:255]\n"
    "s_waitcnt vmcnt(16)\n"
    "s_barrier\n"
    "s_waitcnt lgkmcnt(7)\n"
    "v_mfma_f32_16x16x32_bf16 v[224:227], v[144:147], v[136:139], v[224:227]\n"
    "ds_read2st64_b64 v[168:171], v37 offset0:160 offset1:161\n"
    "ds_read_b64_tr_b16 v[176:177], v42 offset:16384\n"
    "s_waitcnt lgkmcnt(7)\n"
    "v_mfma_f32_16x16x32_bf16 v[228:231], v[148:151], v[136:139], v[228:231]\n"
    "ds_read_b64_tr_b16 v[178:179], v42 offset:20480\n"
    "ds_read_b64_tr_b16 v[180:181], v43 offset:16384\n"
    "s_waitcnt lgkmcnt(7)\n"
    "v_mfma_f32_16x16x32_bf16 v[232:235], v[152:155], v[136:139], v[232:235]\n"
    "ds_read_b64_tr_b16 v[182:183], v43 offset:20480\n"
    "s_waitcnt lgkmcnt(6)\n"
    "v_mfma_f32_16x16x32_bf16 v[236:239], v[156:159], v[136:139], v[236:239]\n"
    "ds_read_b64_tr_b16 v[184:185], v44 offset:16384\n"
    "ds_read_b64_tr_b16 v[186:187], v44 offset:20480\n"
    "s_waitcnt lgkmcnt(7)\n"
    "v_mfma_f32_16x16x32_bf16 v[240:243], v[144:147], v[140:143], v[240:243]\n"
    "ds_read_b64_tr_b16 v[188:189], v45 offset:16384\n"
    "ds_read_b64_tr_b16 v[190:191], v45 offset:20480\n"
    "v_mfma_f32_16x16x32_bf16 v[244:247], v[148:151], v[140:143], v[244:247]\n"
    "ds_read2st64_b64 v[172:175], v37 offset0:162 offset1:163\n"
    "v_mfma_f32_16x16x32_bf16 v[248:251], v[152:155], v[140:143], v[248:251]\n"
    "v_mfma_f32_16x16x32_bf16 v[252:255], v[156:159], v[140:143], v[252:255]\n"
    "s_waitcnt vmcnt(12)\n"
    "s_barrier\n"
    "s_waitcnt lgkmcnt(7)\n"
    "v_mfma_f32_16x16x32_bf16 v[224:227], v[176:179], v[168:171], v[224:227]\n"
    "ds_read2st64_b64 v[136:139], v37 offset0:192 offset1:193\n"
    "ds_read_b64_tr_b16 v[144:145], v42 offset:32768\n"
    "s_waitcnt lgkmcnt(7)\n"
    "v_mfma_f32_16x16x32_bf16 v[228:231], v[180:183], v[168:171], v[228:231]\n"
    "ds_read_b64_tr_b16 v[146:147], v42 offset:36864\n"
    "ds_read_b64_tr_b16 v[148:149], v43 offset:32768\n"
    "s_waitcnt lgkmcnt(7)\n"
    "v_mfma_f32_16x16x32_bf16 v[232:235], v[184:187], v[168:171], v[232:235]\n"
    "ds_read_b64_tr_b16 v[150:151], v43 offset:36864\n"
    "s_waitcnt lgkmcnt(6)\n"
    "v_mfma_f32_16x16x32_bf16 v[236:239], v[188:191], v[168:171], v[236:239]\n"
    "ds_read_b64_tr_b16 v[152:153], v44 offset:32768\n"
    "ds_read_b64_tr_b16 v[154:155], v44 offset:36864\n"
    "s_waitcnt lgkmcnt(7)\n"
    "v_mfma_f32_16x16x32_bf16 v[240:243], v[176:179], v[172:175], v[240:243]\n"
    "ds_read_b64_tr_b16 v[156:157], v45 offset:32768\n"
    "ds_read_b64_tr_b16 v[158:159], v45 offset:36864\n"
    "v_mfma_f32_16x16x32_bf16 v[244:247], v[180:183], v[172:175], v[244:247]\n"
    "ds_read2st64_b64 v[140:143], v37 offset0:194 offset1:195\n"
    "v_mfma_f32_16x16x32_bf16 v[248:251], v[184:187], v[172:175], v[248:251]\n"
    "v_mfma_f32_16x16x32_bf16 v[252:255], v[188:191], v[172:175], v[252:255]\n"
    "s_waitcnt vmcnt(8)\n"
    "s_barrier\n"
    "s_waitcnt lgkmcnt(7)\n"
    "v_mfma_f32_16x16x32_bf16 v[224:227], v[144:147], v[136:139], v[224:227]\n"
    "ds_read2st64_b64 v[168:171], v37 offset0:224 offset1:225\n"
    "ds_read_b64_tr_b16 v[176:177], v42 offset:49152\n"
    "s_waitcnt lgkmcnt(7)\n"
    "v_mfma_f32_16x16x32_bf16 v[228:231], v[148:151], v[136:139], v[228:231]\n"
    "ds_read_b64_tr_b16 v[178:179], v42 offset:53248\n"
    "ds_read_b64_tr_b16 v[180:181], v43 offset:49152\n"
    "s_waitcnt lgkmcnt(7)\n"
    "v_mfma_f32_16x16x32_bf16 v[232:235], v[152:155], v[136:139], v[232:235]\n"
    "ds_read_b64_tr_b16 v[182:183], v43 offset:53248\n"
    "s_waitcnt lgkmcnt(6)\n"
    "v_mfma_f32_16x16x32_bf16 v[236:239], v[156:159], v[136:139], v[236:239]\n"
    "ds_read_b64_tr_b16 v[184:185], v44 offset:49152\n"
    "ds_read_b64_tr_b16 v[186:187], v44 offset:53248\n"
    "s_waitcnt lgkmcnt(7)\n"
    "v_mfma_f32_16x16x32_bf16 v[240:243], v[144:147], v[140:143], v[240:243]\n"
    "ds_read_b64_tr_b16 v[188:189], v45 offset:49152\n"
    "ds_read_b64_tr_b16 v[190:191], v45 offset:53248\n"
    "v_mfma_f32_16x16x32_bf16 v[244:247], v[148:151], v[140:143], v[244:247]\n"
    "ds_read2st64_b64 v[172:175], v37 offset0:226 offset1:227\n"
    "v_mfma_f32_16x16x32_bf16 v[248:251], v[152:155], v[140:143], v[248:251]\n"
    "v_mfma_f32_16x16x32_bf16 v[252:255], v[156:159], v[140:143], v[252:255]\n"
    "s_waitcnt lgkmcnt(7)\n"
    "v_mfma_f32_16x16x32_bf16 v[224:227], v[176:179], v[168:171], v[224:227]\n"
    "ds_read2st64_b64 v[128:131], v36 offset0:32 offset1:33\n"
    "ds_read_b64_tr_b16 v[144:145], v38 offset:16384\n"
    "s_waitcnt lgkmcnt(7)\n"
    "v_mfma_f32_16x16x32_bf16 v[228:231], v[180:183], v[168:171], v[228:231]\n"
    "ds_read_b64_tr_b16 v[146:147], v38 offset:20480\n"
    "ds_read_b64_tr_b16 v[148:149], v39 offset:16384\n"
    "s_waitcnt lgkmcnt(7)\n"
    "v_mfma_f32_16x16x32_bf16 v[232:235], v[184:187], v[168:171], v[232:235]\n"
    "ds_read_b64_tr_b16 v[150:151], v39 offset:20480\n"
    "ds_read_b64_tr_b16 v[152:153], v40 offset:16384\n"
    "s_waitcnt lgkmcnt(7)\n"
    "v_mfma_f32_16x16x32_bf16 v[236:239], v[188:191], v[168:171], v[236:239]\n"
    "ds_read_b64_tr_b16 v[154:155], v40 offset:20480\n"
    "ds_read_b64_tr_b16 v[156:157], v41 offset:16384\n"
    "s_waitcnt lgkmcnt(8)\n"
    "v_mfma_f32_16x16x32_bf16 v[240:243], v[176:179], v[172:175], v[240:243]\n"
    "ds_read_b64_tr_b16 v[158:159], v41 offset:20480\n"
    "ds_read2st64_b64 v[132:135], v36 offset0:34 offset1:35\n"
    "v_mfma_f32_16x16x32_bf16 v[244:247], v[180:183], v[172:175], v[244:247]\n"
    "v_mfma_f32_16x16x32_bf16 v[248:251], v[184:187], v[172:175], v[248:251]\n"
    "v_mfma_f32_16x16x32_bf16 v[252:255], v[188:191], v[172:175], v[252:255]\n"
    "s_waitcnt lgkmcnt(7)\n"
    "v_mfma_f32_16x16x32_bf16 v[192:195], v[144:147], v[128:131], v[192:195]\n"
    "ds_read2st64_b64 v[160:163], v36 offset0:64 offset1:65\n"
    "ds_read_b64_tr_b16 v[176:177], v38 offset:32768\n"
    "v_cvt_pk_bf16_f32 v224, v224, v225\n"
    "v_cvt_pk_bf16_f32 v225, v226, v227\n"
    "s_waitcnt lgkmcnt(7)\n"
    "v_mfma_f32_16x16x32_bf16 v[196:199], v[148:151], v[128:131], v[196:199]\n"
    "ds_read_b64_tr_b16 v[178:179], v38 offset:36864\n"
    "ds_read_b64_tr_b16 v[180:181], v39 offset:32768\n"
    "v_cvt_pk_bf16_f32 v226, v228, v229\n"
    "v_cvt_pk_bf16_f32 v227, v230, v231\n"
    "global_store_dwordx4 v49, v[224:227], s[52:53] offset:0 sc1\n"
    "s_waitcnt lgkmcnt(7)\n"
    "v_mfma_f32_16x16x32_bf16 v[200:203], v[152:155], v[128:131], v[200:203]\n"
    "ds_read_b64_tr_b16 v[182:183], v39 offset:36864\n"
    "ds_read_b64_tr_b16 v[184:185], v40 offset:32768\n"
    "v_cvt_pk_bf16_f32 v232, v232, v233\n"
    "v_cvt_pk_bf16_f32 v233, v234, v235\n"
    "s_waitcnt lgkmcnt(7)\n"
    "v_mfma_f32_16x16x32_bf16 v[204:207], v[156:159], v[128:131], v[204:207]\n"
    "ds_read_b64_tr_b16 v[186:187], v40 offset:36864\n"
    "ds_read_b64_tr_b16 v[188:189], v41 offset:32768\n"
    "v_cvt_pk_bf16_f32 v234, v236, v237\n"
    "v_cvt_pk_bf16_f32 v235, v238, v239\n"
    "global_store_dwordx4 v49, v[232:235], s[52:53] offset:1024 sc1\n"
    "s_waitcnt lgkmcnt(8)\n"
    "v_mfma_f32_16x16x32_bf16 v[208:211], v[144:147], v[132:135], v[208:211]\n"
    "ds_read_b64_tr_b16 v[190:191], v41 offset:36864\n"
    "ds_read2st64_b64 v[164:167], v36 offset0:66 offset1:67\n"
    "v_cvt_pk_bf16_f32 v240, v240, v241\n"
    "v_cvt_pk_bf16_f32 v241, v242, v243\n"
    "v_mfma_f32_16x16x32_bf16 v[212:215], v[148:151], v[132:135], v[212:215]\n"
    "v_cvt_pk_bf16_f32 v242, v244, v245\n"
    "v_cvt_pk_bf16_f32 v243, v246, v247\n"
    "global_store_dwordx4 v49, v[240:243], s[52:53] offset:2048 sc1\n"
    "v_mfma_f32_16x16x32_bf16 v[216:219], v[152:155], v[132:135], v[216:219]\n"
    "v_cvt_pk_bf16_f32 v248, v248, v249\n"
    "v_cvt_pk_bf16_f32 v249, v250, v251\n"
    "v_mfma_f32_16x16x32_bf16 v[220:223], v[156:159], v[132:135], v[220:223]\n"
    "v_cvt_pk_bf16_f32 v250, v252, v253\n"
    "v_cvt_pk_bf16_f32 v251, v254, v255\n"
    "global_store_dwordx4 v49, v[248:251], s[52:53] offset:3072 sc1\n"
    "s_waitcnt lgkmcnt(7)\n"
    "v_mfma_f32_16x16x32_bf16 v[192:195], v[176:179], v[160:163], v[192:195]\n"
    "ds_read2st64_b64 v[128:131], v36 offset0:96 offset1:97\n"
    "ds_read_b64_tr_b16 v[144:145], v38 offset:49152\n"
    "s_waitcnt lgkmcnt(7)\n"
    "v_mfma_f32_16x16x32_bf16 v[196:199], v[180:183], v[160:163], v[196:199]\n"
    "ds_read_b64_tr_b16 v[146:147], v38 offset:53248\n"
    "ds_read_b64_tr_b16 v[148:149], v39 offset:49152\n"
    "s_waitcnt lgkmcnt(7)\n"
    "v_mfma_f32_16x16x32_bf16 v[200:203], v[184:187], v[160:163], v[200:203]\n"
    "ds_read_b64_tr_b16 v[150:151], v39 offset:53248\n"
    "ds_read_b64_tr_b16 v[152:153], v40 offset:49152\n"
    "s_waitcnt lgkmcnt(7)\n"
    "v_mfma_f32_16x16x32_bf16 v[204:207], v[188:191], v[160:163], v[204:207]\n"
    "ds_read_b64_tr_b16 v[154:155], v40 offset:53248\n"
    "ds_read_b64_tr_b16 v[156:157], v41 offset:49152\n"
    "s_waitcnt lgkmcnt(8)\n"
    "v_mfma_f32_16x16x32_bf16 v[208:211], v[176:179], v[164:167], v[208:211]\n"
    "ds_read_b64_tr_b16 v[158:159], v41 offset:53248\n"
    "ds_read2st64_b64 v[132:135], v36 offset0:98 offset1:99\n"
    "v_mfma_f32_16x16x32_bf16 v[212:215], v[180:183], v[164:167], v[212:215]\n"
    "v_mfma_f32_16x16x32_bf16 v[216:219], v[184:187], v[164:167], v[216:219]\n"
    "v_mfma_f32_16x16x32_bf16 v[220:223], v[188:191], v[164:167], v[220:223]\n"
    "s_waitcnt lgkmcnt(7)\n"
    "v_mfma_f32_16x16x32_bf16 v[192:195], v[144:147], v[128:131], v[192:195]\n"
    "ds_read2st64_b64 v[160:163], v36 offset0:128 offset1:129\n"
    "ds_read_b64_tr_b16 v[176:177], v42 offset:0\n"
    "s_waitcnt lgkmcnt(7)\n"
    "v_mfma_f32_16x16x32_bf16 v[196:199], v[148:151], v[128:131], v[196:199]\n"
    "ds_read_b64_tr_b16 v[178:179], v42 offset:4096\n"
    "ds_read_b64_tr_b16 v[180:181], v43 offset:0\n"
    "s_waitcnt lgkmcnt(7)\n"
    "v_mfma_f32_16x16x32_bf16 v[200:203], v[152:155], v[128:131], v[200:203]\n"
    "ds_read_b64_tr_b16 v[182:183], v43 offset:4096\n"
    "ds_read_b64_tr_b16 v[184:185], v44 offset:0\n"
    "s_waitcnt lgkmcnt(7)\n"
    "v_mfma_f32_16x16x32_bf16 v[204:207], v[156:159], v[128:131], v[204:207]\n"
    "ds_read_b64_tr_b16 v[186:187], v44 offset:4096\n"
    "ds_read_b64_tr_b16 v[188:189], v45 offset:0\n"
    "s_waitcnt lgkmcnt(8)\n"
    "v_mfma_f32_16x16x32_bf16 v[208:211], v[144:147], v[132:135], v[208:211]\n"
    "ds_read_b64_tr_b16 v[190:191], v45 offset:4096\n"
    "ds_read2st64_b64 v[164:167], v36 offset0:130 offset1:131\n"
    "v_mfma_f32_16x16x32_bf16 v[212:215], v[148:151], v[132:135], v[212:215]\n"
    "v_mfma_f32_16x16x32_bf16 v[216:219], v[152:155], v[132:135], v[216:219]\n"
    "v_mfma_f32_16x16x32_bf16 v[220:223], v[156:159], v[132:135], v[220:223]\n"
    "s_waitcnt lgkmcnt(7)\n"
    "v_mfma_f32_16x16x32_bf16 v[192:195], v[176:179], v[160:163], v[192:195]\n"
    "ds_read2st64_b64 v[128:131], v36 offset0:160 offset1:161\n"
    "ds_read_b64_tr_b16 v[144:145], v42 offset:16384\n"
    "s_waitcnt lgkmcnt(7)\n"
    "v_mfma_f32_16x16x32_bf16 v[196:199], v[180:183], v[160:163], v[196:199]\n"
    "ds_read_b64_tr_b16 v[146:147], v42 offset:20480\n"
    "ds_read_b64_tr_b16 v[148:149], v43 offset:16384\n"
    "s_waitcnt lgkmcnt(7)\n"
    "v_mfma_f32_16x16x32_bf16 v[200:203], v[184:187], v[160:163], v[200:203]\n"
    "ds_read_b64_tr_b16 v[150:151], v43 offset:20480\n"
    "ds_read_b64_tr_b16 v[152:153], v44 offset:16384\n"
    "s_waitcnt lgkmcnt(7)\n"
    "v_mfma_f32_16x16x32_bf16 v[204:207], v[188:191], v[160:163], v[204:207]\n"
    "ds_read_b64_tr_b16 v[154:155], v44 offset:20480\n"
    "ds_read_b64_tr_b16 v[156:157], v45 offset:16384\n"
    "s_waitcnt lgkmcnt(8)\n"
    "v_mfma_f32_16x16x32_bf16 v[208:211], v[176:179], v[164:167], v[208:211]\n"
    "ds_read_b64_tr_b16 v[158:159], v45 offset:20480\n"
    "ds_read2st64_b64 v[132:135], v36 offset0:162 offset1:163\n"
    "v_mfma_f32_16x16x32_bf16 v[212:215], v[180:183], v[164:167], v[212:215]\n"
    "v_mfma_f32_16x16x32_bf16 v[216:219], v[184:187], v[164:167], v[216:219]\n"
    "v_mfma_f32_16x16x32_bf16 v[220:223], v[188:191], v[164:167], v[220:223]\n"
    "s_waitcnt lgkmcnt(7)\n"
    "v_mfma_f32_16x16x32_bf16 v[192:195], v[144:147], v[128:131], v[192:195]\n"
    "ds_read2st64_b64 v[160:163], v36 offset0:192 offset1:193\n"
    "ds_read_b64_tr_b16 v[176:177], v42 offset:32768\n"
    "s_waitcnt lgkmcnt(7)\n"
    "v_mfma_f32_16x16x32_bf16 v[196:199], v[148:151], v[128:131], v[196:199]\n"
    "ds_read_b64_tr_b16 v[178:179], v42 offset:36864\n"
    "ds_read_b64_tr_b16 v[180:181], v43 offset:32768\n"
    "s_waitcnt lgkmcnt(7)\n"
    "v_mfma_f32_16x16x32_bf16 v[200:203], v[152:155], v[128:131], v[200:203]\n"
    "ds_read_b64_tr_b16 v[182:183], v43 offset:36864\n"
    "ds_read_b64_tr_b16 v[184:185], v44 offset:32768\n"
    "s_waitcnt lgkmcnt(7)\n"
    "v_mfma_f32_16x16x32_bf16 v[204:207], v[156:159], v[128:131], v[204:207]\n"
    "ds_read_b64_tr_b16 v[186:187], v44 offset:36864\n"
    "ds_read_b64_tr_b16 v[188:189], v45 offset:32768\n"
    "s_waitcnt lgkmcnt(8)\n"
    "v_mfma_f32_16x16x32_bf16 v[208:211], v[144:147], v[132:135], v[208:211]\n"
    "ds_read_b64_tr_b16 v[190:191], v45 offset:36864\n"
    "ds_read2st64_b64 v[164:167], v36 offset0:194 offset1:195\n"
    "v_mfma_f32_16x16x32_bf16 v[212:215], v[148:151], v[132:135], v[212:215]\n"
    "v_mfma_f32_16x16x32_bf16 v[216:219], v[152:155], v[132:135], v[216:219]\n"
    "v_mfma_f32_16x16x32_bf16 v[220:223], v[156:159], v[132:135], v[220:223]\n"
    "s_waitcnt lgkmcnt(7)\n"
    "v_mfma_f32_16x16x32_bf16 v[192:195], v[176:179], v[160:163], v[192:195]\n"
    "ds_read2st64_b64 v[128:131], v36 offset0:224 offset1:225\n"
    "ds_read_b64_tr_b16 v[144:145], v42 offset:49152\n"
    "s_waitcnt lgkmcnt(7)\n"
    "v_mfma_f32_16x16x32_bf16 v[196:199], v[180:183], v[160:163], v[196:199]\n"
    "ds_read_b64_tr_b16 v[146:147], v42 offset:53248\n"
    "ds_read_b64_tr_b16 v[148:149], v43 offset:49152\n"
    "s_waitcnt lgkmcnt(7)\n"
    "v_mfma_f32_16x16x32_bf16 v[200:203], v[184:187], v[160:163], v[200:203]\n"
    "ds_read_b64_tr_b16 v[150:151], v43 offset:53248\n"
    "ds_read_b64_tr_b16 v[152:153], v44 offset:49152\n"
    "s_waitcnt lgkmcnt(7)\n"
    "v_mfma_f32_16x16x32_bf16 v[204:207], v[188:191], v[160:163], v[204:207]\n"
    "ds_read_b64_tr_b16 v[154:155], v44 offset:53248\n"
    "ds_read_b64_tr_b16 v[156:157], v45 offset:49152\n"
    "s_waitcnt lgkmcnt(8)\n"
    "v_mfma_f32_16x16x32_bf16 v[208:211], v[176:179], v[164:167], v[208:211]\n"
    "ds_read_b64_tr_b16 v[158:159], v45 offset:53248\n"
    "ds_read2st64_b64 v[132:135], v36 offset0:226 offset1:227\n"
    "v_mfma_f32_16x16x32_bf16 v[212:215], v[180:183], v[164:167], v[212:215]\n"
    "v_mfma_f32_16x16x32_bf16 v[216:219], v[184:187], v[164:167], v[216:219]\n"
    "v_mfma_f32_16x16x32_bf16 v[220:223], v[188:191], v[164:167], v[220:223]\n"
    "s_waitcnt lgkmcnt(7)\n"
    "v_mfma_f32_16x16x32_bf16 v[192:195], v[144:147], v[128:131], v[192:195]\n"
    "s_waitcnt lgkmcnt(5)\n"
    "v_mfma_f32_16x16x32_bf16 v[196:199], v[148:151], v[128:131], v[196:199]\n"
    "s_waitcnt lgkmcnt(3)\n"
    "v_mfma_f32_16x16x32_bf16 v[200:203], v[152:155], v[128:131], v[200:203]\n"
    "s_waitcnt lgkmcnt(1)\n"
    "v_mfma_f32_16x16x32_bf16 v[204:207], v[156:159], v[128:131], v[204:207]\n"
    "s_waitcnt lgkmcnt(0)\n"
    "v_mfma_f32_16x16x32_bf16 v[208:211], v[144:147], v[132:135], v[208:211]\n"
    "v_mfma_f32_16x16x32_bf16 v[212:215], v[148:151], v[132:135], v[212:215]\n"
    "v_mfma_f32_16x16x32_bf16 v[216:219], v[152:155], v[132:135], v[216:219]\n"
    "v_mfma_f32_16x16x32_bf16 v[220:223], v[156:159], v[132:135], v[220:223]\n"
    "s_waitcnt vmcnt(6)\n"
    "s_nop 7\n"
    :
      "=&{v[192:223]}"(keep), "=&{v[96:127]}"(ex), "+{s[40:41]}"(pA), "+{s[42:43]}"(pB)
    : "{v32}"(voffA), "{v33}"(tid * 128), "{v34}"(voffB + 1024), "{v35}"(voffB + 128), "{v36}"(dsA + rt0 * 1024), "{v37}"(dsA + (rt0 ^ 2) * 1024),
      "{v38}"(b0), "{v39}"(b1), "{v40}"(b2), "{v41}"(b3), "{v42}"(b0 + 65536), "{v43}"(b1 + 65536), "{v44}"(b2 + 65536), "{v45}"(b3 + 65536),
      "{v46}"(voffX), "{v47}"(voffX + 8192), "{v48}"((unsigned)(16 * rt0 + r) * 4), "{v49}"(w * 4096 + lane * 16),
      "{s[52:53]}"(ws + WS_XC + (layer * 256 + bx) * 16384), "{s44}"(lb + w * 2048), "{s47}"(lb + L_SCR), "{s[48:49]}"(pX), "{s[50:51]}"(pR)
    : "memory", "scc", "v31", "v128", "v129", "v130", "v131", "v132", "v133", "v134", "v135", "v136", "v137", "v138", "v139", "v140", "v141", "v142", "v143", "v144", "v145", "v146", "v147", "v148", "v149", "v150", "v151", "v152", "v153", "v154", "v155", "v156", "v157", "v158", "v159", "v160", "v161", "v162", "v163", "v164", "v165", "v166", "v167", "v168", "v169", "v170", "v171", "v172", "v173", "v174", "v175", "v176", "v177", "v178", "v179", "v180", "v181", "v182", "v183", "v184", "v185", "v186", "v187", "v188", "v189", "v190", "v191", "v224", "v225", "v226", "v227", "v228", "v229", "v230", "v231", "v232", "v233", "v234", "v235", "v236", "v237", "v238", "v239", "v240", "v241", "v242", "v243", "v244", "v245", "v246", "v247", "v248", "v249", "v250", "v251", "v252", "v253", "v254", "v255", "s46", "s70", "s71");
  {
  LANE_IDS
  const rsrc_t rw = make_rsrc(ws, WS_END);
  drain();
  u32x4 got[4];
  const unsigned xo = WS_XC + (layer * 256 + (bx ^ 1)) * 16384 + w * 4096;
  { u32x4 g1[1]; fetch(rw, g1, [&](int) { return xo + 3 * 1024 + (lane | 56) * 16; }); }
  fetch(rw, got, [&](int f) { return xo + f * 1024 + lane * 16; });
  wg_barrier();
  epi_res<2>([&](int m, int n, int j) { const unsigned x = got[m * 2 + (n >> 1)][(n & 1) * 2 + (j >> 1)]; return (keep[(m * 4 + n) * 4 + j] + ((j & 1) ? bf_hi(x) : bf_lo(x))) * ex[16 + m]; },
             [&](int kf, int m, int i) { return ex[(kf * 2 + m) * 4 + i]; }, ws, out, lds + w * 4608, 2, 8 * pm + rt0, 2 * pn + wn, lane);
  if (layer == 0) {
    PUBLISH(stage_qkv, NB_1, if (tid < 4) set_word(ws, flag(0, FL_X + pm * 4 + tid, pn * 2 + ks)))
  }
  }
}

}

extern "C" __global__ __launch_bounds__(256, 1) void lin_main(const u16* __restrict__ wo, const u16* __restrict__ wg, const u16* __restrict__ wu, const u16* __restrict__ wd,
                                                              char* __restrict__ ws, u16* __restrict__ out, int nphase) {
  extern __shared__ __attribute__((aligned(16))) char smem[];
  LAS char* lds = (LAS char*)smem;
  const Args A = {wo, wg, wu, wd, ws, out, (int)__builtin_amdgcn_readfirstlane(threadIdx.x >> 6)};
  stage_qkv(A, lds);
#pragma unroll 1
  for (int layer = 0; layer < 2; ++layer) {
    phase_qkv(A, lds, layer);
    phase_attn(A, lds, layer);
    phase_gu(A, lds, layer);
    phase_down(A, lds, layer);
  }
  drain();
}

extern "C" __global__ void lin_end() {}

namespace {
void run_all(void* const* d_in, void* d_out, void* d_ws, hipStream_t stream, int upto) {
  const u16* x = (const u16*)d_in[0]; const u16* wqkv = (const u16*)d_in[1]; const u16* wo = (const u16*)d_in[2];
  const u16* wg = (const u16*)d_in[4]; const u16* wu = (const u16*)d_in[5]; const u16* wd = (const u16*)d_in[6];
  char* ws = (char*)d_ws; u16* out = (u16*)d_out;
  (void)hipFuncSetAttribute((const void*)lin_main, hipFuncAttributeMaxDynamicSharedMemorySize, L_END);
  lin_prep<<<352, 256, 0, stream>>>(x, wqkv, wo, wg, wu, wd, ws, out);
  lin_main<<<256, 256, L_END, stream>>>(wo, wg, wu, wd, ws, out, upto / 2);
}
}

extern "C" void kernel_launch(void* const* d_in, const int* in_sizes, int n_in, void* d_out, int out_size, void* d_ws, size_t ws_size, hipStream_t stream) {
  run_all(d_in, d_out, d_ws, stream, 8);
}
```
